```python
import torch

def _softmax(x, axis=-1):
    return torch.softmax(x, dim=axis)
def _gelu(x):
    return torch.nn.functional.gelu(x, approximate="tanh")
def _silu(x):
    return torch.nn.functional.silu(x)
def _sigmoid(x):
    return torch.sigmoid(x)
def _layernorm(x, axis=-1, eps=1e-5):
    m = x.mean(dim=axis, keepdim=True)
    v = x.var(dim=axis, keepdim=True, unbiased=False)
    return (x - m) / torch.sqrt(v + eps)
def _scatter_add(tgt, idx, src, axis=0):
    return tgt.clone().index_add_(axis, idx, src)
def _segment_sum(src, seg, n):
    out = torch.zeros((n,) + src.shape[1:], dtype=src.dtype)
    return out.index_add_(0, seg, src)
def _pad(x, pw):
    flat = tuple(v for pair in reversed(pw) for v in pair)
    return torch.nn.functional.pad(x, flat)

kernel_name = 'parallel_sum__softmax_full_mha__mixer_s_mlp__prompt'

def setup_inputs():

    _w = (lambda a: (lambda t: t.to(torch.bfloat16) if t.is_floating_point() else t)(torch.as_tensor(a)))
    def _A_setup():
        B, S, D = 1, 2048, 512
        return {
            "x": _w((lambda *s: torch.randn(*s, dtype=torch.bfloat16) * 0.05)(B, S, D)),
            "wqkv": _w((lambda *s: torch.randn(*s, dtype=torch.bfloat16) * 0.1)(D, 3 * D)),
            "wo": _w((lambda *s: torch.randn(*s, dtype=torch.bfloat16) * 0.05)(D, D)),
        }
    _A_in = _A_setup()

    def _B_setup():
        B, S, D, F = 1, 2048, 512, 1376
        return {
            "x":  _w((lambda *s: torch.randn(*s, dtype=torch.bfloat16) * 0.05)(B, S, D)),
            "w1": _w((lambda *s: torch.randn(*s, dtype=torch.bfloat16) * 3.2)(D, D) * (D ** -0.5)),
            "w2": _w((lambda *s: torch.randn(*s, dtype=torch.bfloat16) * 3.2)(D, D) * (D ** -0.5)),
            "w3": _w((lambda *s: torch.randn(*s, dtype=torch.bfloat16) * 3.2)(D, F) * (D ** -0.5)),
            "w4": _w((lambda *s: torch.randn(*s, dtype=torch.bfloat16) * 3.2)(F, D) * (F ** -0.5)),
        }
    _B_in = _B_setup()

    return {
        "A_x": _A_in["x"],
        "A_wqkv": _A_in["wqkv"],
        "A_wo": _A_in["wo"],
        "B_x": _B_in["x"],
        "B_w1": _B_in["w1"],
        "B_w2": _B_in["w2"],
        "B_w3": _B_in["w3"],
        "B_w4": _B_in["w4"],
    }

def reference(A_x, A_wqkv, A_wo, B_x, B_w1, B_w2, B_w3, B_w4):
    def _A_ref(x, wqkv, wo):
        B, S, D = x.shape
        H = 32; Dh = D // H
        qkv = (lambda x, *d: x.permute(*d))((x @ wqkv).reshape(B, S, 3, H, Dh), 2, 0, 3, 1, 4)
        q, k, v = qkv[0], qkv[1], qkv[2]
        scale = Dh ** -0.5
        scores = torch.einsum("bhqd,bhkd->bhqk", q, k) * scale
        p = _softmax(scores, axis=-1)
        o = torch.einsum("bhqk,bhkd->bhqd", p, v)
        return (lambda x, *d: x.permute(*d))(o, 0, 2, 1, 3).reshape(B, S, D) @ wo

    def _B_ref(x, w1, w2, w3, w4):
        B, S, D = x.shape
        h = _gelu(_layernorm(x, axis=-1) @ w1)
        c = D // 4
        g0 = _pad(h[:, :, 0*c:1*c], ((0, 0), (1, 0), (0, 0)))[:, :S, :]
        g1 = _pad(h[:, :, 1*c:2*c], ((0, 0), (0, 1), (0, 0)))[:, 1:, :]
        g2 = _pad(h[:, :, 2*c:3*c], ((0, 0), (2, 0), (0, 0)))[:, :S, :]
        g3 = _pad(h[:, :, 3*c:   ], ((0, 0), (0, 2), (0, 0)))[:, 2:, :]
        hs = torch.concatenate([g0, g1, g2, g3], axis=-1) @ w2
        x1 = x + hs
        h2 = _gelu(_layernorm(x1, axis=-1) @ w3) @ w4
        return x1 + h2

    x = A_x
    xn = _layernorm(x, axis=-1)
    return x + _A_ref(xn, A_wqkv, A_wo) + _B_ref(xn, B_w1, B_w2, B_w3, B_w4)
```

```cpp
#include <hip/hip_runtime.h>
#include <hip/hip_bf16.h>
#include <stdint.h>

typedef __attribute__((ext_vector_type(8))) __bf16 bf16x8;
typedef __attribute__((ext_vector_type(4))) __bf16 bf16x4;
typedef __attribute__((ext_vector_type(4))) short s16x4;
typedef __attribute__((ext_vector_type(16))) float f32x16;
typedef __attribute__((ext_vector_type(4))) float f32x4;
typedef __attribute__((ext_vector_type(4))) unsigned u32x4;
typedef __attribute__((ext_vector_type(2))) unsigned u32x2;
typedef __attribute__((address_space(3))) void* ldsptr_t;
typedef unsigned short u16;

#define DEV __device__ __forceinline__

constexpr int S_ = 2048, D_ = 512, H_ = 32, F_ = 1376;
constexpr float LN_EPS = 1e-5f;
constexpr float QSCALE = 0.25f * 1.4426950408889634f;

DEV unsigned pk_bf16(float lo, float hi) {
  typedef __attribute__((ext_vector_type(2))) __bf16 bf16x2_t;
  typedef __attribute__((ext_vector_type(2))) float f32x2_t;
  f32x2_t v = {lo, hi};
  return __builtin_bit_cast(unsigned, __builtin_convertvector(v, bf16x2_t));
}
DEV float bf_lo(unsigned u) { return __uint_as_float(u << 16); }
DEV float bf_hi(unsigned u) { return __uint_as_float(u & 0xffff0000u); }
DEV float gelu_tanh(float x) {
  const float c1 = -2.0f * 0.7978845608028654f * 1.4426950408889634f;
  const float c2 = c1 * 0.044715f;
  float t = x * (c1 + c2 * x * x);
  float e = __builtin_amdgcn_exp2f(t);
  return x * __builtin_amdgcn_rcpf(1.0f + e);
}
#define WAIT_V(n) asm volatile("s_waitcnt vmcnt(%0)" ::"n"(n) : "memory")
#define WAIT_L(n) asm volatile("s_waitcnt lgkmcnt(%0)" ::"n"(n) : "memory")
#define RAW_BARRIER() do { WAIT_L(0); __builtin_amdgcn_s_barrier(); } while (0)

DEV __amdgpu_buffer_rsrc_t mk_rsrc(const void* p, unsigned bytes) {
  return __builtin_amdgcn_make_buffer_rsrc((void*)p, 0, (int)bytes, 0x00020000);
}

__global__ __launch_bounds__(256) void k_ln(const u16* __restrict__ x, u16* __restrict__ y) {
  int row = blockIdx.x * 4 + (threadIdx.x >> 6), lane = threadIdx.x & 63;
  u32x4 v = *(const u32x4*)(x + (size_t)row * D_ + lane * 8);
  float f[8];
#pragma unroll
  for (int i = 0; i < 4; ++i) { f[2 * i] = bf_lo(v[i]); f[2 * i + 1] = bf_hi(v[i]); }
  float s = 0;
#pragma unroll
  for (int i = 0; i < 8; ++i) s += f[i];
#pragma unroll
  for (int o = 32; o; o >>= 1) s += __shfl_xor(s, o);
  float m = s * (1.0f / D_);
  float q = 0;
#pragma unroll
  for (int i = 0; i < 8; ++i) { f[i] -= m; q += f[i] * f[i]; }
#pragma unroll
  for (int o = 32; o; o >>= 1) q += __shfl_xor(q, o);
  float r = 1.0f / sqrtf(q * (1.0f / D_) + LN_EPS);
  u32x4 o4;
#pragma unroll
  for (int i = 0; i < 4; ++i) o4[i] = pk_bf16(f[2 * i] * r, f[2 * i + 1] * r);
  *(u32x4*)(y + (size_t)row * D_ + lane * 8) = o4;
}

template <int RB> DEV unsigned w_swz(unsigned row) {
  if constexpr (RB % 256 == 0) return (row & 3) << 2;
  else if constexpr (RB % 256 == 128) return ((row >> 1) & 1) << 2;
  else return 0;
}
DEV bf16x8 tr_frag(const char* lds_tile_base_plus_lane0, const char* p1) {
  s16x4 a = __builtin_amdgcn_ds_read_tr16_b64_v4i16((__attribute__((address_space(3))) s16x4*)lds_tile_base_plus_lane0);
  s16x4 b = __builtin_amdgcn_ds_read_tr16_b64_v4i16((__attribute__((address_space(3))) s16x4*)p1);
  typedef __attribute__((ext_vector_type(8))) short s16x8;
  s16x8 r = __builtin_shufflevector(a, b, 0, 1, 2, 3, 4, 5, 6, 7);
  return __builtin_bit_cast(bf16x8, r);
}

template <int BM, int BN, int WM, int WN, int NST, int K16, bool SWAP>
struct Gemm {
  static constexpr int NW = WM * WN;
  static constexpr int TM = BM / WM / 32, TN = BN / WN / 32;
  static constexpr int XB = BM * 128, RB = BN * 2, WB = 64 * RB, STB = XB + WB;
  static constexpr int XL = XB / 1024 / NW, WL = WB / 1024 / NW, LPS = XL + WL;
  static constexpr int NT = (K16 + 3) / 4;
  static_assert(XB % (1024 * NW) == 0 && WB % (1024 * NW) == 0, "staging split");

  template <class XS>
  static DEV void run(char* lds, __amdgpu_buffer_rsrc_t xrs, unsigned xoff0, unsigned ldx,
                      __amdgpu_buffer_rsrc_t wrs, unsigned woff0, unsigned ldw, XS xshift, f32x16 (&acc)[TM][TN]) {
    const int tid = threadIdx.x, lane = tid & 63;
    const int wid = __builtin_amdgcn_readfirstlane(tid >> 6);
    const int wm = wid / WN, wn = wid % WN;
    unsigned xv[XL], wv[WL];
#pragma unroll
    for (int i = 0; i < XL; ++i) {
      unsigned b = (wid + i * NW) * 1024 + lane * 16;
      unsigned row = b >> 7, cp = (b >> 4) & 7, c = cp ^ ((row >> 1) & 7);
      xv[i] = xoff0 + row * ldx + c * 16;
    }
#pragma unroll
    for (int i = 0; i < WL; ++i) {
      unsigned b = (wid + i * NW) * 1024 + lane * 16;
      unsigned row = b / RB, cp = (b % RB) >> 4, c = cp ^ w_swz<RB>(row);
      wv[i] = woff0 + row * ldw + c * 16;
    }
    auto issue = [&](int t) {
      char* sb = lds + (t % NST) * STB;
      unsigned xs = t * 128 + xshift(t), ws = t * 64 * ldw;
#pragma unroll
      for (int i = 0; i < XL; ++i)
        __builtin_amdgcn_raw_ptr_buffer_load_lds(xrs, (ldsptr_t)(sb + (wid + i * NW) * 1024), 16, xv[i], xs, 0, 0);
#pragma unroll
      for (int i = 0; i < WL; ++i)
        __builtin_amdgcn_raw_ptr_buffer_load_lds(wrs, (ldsptr_t)(sb + XB + (wid + i * NW) * 1024), 16, wv[i], ws, 0, 0);
    };
    const unsigned l31 = lane & 31, h = lane >> 5;
    unsigned xr[TM];
#pragma unroll
    for (int i = 0; i < TM; ++i) xr[i] = (wm * TM * 32 + i * 32 + l31);
    const unsigned blk = (lane >> 4) & 1, q = (lane & 15) >> 2, p = lane & 3;
#pragma unroll
    for (int i = 0; i < TM; ++i)
#pragma unroll
      for (int j = 0; j < TN; ++j)
#pragma unroll
        for (int r = 0; r < 16; ++r) acc[i][j][r] = 0.f;

#pragma unroll
    for (int t = 0; t < NST - 1; ++t) if (t < NT) issue(t);
#pragma unroll
    for (int t = 0; t < NT; ++t) {
      constexpr int dummy = 0; (void)dummy;
      {
        int ahead = (t + NST - 2 < NT - 1 ? t + NST - 2 : NT - 1) - t;
        if (ahead >= 3) WAIT_V(3 * LPS); else if (ahead == 2) WAIT_V(2 * LPS); else if (ahead == 1) WAIT_V(LPS); else WAIT_V(0);
      }
      RAW_BARRIER();
      if (t + NST - 1 < NT) issue(t + NST - 1);
      const char* sx = lds + (t % NST) * STB;
      const char* sw = sx + XB;
#pragma unroll
      for (int s = 0; s < 4; ++s) {
        if (t * 4 + s < K16) {
          bf16x8 xf[TM], wf[TN];
#pragma unroll
          for (int i = 0; i < TM; ++i) {
            unsigned row = xr[i], c = 2 * s + h;
            xf[i] = *(const bf16x8*)(sx + row * 128 + ((c ^ ((row >> 1) & 7)) << 4));
          }
#pragma unroll
          for (int j = 0; j < TN; ++j) {
            unsigned cb = (wn * TN + j) * 4 + 2 * blk + (p >> 1);
            unsigned r0 = 16 * s + 8 * h + q, r1 = r0 + 4;
            wf[j] = tr_frag(sw + r0 * RB + ((cb ^ w_swz<RB>(r0)) << 4) + 8 * (p & 1),
                            sw + r1 * RB + ((cb ^ w_swz<RB>(r1)) << 4) + 8 * (p & 1));
          }
#pragma unroll
          for (int i = 0; i < TM; ++i)
#pragma unroll
            for (int j = 0; j < TN; ++j)
              acc[i][j] = SWAP ? __builtin_amdgcn_mfma_f32_32x32x16_bf16(wf[j], xf[i], acc[i][j], 0, 0, 0)
                               : __builtin_amdgcn_mfma_f32_32x32x16_bf16(xf[i], wf[j], acc[i][j], 0, 0, 0);
        }
      }
    }
    RAW_BARRIER();
  }
};

struct NoShift { DEV unsigned operator()(int) const { return 0; } };
struct MixShift {
  DEV unsigned operator()(int t) const {
    int g = t >> 1;
    int d = (g == 0) ? -1 : (g == 1) ? 1 : (g == 2) ? -2 : 2;
    return (unsigned)((d + 2) * (int)(D_ * 2));
  }
};

DEV u32x2 pk4(float a, float b, float c, float d) { u32x2 r; r[0] = pk_bf16(a, b); r[1] = pk_bf16(c, d); return r; }
DEV u32x4 widen(u32x2 X, u32x2 Y) {
  u32x4 o;
  auto a = __builtin_amdgcn_permlane32_swap(X[0], Y[0], false, false);
  auto b = __builtin_amdgcn_permlane32_swap(X[1], Y[1], false, false);
  o[0] = a[0]; o[1] = b[0]; o[2] = a[1]; o[3] = b[1];
  return o;
}

constexpr int VT_B = 1088;
constexpr int VH_B = 64 * VT_B;
__global__ __launch_bounds__(512, 2) void k_qkv(const u16* __restrict__ xn, const u16* __restrict__ wqkv,
                                                u16* __restrict__ Q, u16* __restrict__ Kf, u16* __restrict__ Vf) {
  extern __shared__ __attribute__((aligned(16))) char lds[];
  const int mt = blockIdx.x & 15, nt = blockIdx.x >> 4;
  const int m0 = mt * 128, n0 = nt * 128;
  const int lane = threadIdx.x & 63, wid = threadIdx.x >> 6, l31 = lane & 31, h = lane >> 5;
  auto xrs = mk_rsrc(xn, S_ * D_ * 2);
  auto wrs = mk_rsrc(wqkv, D_ * 3 * D_ * 2);
  if (nt < 8) {
    using G = Gemm<128, 128, 2, 4, 4, 32, true>;
    f32x16 acc[2][1];
    G::run(lds, xrs, m0 * D_ * 2, D_ * 2, wrs, n0 * 2, 3 * D_ * 2, NoShift(), acc);
    const int wm = wid / 4, wn = wid % 4;
    const float sc = nt < 4 ? QSCALE : 1.0f;
#pragma unroll
    for (int i = 0; i < 2; ++i) {
      int s = m0 + wm * 64 + i * 32 + l31;
#pragma unroll
      for (int hh = 0; hh < 2; ++hh) {
        const f32x16& a = acc[i][0];
        u32x2 X = pk4(a[8 * hh + 0] * sc, a[8 * hh + 1] * sc, a[8 * hh + 2] * sc, a[8 * hh + 3] * sc);
        u32x2 Y = pk4(a[8 * hh + 4] * sc, a[8 * hh + 5] * sc, a[8 * hh + 6] * sc, a[8 * hh + 7] * sc);
        u32x4 o = widen(X, Y);
        int n = n0 + wn * 32 + hh * 16;
        if (nt < 4) {
          int head = n >> 4;
          *(u32x4*)((char*)Q + (size_t)head * (S_ * 32) + s * 32 + h * 16) = o;
        } else {
          int head = (n - 512) >> 4;
          *(u32x4*)((char*)Kf + (size_t)head * (S_ * 32) + (s >> 5) * 1024 + h * 512 + (s & 31) * 16) = o;
        }
      }
    }
  } else {
    using G = Gemm<128, 128, 2, 4, 4, 32, false>;
    f32x16 acc[2][1];
    G::run(lds, xrs, m0 * D_ * 2, D_ * 2, wrs, n0 * 2, 3 * D_ * 2, NoShift(), acc);
    const int wm = wid / 4, wn = wid % 4;
#pragma unroll
    for (int i = 0; i < 2; ++i) {
      int tile = (m0 + wm * 64 + i * 32) >> 5;
      int n = n0 - 1024 + wn * 32 + l31;
      int head = n >> 4, d = n & 15;
      const f32x16& a = acc[i][0];
      char* base = (char*)Vf + (size_t)head * VH_B + tile * VT_B;
#pragma unroll
      for (int s = 0; s < 2; ++s) {
        u32x4 o;
#pragma unroll
        for (int j = 0; j < 4; ++j) o[j] = pk_bf16(a[8 * s + 2 * j], a[8 * s + 2 * j + 1]);
        *(u32x4*)(base + (s * 2 + h) * 272 + d * 16) = o;
        if (d == 0) {
          u32x4 ones = {0x3f803f80u, 0x3f803f80u, 0x3f803f80u, 0x3f803f80u};
          *(u32x4*)(base + (s * 2 + h) * 272 + 256) = ones;
        }
      }
    }
  }
}

enum { EPI_H = 0, EPI_X1 = 1, EPI_H2 = 2, EPI_B = 3, EPI_OUT = 4 };
template <int EPI>
__global__ __launch_bounds__(256, 2) void k_gemm64(const u16* __restrict__ A, const u16* __restrict__ W,
                                                   const u16* __restrict__ add1, const u16* __restrict__ add2,
                                                   u16* __restrict__ out) {
#if defined(__HIP_DEVICE_COMPILE__)
  extern __shared__ __attribute__((aligned(16))) char lds[];
  constexpr int N = (EPI == EPI_H2) ? F_ : D_;
  constexpr int K = (EPI == EPI_B) ? F_ : D_;
  const int mt = blockIdx.x & 31, nt = blockIdx.x >> 5;
  const int m0 = mt * 64, n0 = nt * 64;
  const int lane = threadIdx.x & 63, wid = threadIdx.x >> 6, l31 = lane & 31, h = lane >> 5;
  using G = Gemm<64, 64, 2, 2, 4, K / 16, true>;
  f32x16 acc[1][1];
  auto wrs = mk_rsrc(W, K * N * 2);
  if constexpr (EPI == EPI_X1) {
    auto xrs = mk_rsrc(A, (S_ + 4) * D_ * 2);
    G::run(lds, xrs, m0 * K * 2, K * 2, wrs, n0 * 2, N * 2, MixShift(), acc);
  } else {
    auto xrs = mk_rsrc(A, S_ * K * 2);
    G::run(lds, xrs, m0 * K * 2, K * 2, wrs, n0 * 2, N * 2, NoShift(), acc);
  }
  const int wm = wid / 2, wn = wid % 2;
  const int m = m0 + wm * 32 + l31;
  const f32x16& a = acc[0][0];
#pragma unroll
  for (int g = 0; g < 2; ++g) {
    int n = n0 + wn * 32 + g * 16 + h * 8;
    float v[8];
#pragma unroll
    for (int j = 0; j < 8; ++j) v[j] = a[8 * g + j];
    if constexpr (EPI == EPI_H || EPI == EPI_H2) {
#pragma unroll
      for (int j = 0; j < 8; ++j) v[j] = gelu_tanh(v[j]);
      u32x4 o = widen(pk4(v[0], v[1], v[2], v[3]), pk4(v[4], v[5], v[6], v[7]));
      if constexpr (EPI == EPI_H) {
        *(u32x4*)(out + (size_t)(m + 2) * N + n) = o;
      } else {
        if (n < N) *(u32x4*)(out + (size_t)m * N + n) = o;
      }
    } else {
      int nb = n0 + wn * 32 + g * 16 + 4 * h;
      u32x2 p0 = *(const u32x2*)(add1 + (size_t)m * N + nb), p1 = *(const u32x2*)(add1 + (size_t)m * N + nb + 8);
      v[0] += bf_lo(p0[0]); v[1] += bf_hi(p0[0]); v[2] += bf_lo(p0[1]); v[3] += bf_hi(p0[1]);
      v[4] += bf_lo(p1[0]); v[5] += bf_hi(p1[0]); v[6] += bf_lo(p1[1]); v[7] += bf_hi(p1[1]);
      if constexpr (EPI == EPI_OUT) {
        u32x2 q0 = *(const u32x2*)(add2 + (size_t)m * N + nb), q1 = *(const u32x2*)(add2 + (size_t)m * N + nb + 8);
        v[0] += bf_lo(q0[0]); v[1] += bf_hi(q0[0]); v[2] += bf_lo(q0[1]); v[3] += bf_hi(q0[1]);
        v[4] += bf_lo(q1[0]); v[5] += bf_hi(q1[0]); v[6] += bf_lo(q1[1]); v[7] += bf_hi(q1[1]);
      }
      u32x4 o = widen(pk4(v[0], v[1], v[2], v[3]), pk4(v[4], v[5], v[6], v[7]));
      *(u32x4*)(out + (size_t)m * N + n) = o;
    }
  }
  if constexpr (EPI == EPI_H) {
    if (mt == 0 && threadIdx.x < 16) {
      u32x4 z = {0, 0, 0, 0};
      int r = threadIdx.x >> 3, c = threadIdx.x & 7;
      *(u32x4*)(out + (size_t)r * N + n0 + c * 8) = z;
    }
    if (mt == 31 && threadIdx.x < 16) {
      u32x4 z = {0, 0, 0, 0};
      int r = threadIdx.x >> 3, c = threadIdx.x & 7;
      *(u32x4*)(out + (size_t)(S_ + 2 + r) * N + n0 + c * 8) = z;
    }
  }
#endif
}

constexpr int CH_K = 16 * 1024, CH_V = 16 * VT_B, CH_B = CH_K + CH_V;
__global__ __launch_bounds__(512, 2) void k_attn(const u16* __restrict__ Q, const u16* __restrict__ Kf,
                                                 const u16* __restrict__ Vf, u16* __restrict__ O) {
  extern __shared__ __attribute__((aligned(16))) char lds[];
  const int head = blockIdx.x >> 3, qb = blockIdx.x & 7;
  const int tid = threadIdx.x, lane = tid & 63, l31 = lane & 31, h = lane >> 5;
  const int wid = __builtin_amdgcn_readfirstlane(tid >> 6);
  auto krs = mk_rsrc((const char*)Kf + (size_t)head * (S_ * 32), S_ * 32);
  auto vrs = mk_rsrc((const char*)Vf + (size_t)head * VH_B, VH_B);
  const int q = qb * 256 + wid * 32 + l31;
  bf16x8 qf = *(const bf16x8*)((const char*)Q + (size_t)head * (S_ * 32) + q * 32 + h * 16);
  auto issue = [&](int c) {
    char* sb = lds + (c & 1) * CH_B;
#pragma unroll
    for (int i = 0; i < 2; ++i)
      __builtin_amdgcn_raw_ptr_buffer_load_lds(krs, (ldsptr_t)(sb + (wid + i * 8) * 1024), 16, lane * 16, c * CH_K + (wid + i * 8) * 1024, 0, 0);
#pragma unroll
    for (int i = 0; i < 2; ++i)
      __builtin_amdgcn_raw_ptr_buffer_load_lds(vrs, (ldsptr_t)(sb + CH_K + (wid + i * 8) * 1024), 16, lane * 16, c * CH_V + (wid + i * 8) * 1024, 0, 0);
    if (wid == 0)
      __builtin_amdgcn_raw_ptr_buffer_load_lds(vrs, (ldsptr_t)(sb + CH_K + 16 * 1024), 16, lane * 16, c * CH_V + 16 * 1024, 0, 0);
  };
  const unsigned vrow = l31 < 16 ? l31 : 16;
  const unsigned voff = h * 272 + vrow * 16;
  int* flag = (int*)(lds + 2 * CH_B);
  float mref = 0.f;
  f32x16 o;
  for (int attempt = 0; attempt < 2; ++attempt) {
    if (attempt == 1) {
      float mx = -__builtin_inff();
      issue(0);
      for (int c = 0; c < 4; ++c) {
        WAIT_V(0); RAW_BARRIER();
        if (c + 1 < 4) issue(c + 1);
        const char* sk = lds + (c & 1) * CH_B;
        for (int t = 0; t < 16; ++t) {
          bf16x8 kf = *(const bf16x8*)(sk + t * 1024 + lane * 16);
          f32x16 z = {};
          f32x16 s = __builtin_amdgcn_mfma_f32_32x32x16_bf16(kf, qf, z, 0, 0, 0);
#pragma unroll
          for (int r = 0; r < 16; ++r) mx = fmaxf(mx, s[r]);
        }
      }
      mx = fmaxf(mx, __shfl_xor(mx, 32));
      mref = mx;
      RAW_BARRIER();
    }
    issue(0);
#pragma unroll
    for (int r = 0; r < 16; ++r) o[r] = 0.f;
    f32x16 negm;
    for (int c = 0; c < 4; ++c) {
      WAIT_V(0); RAW_BARRIER();
      if (c + 1 < 4) issue(c + 1);
      const char* sk = lds + (c & 1) * CH_B;
      const char* sv = sk + CH_K;
      if (c == 0) {
        if (attempt == 0) {
          bf16x8 kf = *(const bf16x8*)(sk + lane * 16);
          f32x16 z = {};
          f32x16 s = __builtin_amdgcn_mfma_f32_32x32x16_bf16(kf, qf, z, 0, 0, 0);
          float mx = s[0];
#pragma unroll
          for (int r = 1; r < 16; ++r) mx = fmaxf(mx, s[r]);
          mx = fmaxf(mx, __shfl_xor(mx, 32));
          mref = mx;
        }
#pragma unroll
        for (int r = 0; r < 16; ++r) negm[r] = -mref;
      }
#pragma unroll 4
      for (int t = 0; t < 16; ++t) {
        bf16x8 kf = *(const bf16x8*)(sk + t * 1024 + lane * 16);
        bf16x8 v0 = *(const bf16x8*)(sv + t * VT_B + voff);
        bf16x8 v1 = *(const bf16x8*)(sv + t * VT_B + 544 + voff);
        f32x16 s = __builtin_amdgcn_mfma_f32_32x32x16_bf16(kf, qf, negm, 0, 0, 0);
        u32x4 p0, p1;
#pragma unroll
        for (int j = 0; j < 4; ++j) {
          p0[j] = pk_bf16(__builtin_amdgcn_exp2f(s[2 * j]), __builtin_amdgcn_exp2f(s[2 * j + 1]));
          p1[j] = pk_bf16(__builtin_amdgcn_exp2f(s[8 + 2 * j]), __builtin_amdgcn_exp2f(s[8 + 2 * j + 1]));
        }
        o = __builtin_amdgcn_mfma_f32_32x32x16_bf16(v0, __builtin_bit_cast(bf16x8, p0), o, 0, 0, 0);
        o = __builtin_amdgcn_mfma_f32_32x32x16_bf16(v1, __builtin_bit_cast(bf16x8, p1), o, 0, 0, 0);
      }
    }
    bool bad = !(o[8] < 1.0e30f);
#pragma unroll
    for (int r = 0; r < 8; ++r) bad |= !(fabsf(o[r]) < 3.0e38f);
    if (tid == 0) *flag = 0;
    RAW_BARRIER();
    if (attempt == 0 && __any(bad)) { if (lane == 0) *flag = 1; }
    RAW_BARRIER();
    int f = *flag;
    RAW_BARRIER();
    if (f == 0) break;
  }
  float inv = 1.0f / o[8];
  u32x4 ov = widen(pk4(o[0] * inv, o[1] * inv, o[2] * inv, o[3] * inv), pk4(o[4] * inv, o[5] * inv, o[6] * inv, o[7] * inv));
  *(u32x4*)(O + (size_t)q * D_ + head * 16 + h * 8) = ov;
}

extern "C" void kernel_launch(void* const* d_in, const int* in_sizes, int n_in, void* d_out, int out_size,
                              void* d_ws, size_t ws_size, hipStream_t stream) {
  const u16* x = (const u16*)d_in[0];
  const u16* wqkv = (const u16*)d_in[1];
  const u16* wo = (const u16*)d_in[2];
  const u16* w1 = (const u16*)d_in[4];
  const u16* w2 = (const u16*)d_in[5];
  const u16* w3 = (const u16*)d_in[6];
  const u16* w4 = (const u16*)d_in[7];
  u16* out = (u16*)d_out;
  char* ws = (char*)d_ws;
  const size_t MB = 1 << 20;
  u16* xn = (u16*)(ws + 0 * MB);
  u16* Q = (u16*)(ws + 2 * MB);
  u16* Kf = (u16*)(ws + 4 * MB);
  u16* Vf = (u16*)(ws + 6 * MB);
  u16* O = (u16*)(ws + 10 * MB);
  u16* hbuf = (u16*)(ws + 12 * MB);
  u16* x1 = (u16*)(ws + 16 * MB);
  u16* x1n = (u16*)(ws + 18 * MB);
  u16* h2 = (u16*)(ws + 20 * MB);
  u16* bout = (u16*)(ws + 28 * MB);

  k_ln<<<512, 256, 0, stream>>>(x, xn);
  {
    size_t sh = 4 * (128 * 128 + 64 * 256);
    (void)hipFuncSetAttribute((const void*)k_qkv, hipFuncAttributeMaxDynamicSharedMemorySize, (int)sh);
    k_qkv<<<16 * 12, 512, sh, stream>>>(xn, wqkv, Q, Kf, Vf);
  }
  size_t sh64 = 4 * (64 * 128 + 64 * 128);
  (void)hipFuncSetAttribute((const void*)k_gemm64<EPI_H>, hipFuncAttributeMaxDynamicSharedMemorySize, (int)sh64);
  (void)hipFuncSetAttribute((const void*)k_gemm64<EPI_X1>, hipFuncAttributeMaxDynamicSharedMemorySize, (int)sh64);
  (void)hipFuncSetAttribute((const void*)k_gemm64<EPI_H2>, hipFuncAttributeMaxDynamicSharedMemorySize, (int)sh64);
  (void)hipFuncSetAttribute((const void*)k_gemm64<EPI_B>, hipFuncAttributeMaxDynamicSharedMemorySize, (int)sh64);
  (void)hipFuncSetAttribute((const void*)k_gemm64<EPI_OUT>, hipFuncAttributeMaxDynamicSharedMemorySize, (int)sh64);
  k_gemm64<EPI_H><<<32 * 8, 256, sh64, stream>>>(xn, w1, nullptr, nullptr, hbuf);
  {
    size_t sh = 2 * CH_B + 16;
    (void)hipFuncSetAttribute((const void*)k_attn, hipFuncAttributeMaxDynamicSharedMemorySize, (int)sh);
    k_attn<<<256, 512, sh, stream>>>(Q, Kf, Vf, O);
  }
  k_gemm64<EPI_X1><<<32 * 8, 256, sh64, stream>>>(hbuf, w2, xn, nullptr, x1);
  k_ln<<<512, 256, 0, stream>>>(x1, x1n);
  k_gemm64<EPI_H2><<<32 * 22, 256, sh64, stream>>>(x1n, w3, nullptr, nullptr, h2);
  k_gemm64<EPI_B><<<32 * 8, 256, sh64, stream>>>(h2, w4, x1, nullptr, bout);
  k_gemm64<EPI_OUT><<<32 * 8, 256, sh64, stream>>>(O, wo, x, bout, out);
}
```

```cpp
#include <hip/hip_runtime.h>
#include <hip/hip_bf16.h>
#include <stdint.h>

typedef __attribute__((ext_vector_type(8))) __bf16 bf16x8;
typedef __attribute__((ext_vector_type(4))) __bf16 bf16x4;
typedef __attribute__((ext_vector_type(4))) short s16x4;
typedef __attribute__((ext_vector_type(16))) float f32x16;
typedef __attribute__((ext_vector_type(4))) float f32x4;
typedef __attribute__((ext_vector_type(4))) unsigned u32x4;
typedef __attribute__((ext_vector_type(2))) unsigned u32x2;
typedef __attribute__((address_space(3))) void* ldsptr_t;
typedef unsigned short u16;

#define DEV __device__ __forceinline__

constexpr int S_ = 2048, D_ = 512, H_ = 32, F_ = 1376;
constexpr float LN_EPS = 1e-5f;
constexpr float QSCALE = 0.25f * 1.4426950408889634f;

DEV unsigned pk_bf16(float lo, float hi) {
  typedef __attribute__((ext_vector_type(2))) __bf16 bf16x2_t;
  typedef __attribute__((ext_vector_type(2))) float f32x2_t;
  f32x2_t v = {lo, hi};
  return __builtin_bit_cast(unsigned, __builtin_convertvector(v, bf16x2_t));
}
DEV float bf_lo(unsigned u) { return __uint_as_float(u << 16); }
DEV float bf_hi(unsigned u) { return __uint_as_float(u & 0xffff0000u); }
DEV float gelu_tanh(float x) {
  const float c1 = -2.0f * 0.7978845608028654f * 1.4426950408889634f;
  const float c2 = c1 * 0.044715f;
  float t = x * (c1 + c2 * x * x);
  float e = __builtin_amdgcn_exp2f(t);
  return x * __builtin_amdgcn_rcpf(1.0f + e);
}
#define STAMP(i)
#define RSTAMP(i)
#define STAMP_DECL
#define STAMP_ARG
#define STAMP_PASS
#define STAMP_FLUSH
#define WAIT_V(n) asm volatile("s_waitcnt vmcnt(%0)" ::"n"(n) : "memory")
#define WAIT_L(n) asm volatile("s_waitcnt lgkmcnt(%0)" ::"n"(n) : "memory")
#define RAW_BARRIER() do { WAIT_L(0); __builtin_amdgcn_s_barrier(); } while (0)

DEV __amdgpu_buffer_rsrc_t mk_rsrc(const void* p, unsigned bytes) {
  return __builtin_amdgcn_make_buffer_rsrc((void*)p, 0, (int)bytes, 0x00020000);
}

__global__ __launch_bounds__(256) void k_ln(const u16* __restrict__ x, u16* __restrict__ y) {
  int row = blockIdx.x * 4 + (threadIdx.x >> 6), lane = threadIdx.x & 63;
  u32x4 v = *(const u32x4*)(x + (size_t)row * D_ + lane * 8);
  float f[8];
#pragma unroll
  for (int i = 0; i < 4; ++i) { f[2 * i] = bf_lo(v[i]); f[2 * i + 1] = bf_hi(v[i]); }
  float s = 0;
#pragma unroll
  for (int i = 0; i < 8; ++i) s += f[i];
#pragma unroll
  for (int o = 32; o; o >>= 1) s += __shfl_xor(s, o);
  float m = s * (1.0f / D_);
  float q = 0;
#pragma unroll
  for (int i = 0; i < 8; ++i) { f[i] -= m; q += f[i] * f[i]; }
#pragma unroll
  for (int o = 32; o; o >>= 1) q += __shfl_xor(q, o);
  float r = 1.0f / sqrtf(q * (1.0f / D_) + LN_EPS);
  u32x4 o4;
#pragma unroll
  for (int i = 0; i < 4; ++i) o4[i] = pk_bf16(f[2 * i] * r, f[2 * i + 1] * r);
  *(u32x4*)(y + (size_t)row * D_ + lane * 8) = o4;
}

template <int RB> DEV unsigned w_swz(unsigned row) {
  if constexpr (RB % 256 == 0) return (row & 3) << 2;
  else if constexpr (RB % 256 == 128) return ((row >> 1) & 1) << 2;
  else return 0;
}
DEV bf16x8 tr_frag(const char* lds_tile_base_plus_lane0, const char* p1) {
  s16x4 a = __builtin_amdgcn_ds_read_tr16_b64_v4i16((__attribute__((address_space(3))) s16x4*)lds_tile_base_plus_lane0);
  s16x4 b = __builtin_amdgcn_ds_read_tr16_b64_v4i16((__attribute__((address_space(3))) s16x4*)p1);
  typedef __attribute__((ext_vector_type(8))) short s16x8;
  s16x8 r = __builtin_shufflevector(a, b, 0, 1, 2, 3, 4, 5, 6, 7);
  return __builtin_bit_cast(bf16x8, r);
}

template <int BM, int BN, int WM, int WN, int NST, int K16, bool SWAP>
struct Gemm {
  static constexpr int NW = WM * WN;
  static constexpr int TM = BM / WM / 32, TN = BN / WN / 32;
  static constexpr int XB = BM * 128, RB = BN * 2, WB = 64 * RB, STB = XB + WB;
  static constexpr int XL = XB / 1024 / NW, WL = WB / 1024 / NW, LPS = XL + WL;
  static constexpr int NT = (K16 + 3) / 4;
  static_assert(XB % (1024 * NW) == 0 && WB % (1024 * NW) == 0, "staging split");

  template <class XS>
  static DEV void run(char* lds, __amdgpu_buffer_rsrc_t xrs, unsigned xoff0, unsigned ldx,
                      __amdgpu_buffer_rsrc_t wrs, unsigned woff0, unsigned ldw, XS xshift, f32x16 (&acc)[TM][TN] STAMP_ARG) {
    const int tid = threadIdx.x, lane = tid & 63;
    const int wid = __builtin_amdgcn_readfirstlane(tid >> 6);
    const int wm = wid / WN, wn = wid % WN;
    unsigned xv[XL], wv[WL];
#pragma unroll
    for (int i = 0; i < XL; ++i) {
      unsigned b = (wid + i * NW) * 1024 + lane * 16;
      unsigned row = b >> 7, cp = (b >> 4) & 7, c = cp ^ ((row >> 1) & 7);
      xv[i] = xoff0 + row * ldx + c * 16;
    }
#pragma unroll
    for (int i = 0; i < WL; ++i) {
      unsigned b = (wid + i * NW) * 1024 + lane * 16;
      unsigned row = b / RB, cp = (b % RB) >> 4, c = cp ^ w_swz<RB>(row);
      wv[i] = woff0 + row * ldw + c * 16;
    }
    auto issue = [&](int t) {
      char* sb = lds + (t % NST) * STB;
      unsigned xs = t * 128 + xshift(t), ws = t * 64 * ldw;
#pragma unroll
      for (int i = 0; i < XL; ++i)
        __builtin_amdgcn_raw_ptr_buffer_load_lds(xrs, (ldsptr_t)(sb + (wid + i * NW) * 1024), 16, xv[i], xs, 0, 0);
#pragma unroll
      for (int i = 0; i < WL; ++i)
        __builtin_amdgcn_raw_ptr_buffer_load_lds(wrs, (ldsptr_t)(sb + XB + (wid + i * NW) * 1024), 16, wv[i], ws, 0, 0);
    };
    const unsigned l31 = lane & 31, h = lane >> 5;
    unsigned xr[TM];
#pragma unroll
    for (int i = 0; i < TM; ++i) xr[i] = (wm * TM * 32 + i * 32 + l31);
    const unsigned blk = (lane >> 4) & 1, q = (lane & 15) >> 2, p = lane & 3;
#pragma unroll
    for (int i = 0; i < TM; ++i)
#pragma unroll
      for (int j = 0; j < TN; ++j)
#pragma unroll
        for (int r = 0; r < 16; ++r) acc[i][j][r] = 0.f;

    STAMP(2);
#pragma unroll
    for (int t = 0; t < NST - 1; ++t) if (t < NT) issue(t);
    STAMP(3);
#pragma unroll
    for (int t = 0; t < NT; ++t) {
      constexpr int dummy = 0; (void)dummy;
      {
        int ahead = (t + NST - 2 < NT - 1 ? t + NST - 2 : NT - 1) - t;
        if (ahead >= 3) WAIT_V(3 * LPS); else if (ahead == 2) WAIT_V(2 * LPS); else if (ahead == 1) WAIT_V(LPS); else WAIT_V(0);
      }
      RAW_BARRIER();
      STAMP(4 + t);
      if (t + NST - 1 < NT) issue(t + NST - 1);
      const char* sx = lds + (t % NST) * STB;
      const char* sw = sx + XB;
#pragma unroll
      for (int s = 0; s < 4; ++s) {
        if (t * 4 + s < K16) {
          bf16x8 xf[TM], wf[TN];
#pragma unroll
          for (int i = 0; i < TM; ++i) {
            unsigned row = xr[i], c = 2 * s + h;
            xf[i] = *(const bf16x8*)(sx + row * 128 + ((c ^ ((row >> 1) & 7)) << 4));
          }
#pragma unroll
          for (int j = 0; j < TN; ++j) {
            unsigned cb = (wn * TN + j) * 4 + 2 * blk + (p >> 1);
            unsigned r0 = 16 * s + 8 * h + q, r1 = r0 + 4;
            wf[j] = tr_frag(sw + r0 * RB + ((cb ^ w_swz<RB>(r0)) << 4) + 8 * (p & 1),
                            sw + r1 * RB + ((cb ^ w_swz<RB>(r1)) << 4) + 8 * (p & 1));
          }
#pragma unroll
          for (int i = 0; i < TM; ++i)
#pragma unroll
            for (int j = 0; j < TN; ++j)
              acc[i][j] = SWAP ? __builtin_amdgcn_mfma_f32_32x32x16_bf16(wf[j], xf[i], acc[i][j], 0, 0, 0)
                               : __builtin_amdgcn_mfma_f32_32x32x16_bf16(xf[i], wf[j], acc[i][j], 0, 0, 0);
        }
      }
    }
    RAW_BARRIER();
    STAMP(40);
  }
};

struct NoShift { DEV unsigned operator()(int) const { return 0; } };
struct MixShift {
  DEV unsigned operator()(int t) const {
    int g = t >> 1;
    int d = (g == 0) ? -1 : (g == 1) ? 1 : (g == 2) ? -2 : 2;
    return (unsigned)((d + 2) * (int)(D_ * 2));
  }
};

DEV u32x2 pk4(float a, float b, float c, float d) { u32x2 r; r[0] = pk_bf16(a, b); r[1] = pk_bf16(c, d); return r; }
DEV u32x4 widen(u32x2 X, u32x2 Y) {
  u32x4 o;
  auto a = __builtin_amdgcn_permlane32_swap(X[0], Y[0], false, false);
  auto b = __builtin_amdgcn_permlane32_swap(X[1], Y[1], false, false);
  o[0] = a[0]; o[1] = b[0]; o[2] = a[1]; o[3] = b[1];
  return o;
}

constexpr int VT_B = 1024;
constexpr int VH_B = 64 * VT_B;
__global__ __launch_bounds__(512, 2) void k_qkv(const u16* __restrict__ xn, const u16* __restrict__ wqkv,
                                                u16* __restrict__ Q, u16* __restrict__ Kf, u16* __restrict__ Vf) {
  extern __shared__ __attribute__((aligned(16))) char lds[];
  STAMP_DECL
  RSTAMP(0); STAMP(1);
  const int mt = blockIdx.x & 15, nt = blockIdx.x >> 4;
  const int m0 = mt * 128, n0 = nt * 128;
  const int lane = threadIdx.x & 63, wid = threadIdx.x >> 6, l31 = lane & 31, h = lane >> 5;
  auto xrs = mk_rsrc(xn, S_ * D_ * 2);
  auto wrs = mk_rsrc(wqkv, D_ * 3 * D_ * 2);
  if (nt < 8) {
    using G = Gemm<128, 128, 2, 4, 4, 32, true>;
    f32x16 acc[2][1];
    G::run(lds, xrs, m0 * D_ * 2, D_ * 2, wrs, n0 * 2, 3 * D_ * 2, NoShift(), acc STAMP_PASS);
    const int wm = wid / 4, wn = wid % 4;
    const float sc = nt < 4 ? QSCALE : 1.0f;
#pragma unroll
    for (int i = 0; i < 2; ++i) {
      int s = m0 + wm * 64 + i * 32 + l31;
#pragma unroll
      for (int hh = 0; hh < 2; ++hh) {
        const f32x16& a = acc[i][0];
        u32x2 X = pk4(a[8 * hh + 0] * sc, a[8 * hh + 1] * sc, a[8 * hh + 2] * sc, a[8 * hh + 3] * sc);
        u32x2 Y = pk4(a[8 * hh + 4] * sc, a[8 * hh + 5] * sc, a[8 * hh + 6] * sc, a[8 * hh + 7] * sc);
        u32x4 o = widen(X, Y);
        int n = n0 + wn * 32 + hh * 16;
        if (nt < 4) {
          int head = n >> 4;
          *(u32x4*)((char*)Q + (size_t)head * (S_ * 32) + s * 32 + h * 16) = o;
        } else {
          int head = (n - 512) >> 4;
          *(u32x4*)((char*)Kf + (size_t)head * (S_ * 32) + (s >> 5) * 1024 + h * 512 + (s & 31) * 16) = o;
        }
      }
    }
  } else {
    using G = Gemm<128, 128, 2, 4, 4, 32, false>;
    f32x16 acc[2][1];
    G::run(lds, xrs, m0 * D_ * 2, D_ * 2, wrs, n0 * 2, 3 * D_ * 2, NoShift(), acc STAMP_PASS);
    const int wm = wid / 4, wn = wid % 4;
#pragma unroll
    for (int i = 0; i < 2; ++i) {
      int tile = (m0 + wm * 64 + i * 32) >> 5;
      int n = n0 - 1024 + wn * 32 + l31;
      int head = n >> 4, d = n & 15;
      const f32x16& a = acc[i][0];
      char* base = (char*)Vf + (size_t)head * VH_B + tile * VT_B;
#pragma unroll
      for (int s = 0; s < 2; ++s) {
        u32x4 o;
#pragma unroll
        for (int j = 0; j < 4; ++j) o[j] = pk_bf16(a[8 * s + 2 * j], a[8 * s + 2 * j + 1]);
        *(u32x4*)(base + (s * 2 + h) * 256 + d * 16) = o;
      }
    }
  }
  STAMP(41); RSTAMP(42); STAMP_FLUSH;
}

enum { EPI_H = 0, EPI_X1 = 1, EPI_H2 = 2, EPI_B = 3, EPI_OUT = 4 };
template <int EPI>
__global__ __launch_bounds__(256, 2) void k_gemm64(const u16* __restrict__ A, const u16* __restrict__ W,
                                                   const u16* __restrict__ add1, const u16* __restrict__ add2,
                                                   u16* __restrict__ out) {
#if defined(__HIP_DEVICE_COMPILE__)
  extern __shared__ __attribute__((aligned(16))) char lds[];
  constexpr int N = (EPI == EPI_H2) ? F_ : D_;
  constexpr int K = (EPI == EPI_B) ? F_ : D_;
  STAMP_DECL
  RSTAMP(0); STAMP(1);
  const int mt = blockIdx.x & 31, nt = blockIdx.x >> 5;
  const int m0 = mt * 64, n0 = nt * 64;
  const int lane = threadIdx.x & 63, wid = threadIdx.x >> 6, l31 = lane & 31, h = lane >> 5;
  using G = Gemm<64, 64, 2, 2, 4, K / 16, true>;
  f32x16 acc[1][1];
  auto wrs = mk_rsrc(W, K * N * 2);
  if constexpr (EPI == EPI_X1) {
    auto xrs = mk_rsrc(A, (S_ + 4) * D_ * 2);
    G::run(lds, xrs, m0 * K * 2, K * 2, wrs, n0 * 2, N * 2, MixShift(), acc STAMP_PASS);
  } else {
    auto xrs = mk_rsrc(A, S_ * K * 2);
    G::run(lds, xrs, m0 * K * 2, K * 2, wrs, n0 * 2, N * 2, NoShift(), acc STAMP_PASS);
  }
  const int wm = wid / 2, wn = wid % 2;
  const int m = m0 + wm * 32 + l31;
  const f32x16& a = acc[0][0];
#pragma unroll
  for (int g = 0; g < 2; ++g) {
    int n = n0 + wn * 32 + g * 16 + h * 8;
    float v[8];
#pragma unroll
    for (int j = 0; j < 8; ++j) v[j] = a[8 * g + j];
    if constexpr (EPI == EPI_H || EPI == EPI_H2) {
#pragma unroll
      for (int j = 0; j < 8; ++j) v[j] = gelu_tanh(v[j]);
      u32x4 o = widen(pk4(v[0], v[1], v[2], v[3]), pk4(v[4], v[5], v[6], v[7]));
      if constexpr (EPI == EPI_H) {
        *(u32x4*)(out + (size_t)(m + 2) * N + n) = o;
      } else {
        if (n < N) *(u32x4*)(out + (size_t)m * N + n) = o;
      }
    } else {
      int nb = n0 + wn * 32 + g * 16 + 4 * h;
      u32x2 p0 = *(const u32x2*)(add1 + (size_t)m * N + nb), p1 = *(const u32x2*)(add1 + (size_t)m * N + nb + 8);
      v[0] += bf_lo(p0[0]); v[1] += bf_hi(p0[0]); v[2] += bf_lo(p0[1]); v[3] += bf_hi(p0[1]);
      v[4] += bf_lo(p1[0]); v[5] += bf_hi(p1[0]); v[6] += bf_lo(p1[1]); v[7] += bf_hi(p1[1]);
      if constexpr (EPI == EPI_OUT) {
        u32x2 q0 = *(const u32x2*)(add2 + (size_t)m * N + nb), q1 = *(const u32x2*)(add2 + (size_t)m * N + nb + 8);
        v[0] += bf_lo(q0[0]); v[1] += bf_hi(q0[0]); v[2] += bf_lo(q0[1]); v[3] += bf_hi(q0[1]);
        v[4] += bf_lo(q1[0]); v[5] += bf_hi(q1[0]); v[6] += bf_lo(q1[1]); v[7] += bf_hi(q1[1]);
      }
      u32x4 o = widen(pk4(v[0], v[1], v[2], v[3]), pk4(v[4], v[5], v[6], v[7]));
      *(u32x4*)(out + (size_t)m * N + n) = o;
    }
  }
  if constexpr (EPI == EPI_H) {
    if (mt == 0 && threadIdx.x < 16) {
      u32x4 z = {0, 0, 0, 0};
      int r = threadIdx.x >> 3, c = threadIdx.x & 7;
      *(u32x4*)(out + (size_t)r * N + n0 + c * 8) = z;
    }
    if (mt == 31 && threadIdx.x < 16) {
      u32x4 z = {0, 0, 0, 0};
      int r = threadIdx.x >> 3, c = threadIdx.x & 7;
      *(u32x4*)(out + (size_t)(S_ + 2 + r) * N + n0 + c * 8) = z;
    }
  }
  STAMP(41); RSTAMP(42); STAMP_FLUSH;
#endif
}

#define ATTN_ASM \
  "v_mov_b32 v36, %[negm]\n\t" \
  "v_mov_b32 v37, %[negm]\n\t" \
  "v_mov_b32 v38, %[negm]\n\t" \
  "v_mov_b32 v39, %[negm]\n\t" \
  "v_mov_b32 v40, %[negm]\n\t" \
  "v_mov_b32 v41, %[negm]\n\t" \
  "v_mov_b32 v42, %[negm]\n\t" \
  "v_mov_b32 v43, %[negm]\n\t" \
  "v_mov_b32 v44, %[negm]\n\t" \
  "v_mov_b32 v45, %[negm]\n\t" \
  "v_mov_b32 v46, %[negm]\n\t" \
  "v_mov_b32 v47, %[negm]\n\t" \
  "v_mov_b32 v48, %[negm]\n\t" \
  "v_mov_b32 v49, %[negm]\n\t" \
  "v_mov_b32 v50, %[negm]\n\t" \
  "v_mov_b32 v51, %[negm]\n\t" \
  "v_mov_b32 v52, 0\n\t" \
  "v_mov_b32 v53, 0\n\t" \
  "v_mov_b32 v54, 0\n\t" \
  "v_mov_b32 v55, 0\n\t" \
  "v_mov_b32 v56, 0\n\t" \
  "v_mov_b32 v57, 0\n\t" \
  "v_mov_b32 v58, 0\n\t" \
  "v_mov_b32 v59, 0\n\t" \
  "v_mov_b32 v60, 0\n\t" \
  "v_mov_b32 v61, 0\n\t" \
  "v_mov_b32 v62, 0\n\t" \
  "v_mov_b32 v63, 0\n\t" \
  "v_mov_b32 v64, 0\n\t" \
  "v_mov_b32 v65, 0\n\t" \
  "v_mov_b32 v66, 0\n\t" \
  "v_mov_b32 v67, 0\n\t" \
  "v_mov_b32 v104, 0x3f803f80\n\t" \
  "v_mov_b32 v108, 0x3f803f80\n\t" \
  "v_mov_b32 v116, 0\n\t" \
  "v_mov_b32 v105, 0x3f803f80\n\t" \
  "v_mov_b32 v109, 0x3f803f80\n\t" \
  "v_mov_b32 v117, 0\n\t" \
  "v_mov_b32 v106, 0x3f803f80\n\t" \
  "v_mov_b32 v110, 0x3f803f80\n\t" \
  "v_mov_b32 v118, 0\n\t" \
  "v_mov_b32 v107, 0x3f803f80\n\t" \
  "v_mov_b32 v111, 0x3f803f80\n\t" \
  "v_mov_b32 v119, 0\n\t" \
  "s_mov_b32 %[scur], 0\n\t" \
  "s_mov_b32 %[snext], 0x4000\n\t" \
  "s_mov_b32 %[sfree], 0x8000\n\t" \
  "s_mov_b32 %[cnt], 8\n\t" \
  "ds_read_b128 v[100:103], %[lk]\n\t" \
  "s_waitcnt lgkmcnt(0)\n\t" \
  "v_mfma_f32_32x32x16_bf16 v[68:83], v[100:103], v[32:35], v[36:51]\n\t" \
  "ds_read_b128 v[100:103], %[lk] offset:1024\n\t" \
  "s_mov_b64 exec, %[vmask]\n\t" \
  "ds_read_b128 v[104:107], %[lv]\n\t" \
  "s_mov_b64 exec, -1\n\t" \
  "s_nop 7\n\t" \
  "1:\n\t" \
  "v_add_u32 v120, %[scur], %[lk]\n\t" \
  "v_add_u32 v121, %[snext], %[lk]\n\t" \
  "v_add_u32 v122, %[scur], %[lv]\n\t" \
  "v_add_u32 v123, %[snext], %[lv]\n\t" \
  "s_waitcnt lgkmcnt(1)\n\t" \
  "v_mfma_f32_32x32x16_bf16 v[84:99], v[100:103], v[32:35], v[36:51]\n\t" \
  "ds_read_b128 v[100:103], v120 offset:2048\n\t" \
  "v_mfma_f32_32x32x16_bf16 v[52:67], v[108:111], v[116:119], v[52:67]\n\t" \
  "s_mov_b64 exec, %[vmask]\n\t" \
  "ds_read_b128 v[108:111], v122 offset:512\n\t" \
  "s_mov_b64 exec, -1\n\t" \
  "v_exp_f32 v112, v69\n\t" \
  "v_exp_f32 v68, v68\n\t" \
  "v_exp_f32 v113, v71\n\t" \
  "v_mov_b32_sdwa v112, v68 dst_sel:WORD_0 dst_unused:UNUSED_PRESERVE src0_sel:WORD_1\n\t" \
  "v_exp_f32 v70, v70\n\t" \
  "v_exp_f32 v114, v73\n\t" \
  "v_mov_b32_sdwa v113, v70 dst_sel:WORD_0 dst_unused:UNUSED_PRESERVE src0_sel:WORD_1\n\t" \
  "v_exp_f32 v72, v72\n\t" \
  "v_exp_f32 v115, v75\n\t" \
  "v_mov_b32_sdwa v114, v72 dst_sel:WORD_0 dst_unused:UNUSED_PRESERVE src0_sel:WORD_1\n\t" \
  "v_exp_f32 v74, v74\n\t" \
  "v_exp_f32 v116, v77\n\t" \
  "v_mov_b32_sdwa v115, v74 dst_sel:WORD_0 dst_unused:UNUSED_PRESERVE src0_sel:WORD_1\n\t" \
  "v_exp_f32 v76, v76\n\t" \
  "v_exp_f32 v117, v79\n\t" \
  "s_waitcnt lgkmcnt(2)\n\t" \
  "v_mfma_f32_32x32x16_bf16 v[52:67], v[104:107], v[112:115], v[52:67]\n\t" \
  "s_mov_b64 exec, %[vmask]\n\t" \
  "ds_read_b128 v[104:107], v122 offset:1024\n\t" \
  "s_mov_b64 exec, -1\n\t" \
  "v_mov_b32_sdwa v116, v76 dst_sel:WORD_0 dst_unused:UNUSED_PRESERVE src0_sel:WORD_1\n\t" \
  "v_exp_f32 v78, v78\n\t" \
  "v_exp_f32 v118, v81\n\t" \
  "v_mov_b32_sdwa v117, v78 dst_sel:WORD_0 dst_unused:UNUSED_PRESERVE src0_sel:WORD_1\n\t" \
  "v_exp_f32 v80, v80\n\t" \
  "v_exp_f32 v119, v83\n\t" \
  "v_mov_b32_sdwa v118, v80 dst_sel:WORD_0 dst_unused:UNUSED_PRESERVE src0_sel:WORD_1\n\t" \
  "v_exp_f32 v82, v82\n\t" \
  "s_nop 0\n\t" \
  "v_mov_b32_sdwa v119, v82 dst_sel:WORD_0 dst_unused:UNUSED_PRESERVE src0_sel:WORD_1\n\t" \
  "s_waitcnt lgkmcnt(1)\n\t" \
  "v_mfma_f32_32x32x16_bf16 v[68:83], v[100:103], v[32:35], v[36:51]\n\t" \
  "ds_read_b128 v[100:103], v120 offset:3072\n\t" \
  "v_mfma_f32_32x32x16_bf16 v[52:67], v[108:111], v[116:119], v[52:67]\n\t" \
  "s_mov_b64 exec, %[vmask]\n\t" \
  "ds_read_b128 v[108:111], v122 offset:1536\n\t" \
  "s_mov_b64 exec, -1\n\t" \
  "v_exp_f32 v112, v85\n\t" \
  "v_exp_f32 v84, v84\n\t" \
  "v_exp_f32 v113, v87\n\t" \
  "v_mov_b32_sdwa v112, v84 dst_sel:WORD_0 dst_unused:UNUSED_PRESERVE src0_sel:WORD_1\n\t" \
  "v_exp_f32 v86, v86\n\t" \
  "v_exp_f32 v114, v89\n\t" \
  "v_mov_b32_sdwa v113, v86 dst_sel:WORD_0 dst_unused:UNUSED_PRESERVE src0_sel:WORD_1\n\t" \
  "v_exp_f32 v88, v88\n\t" \
  "v_exp_f32 v115, v91\n\t" \
  "v_mov_b32_sdwa v114, v88 dst_sel:WORD_0 dst_unused:UNUSED_PRESERVE src0_sel:WORD_1\n\t" \
  "v_exp_f32 v90, v90\n\t" \
  "v_exp_f32 v116, v93\n\t" \
  "v_mov_b32_sdwa v115, v90 dst_sel:WORD_0 dst_unused:UNUSED_PRESERVE src0_sel:WORD_1\n\t" \
  "v_exp_f32 v92, v92\n\t" \
  "v_exp_f32 v117, v95\n\t" \
  "s_waitcnt lgkmcnt(2)\n\t" \
  "v_mfma_f32_32x32x16_bf16 v[52:67], v[104:107], v[112:115], v[52:67]\n\t" \
  "s_mov_b64 exec, %[vmask]\n\t" \
  "ds_read_b128 v[104:107], v122 offset:2048\n\t" \
  "s_mov_b64 exec, -1\n\t" \
  "v_mov_b32_sdwa v116, v92 dst_sel:WORD_0 dst_unused:UNUSED_PRESERVE src0_sel:WORD_1\n\t" \
  "v_exp_f32 v94, v94\n\t" \
  "v_exp_f32 v118, v97\n\t" \
  "v_mov_b32_sdwa v117, v94 dst_sel:WORD_0 dst_unused:UNUSED_PRESERVE src0_sel:WORD_1\n\t" \
  "v_exp_f32 v96, v96\n\t" \
  "v_exp_f32 v119, v99\n\t" \
  "v_mov_b32_sdwa v118, v96 dst_sel:WORD_0 dst_unused:UNUSED_PRESERVE src0_sel:WORD_1\n\t" \
  "v_exp_f32 v98, v98\n\t" \
  "s_nop 0\n\t" \
  "v_mov_b32_sdwa v119, v98 dst_sel:WORD_0 dst_unused:UNUSED_PRESERVE src0_sel:WORD_1\n\t" \
  "s_waitcnt lgkmcnt(1)\n\t" \
  "v_mfma_f32_32x32x16_bf16 v[84:99], v[100:103], v[32:35], v[36:51]\n\t" \
  "ds_read_b128 v[100:103], v120 offset:4096\n\t" \
  "v_mfma_f32_32x32x16_bf16 v[52:67], v[108:111], v[116:119], v[52:67]\n\t" \
  "s_mov_b64 exec, %[vmask]\n\t" \
  "ds_read_b128 v[108:111], v122 offset:2560\n\t" \
  "s_mov_b64 exec, -1\n\t" \
  "v_exp_f32 v112, v69\n\t" \
  "v_exp_f32 v68, v68\n\t" \
  "v_exp_f32 v113, v71\n\t" \
  "v_mov_b32_sdwa v112, v68 dst_sel:WORD_0 dst_unused:UNUSED_PRESERVE src0_sel:WORD_1\n\t" \
  "v_exp_f32 v70, v70\n\t" \
  "v_exp_f32 v114, v73\n\t" \
  "v_mov_b32_sdwa v113, v70 dst_sel:WORD_0 dst_unused:UNUSED_PRESERVE src0_sel:WORD_1\n\t" \
  "v_exp_f32 v72, v72\n\t" \
  "v_exp_f32 v115, v75\n\t" \
  "v_mov_b32_sdwa v114, v72 dst_sel:WORD_0 dst_unused:UNUSED_PRESERVE src0_sel:WORD_1\n\t" \
  "v_exp_f32 v74, v74\n\t" \
  "v_exp_f32 v116, v77\n\t" \
  "v_mov_b32_sdwa v115, v74 dst_sel:WORD_0 dst_unused:UNUSED_PRESERVE src0_sel:WORD_1\n\t" \
  "v_exp_f32 v76, v76\n\t" \
  "v_exp_f32 v117, v79\n\t" \
  "s_waitcnt lgkmcnt(2)\n\t" \
  "v_mfma_f32_32x32x16_bf16 v[52:67], v[104:107], v[112:115], v[52:67]\n\t" \
  "s_mov_b64 exec, %[vmask]\n\t" \
  "ds_read_b128 v[104:107], v122 offset:3072\n\t" \
  "s_mov_b64 exec, -1\n\t" \
  "v_mov_b32_sdwa v116, v76 dst_sel:WORD_0 dst_unused:UNUSED_PRESERVE src0_sel:WORD_1\n\t" \
  "v_exp_f32 v78, v78\n\t" \
  "v_exp_f32 v118, v81\n\t" \
  "v_mov_b32_sdwa v117, v78 dst_sel:WORD_0 dst_unused:UNUSED_PRESERVE src0_sel:WORD_1\n\t" \
  "v_exp_f32 v80, v80\n\t" \
  "v_exp_f32 v119, v83\n\t" \
  "v_mov_b32_sdwa v118, v80 dst_sel:WORD_0 dst_unused:UNUSED_PRESERVE src0_sel:WORD_1\n\t" \
  "v_exp_f32 v82, v82\n\t" \
  "s_nop 0\n\t" \
  "v_mov_b32_sdwa v119, v82 dst_sel:WORD_0 dst_unused:UNUSED_PRESERVE src0_sel:WORD_1\n\t" \
  "s_waitcnt lgkmcnt(1)\n\t" \
  "v_mfma_f32_32x32x16_bf16 v[68:83], v[100:103], v[32:35], v[36:51]\n\t" \
  "ds_read_b128 v[100:103], v120 offset:5120\n\t" \
  "v_mfma_f32_32x32x16_bf16 v[52:67], v[108:111], v[116:119], v[52:67]\n\t" \
  "s_mov_b64 exec, %[vmask]\n\t" \
  "ds_read_b128 v[108:111], v122 offset:3584\n\t" \
  "s_mov_b64 exec, -1\n\t" \
  "v_exp_f32 v112, v85\n\t" \
  "v_exp_f32 v84, v84\n\t" \
  "v_exp_f32 v113, v87\n\t" \
  "v_mov_b32_sdwa v112, v84 dst_sel:WORD_0 dst_unused:UNUSED_PRESERVE src0_sel:WORD_1\n\t" \
  "v_exp_f32 v86, v86\n\t" \
  "v_exp_f32 v114, v89\n\t" \
  "v_mov_b32_sdwa v113, v86 dst_sel:WORD_0 dst_unused:UNUSED_PRESERVE src0_sel:WORD_1\n\t" \
  "v_exp_f32 v88, v88\n\t" \
  "v_exp_f32 v115, v91\n\t" \
  "v_mov_b32_sdwa v114, v88 dst_sel:WORD_0 dst_unused:UNUSED_PRESERVE src0_sel:WORD_1\n\t" \
  "v_exp_f32 v90, v90\n\t" \
  "v_exp_f32 v116, v93\n\t" \
  "v_mov_b32_sdwa v115, v90 dst_sel:WORD_0 dst_unused:UNUSED_PRESERVE src0_sel:WORD_1\n\t" \
  "v_exp_f32 v92, v92\n\t" \
  "v_exp_f32 v117, v95\n\t" \
  "s_waitcnt lgkmcnt(2)\n\t" \
  "v_mfma_f32_32x32x16_bf16 v[52:67], v[104:107], v[112:115], v[52:67]\n\t" \
  "s_mov_b64 exec, %[vmask]\n\t" \
  "ds_read_b128 v[104:107], v122 offset:4096\n\t" \
  "s_mov_b64 exec, -1\n\t" \
  "v_mov_b32_sdwa v116, v92 dst_sel:WORD_0 dst_unused:UNUSED_PRESERVE src0_sel:WORD_1\n\t" \
  "v_exp_f32 v94, v94\n\t" \
  "v_exp_f32 v118, v97\n\t" \
  "v_mov_b32_sdwa v117, v94 dst_sel:WORD_0 dst_unused:UNUSED_PRESERVE src0_sel:WORD_1\n\t" \
  "v_exp_f32 v96, v96\n\t" \
  "v_exp_f32 v119, v99\n\t" \
  "v_mov_b32_sdwa v118, v96 dst_sel:WORD_0 dst_unused:UNUSED_PRESERVE src0_sel:WORD_1\n\t" \
  "v_exp_f32 v98, v98\n\t" \
  "s_nop 0\n\t" \
  "v_mov_b32_sdwa v119, v98 dst_sel:WORD_0 dst_unused:UNUSED_PRESERVE src0_sel:WORD_1\n\t" \
  "s_waitcnt lgkmcnt(1)\n\t" \
  "v_mfma_f32_32x32x16_bf16 v[84:99], v[100:103], v[32:35], v[36:51]\n\t" \
  "ds_read_b128 v[100:103], v120 offset:6144\n\t" \
  "v_mfma_f32_32x32x16_bf16 v[52:67], v[108:111], v[116:119], v[52:67]\n\t" \
  "s_mov_b64 exec, %[vmask]\n\t" \
  "ds_read_b128 v[108:111], v122 offset:4608\n\t" \
  "s_mov_b64 exec, -1\n\t" \
  "v_exp_f32 v112, v69\n\t" \
  "v_exp_f32 v68, v68\n\t" \
  "v_exp_f32 v113, v71\n\t" \
  "v_mov_b32_sdwa v112, v68 dst_sel:WORD_0 dst_unused:UNUSED_PRESERVE src0_sel:WORD_1\n\t" \
  "v_exp_f32 v70, v70\n\t" \
  "v_exp_f32 v114, v73\n\t" \
  "v_mov_b32_sdwa v113, v70 dst_sel:WORD_0 dst_unused:UNUSED_PRESERVE src0_sel:WORD_1\n\t" \
  "v_exp_f32 v72, v72\n\t" \
  "v_exp_f32 v115, v75\n\t" \
  "v_mov_b32_sdwa v114, v72 dst_sel:WORD_0 dst_unused:UNUSED_PRESERVE src0_sel:WORD_1\n\t" \
  "v_exp_f32 v74, v74\n\t" \
  "v_exp_f32 v116, v77\n\t" \
  "v_mov_b32_sdwa v115, v74 dst_sel:WORD_0 dst_unused:UNUSED_PRESERVE src0_sel:WORD_1\n\t" \
  "v_exp_f32 v76, v76\n\t" \
  "v_exp_f32 v117, v79\n\t" \
  "s_waitcnt lgkmcnt(2)\n\t" \
  "v_mfma_f32_32x32x16_bf16 v[52:67], v[104:107], v[112:115], v[52:67]\n\t" \
  "s_mov_b64 exec, %[vmask]\n\t" \
  "ds_read_b128 v[104:107], v122 offset:5120\n\t" \
  "s_mov_b64 exec, -1\n\t" \
  "v_mov_b32_sdwa v116, v76 dst_sel:WORD_0 dst_unused:UNUSED_PRESERVE src0_sel:WORD_1\n\t" \
  "v_exp_f32 v78, v78\n\t" \
  "v_exp_f32 v118, v81\n\t" \
  "v_mov_b32_sdwa v117, v78 dst_sel:WORD_0 dst_unused:UNUSED_PRESERVE src0_sel:WORD_1\n\t" \
  "v_exp_f32 v80, v80\n\t" \
  "v_exp_f32 v119, v83\n\t" \
  "v_mov_b32_sdwa v118, v80 dst_sel:WORD_0 dst_unused:UNUSED_PRESERVE src0_sel:WORD_1\n\t" \
  "v_exp_f32 v82, v82\n\t" \
  "s_waitcnt vmcnt(0)\n\t" \
  "s_barrier\n\t" \
  "s_add_u32 m0, %[sfree], %[m0k]\n\t" \
  "s_nop 0\n\t" \
  "buffer_load_dwordx4 %[goff], %[rk], %[ssrc] offen lds\n\t" \
  "s_add_u32 m0, m0, 0x2000\n\t" \
  "s_nop 0\n\t" \
  "buffer_load_dwordx4 %[goff], %[rv], %[ssrc] offen lds\n\t" \
  "s_add_u32 %[ssrc], %[ssrc], 0x2000\n\t" \
  "v_mov_b32_sdwa v119, v82 dst_sel:WORD_0 dst_unused:UNUSED_PRESERVE src0_sel:WORD_1\n\t" \
  "s_waitcnt lgkmcnt(1)\n\t" \
  "v_mfma_f32_32x32x16_bf16 v[68:83], v[100:103], v[32:35], v[36:51]\n\t" \
  "ds_read_b128 v[100:103], v120 offset:7168\n\t" \
  "v_mfma_f32_32x32x16_bf16 v[52:67], v[108:111], v[116:119], v[52:67]\n\t" \
  "s_mov_b64 exec, %[vmask]\n\t" \
  "ds_read_b128 v[108:111], v122 offset:5632\n\t" \
  "s_mov_b64 exec, -1\n\t" \
  "v_exp_f32 v112, v85\n\t" \
  "v_exp_f32 v84, v84\n\t" \
  "v_exp_f32 v113, v87\n\t" \
  "v_mov_b32_sdwa v112, v84 dst_sel:WORD_0 dst_unused:UNUSED_PRESERVE src0_sel:WORD_1\n\t" \
  "v_exp_f32 v86, v86\n\t" \
  "v_exp_f32 v114, v89\n\t" \
  "v_mov_b32_sdwa v113, v86 dst_sel:WORD_0 dst_unused:UNUSED_PRESERVE src0_sel:WORD_1\n\t" \
  "v_exp_f32 v88, v88\n\t" \
  "v_exp_f32 v115, v91\n\t" \
  "v_mov_b32_sdwa v114, v88 dst_sel:WORD_0 dst_unused:UNUSED_PRESERVE src0_sel:WORD_1\n\t" \
  "v_exp_f32 v90, v90\n\t" \
  "v_exp_f32 v116, v93\n\t" \
  "v_mov_b32_sdwa v115, v90 dst_sel:WORD_0 dst_unused:UNUSED_PRESERVE src0_sel:WORD_1\n\t" \
  "v_exp_f32 v92, v92\n\t" \
  "v_exp_f32 v117, v95\n\t" \
  "s_waitcnt lgkmcnt(2)\n\t" \
  "v_mfma_f32_32x32x16_bf16 v[52:67], v[104:107], v[112:115], v[52:67]\n\t" \
  "s_mov_b64 exec, %[vmask]\n\t" \
  "ds_read_b128 v[104:107], v122 offset:6144\n\t" \
  "s_mov_b64 exec, -1\n\t" \
  "v_mov_b32_sdwa v116, v92 dst_sel:WORD_0 dst_unused:UNUSED_PRESERVE src0_sel:WORD_1\n\t" \
  "v_exp_f32 v94, v94\n\t" \
  "v_exp_f32 v118, v97\n\t" \
  "v_mov_b32_sdwa v117, v94 dst_sel:WORD_0 dst_unused:UNUSED_PRESERVE src0_sel:WORD_1\n\t" \
  "v_exp_f32 v96, v96\n\t" \
  "v_exp_f32 v119, v99\n\t" \
  "v_mov_b32_sdwa v118, v96 dst_sel:WORD_0 dst_unused:UNUSED_PRESERVE src0_sel:WORD_1\n\t" \
  "v_exp_f32 v98, v98\n\t" \
  "s_nop 0\n\t" \
  "v_mov_b32_sdwa v119, v98 dst_sel:WORD_0 dst_unused:UNUSED_PRESERVE src0_sel:WORD_1\n\t" \
  "s_waitcnt lgkmcnt(1)\n\t" \
  "v_mfma_f32_32x32x16_bf16 v[84:99], v[100:103], v[32:35], v[36:51]\n\t" \
  "ds_read_b128 v[100:103], v121 offset:0\n\t" \
  "v_mfma_f32_32x32x16_bf16 v[52:67], v[108:111], v[116:119], v[52:67]\n\t" \
  "s_mov_b64 exec, %[vmask]\n\t" \
  "ds_read_b128 v[108:111], v122 offset:6656\n\t" \
  "s_mov_b64 exec, -1\n\t" \
  "v_exp_f32 v112, v69\n\t" \
  "v_exp_f32 v68, v68\n\t" \
  "v_exp_f32 v113, v71\n\t" \
  "v_mov_b32_sdwa v112, v68 dst_sel:WORD_0 dst_unused:UNUSED_PRESERVE src0_sel:WORD_1\n\t" \
  "v_exp_f32 v70, v70\n\t" \
  "v_exp_f32 v114, v73\n\t" \
  "v_mov_b32_sdwa v113, v70 dst_sel:WORD_0 dst_unused:UNUSED_PRESERVE src0_sel:WORD_1\n\t" \
  "v_exp_f32 v72, v72\n\t" \
  "v_exp_f32 v115, v75\n\t" \
  "v_mov_b32_sdwa v114, v72 dst_sel:WORD_0 dst_unused:UNUSED_PRESERVE src0_sel:WORD_1\n\t" \
  "v_exp_f32 v74, v74\n\t" \
  "v_exp_f32 v116, v77\n\t" \
  "v_mov_b32_sdwa v115, v74 dst_sel:WORD_0 dst_unused:UNUSED_PRESERVE src0_sel:WORD_1\n\t" \
  "v_exp_f32 v76, v76\n\t" \
  "v_exp_f32 v117, v79\n\t" \
  "s_waitcnt lgkmcnt(2)\n\t" \
  "v_mfma_f32_32x32x16_bf16 v[52:67], v[104:107], v[112:115], v[52:67]\n\t" \
  "s_mov_b64 exec, %[vmask]\n\t" \
  "ds_read_b128 v[104:107], v122 offset:7168\n\t" \
  "s_mov_b64 exec, -1\n\t" \
  "v_mov_b32_sdwa v116, v76 dst_sel:WORD_0 dst_unused:UNUSED_PRESERVE src0_sel:WORD_1\n\t" \
  "v_exp_f32 v78, v78\n\t" \
  "v_exp_f32 v118, v81\n\t" \
  "v_mov_b32_sdwa v117, v78 dst_sel:WORD_0 dst_unused:UNUSED_PRESERVE src0_sel:WORD_1\n\t" \
  "v_exp_f32 v80, v80\n\t" \
  "v_exp_f32 v119, v83\n\t" \
  "v_mov_b32_sdwa v118, v80 dst_sel:WORD_0 dst_unused:UNUSED_PRESERVE src0_sel:WORD_1\n\t" \
  "v_exp_f32 v82, v82\n\t" \
  "s_nop 0\n\t" \
  "v_mov_b32_sdwa v119, v82 dst_sel:WORD_0 dst_unused:UNUSED_PRESERVE src0_sel:WORD_1\n\t" \
  "s_waitcnt lgkmcnt(1)\n\t" \
  "v_mfma_f32_32x32x16_bf16 v[68:83], v[100:103], v[32:35], v[36:51]\n\t" \
  "ds_read_b128 v[100:103], v121 offset:1024\n\t" \
  "v_mfma_f32_32x32x16_bf16 v[52:67], v[108:111], v[116:119], v[52:67]\n\t" \
  "s_mov_b64 exec, %[vmask]\n\t" \
  "ds_read_b128 v[108:111], v122 offset:7680\n\t" \
  "s_mov_b64 exec, -1\n\t" \
  "v_exp_f32 v112, v85\n\t" \
  "v_exp_f32 v84, v84\n\t" \
  "v_exp_f32 v113, v87\n\t" \
  "v_mov_b32_sdwa v112, v84 dst_sel:WORD_0 dst_unused:UNUSED_PRESERVE src0_sel:WORD_1\n\t" \
  "v_exp_f32 v86, v86\n\t" \
  "v_exp_f32 v114, v89\n\t" \
  "v_mov_b32_sdwa v113, v86 dst_sel:WORD_0 dst_unused:UNUSED_PRESERVE src0_sel:WORD_1\n\t" \
  "v_exp_f32 v88, v88\n\t" \
  "v_exp_f32 v115, v91\n\t" \
  "v_mov_b32_sdwa v114, v88 dst_sel:WORD_0 dst_unused:UNUSED_PRESERVE src0_sel:WORD_1\n\t" \
  "v_exp_f32 v90, v90\n\t" \
  "v_exp_f32 v116, v93\n\t" \
  "v_mov_b32_sdwa v115, v90 dst_sel:WORD_0 dst_unused:UNUSED_PRESERVE src0_sel:WORD_1\n\t" \
  "v_exp_f32 v92, v92\n\t" \
  "v_exp_f32 v117, v95\n\t" \
  "s_waitcnt lgkmcnt(2)\n\t" \
  "v_mfma_f32_32x32x16_bf16 v[52:67], v[104:107], v[112:115], v[52:67]\n\t" \
  "s_mov_b64 exec, %[vmask]\n\t" \
  "ds_read_b128 v[104:107], v123 offset:0\n\t" \
  "s_mov_b64 exec, -1\n\t" \
  "v_mov_b32_sdwa v116, v92 dst_sel:WORD_0 dst_unused:UNUSED_PRESERVE src0_sel:WORD_1\n\t" \
  "v_exp_f32 v94, v94\n\t" \
  "v_exp_f32 v118, v97\n\t" \
  "v_mov_b32_sdwa v117, v94 dst_sel:WORD_0 dst_unused:UNUSED_PRESERVE src0_sel:WORD_1\n\t" \
  "v_exp_f32 v96, v96\n\t" \
  "v_exp_f32 v119, v99\n\t" \
  "v_mov_b32_sdwa v118, v96 dst_sel:WORD_0 dst_unused:UNUSED_PRESERVE src0_sel:WORD_1\n\t" \
  "v_exp_f32 v98, v98\n\t" \
  "s_nop 0\n\t" \
  "v_mov_b32_sdwa v119, v98 dst_sel:WORD_0 dst_unused:UNUSED_PRESERVE src0_sel:WORD_1\n\t" \
  "s_mov_b32 %[stmp], %[scur]\n\t" \
  "s_mov_b32 %[scur], %[snext]\n\t" \
  "s_mov_b32 %[snext], %[sfree]\n\t" \
  "s_mov_b32 %[sfree], %[stmp]\n\t" \
  "s_sub_u32 %[cnt], %[cnt], 1\n\t" \
  "s_cmp_lg_u32 %[cnt], 0\n\t" \
  "s_cbranch_scc1 1b\n\t" \
  "s_waitcnt lgkmcnt(0)\n\t" \
  "v_mfma_f32_32x32x16_bf16 v[52:67], v[108:111], v[116:119], v[52:67]\n\t" \
  "s_waitcnt vmcnt(0)\n\t" \
  "s_nop 15\n\t"
#define ATTN_CLOBBERS "v36", "v37", "v38", "v39", "v40", "v41", "v42", "v43", "v44", "v45", "v46", "v47", "v48", "v49", "v50", "v51", "v68", "v69", "v70", "v71", "v72", "v73", "v74", "v75", "v76", "v77", "v78", "v79", "v80", "v81", "v82", "v83", "v84", "v85", "v86", "v87", "v88", "v89", "v90", "v91", "v92", "v93", "v94", "v95", "v96", "v97", "v98", "v99", "v100", "v101", "v102", "v103", "v104", "v105", "v106", "v107", "v108", "v109", "v110", "v111", "v112", "v113", "v114", "v115", "v116", "v117", "v118", "v119", "v120", "v121", "v122", "v123", "m0", "scc", "memory"

constexpr int AT_SLOT = 16384, AT_LDS = 3 * AT_SLOT + 64;
__global__ __launch_bounds__(512, 2) void k_attn(const u16* __restrict__ Q, const u16* __restrict__ Kf,
                                                 const u16* __restrict__ Vf, u16* __restrict__ O) {
#if defined(__HIP_DEVICE_COMPILE__)
  extern __shared__ __attribute__((aligned(16))) char lds[];
  STAMP_DECL
  RSTAMP(0); STAMP(1);
  const int head = blockIdx.x >> 3, qb = blockIdx.x & 7;
  const int tid = threadIdx.x, lane = tid & 63, l31 = lane & 31, h = lane >> 5;
  const int wid = __builtin_amdgcn_readfirstlane(tid >> 6);
  auto krs = mk_rsrc((const char*)Kf + (size_t)head * (S_ * 32), S_ * 32);
  auto vrs = mk_rsrc((const char*)Vf + (size_t)head * (S_ * 32), S_ * 32);
  const int q = qb * 256 + wid * 32 + l31;
  bf16x8 qf = *(const bf16x8*)((const char*)Q + (size_t)head * (S_ * 32) + q * 32 + h * 16);
  auto issue = [&](int c, int slot) {
    char* sb = lds + slot * AT_SLOT + wid * 1024;
    __builtin_amdgcn_raw_ptr_buffer_load_lds(krs, (ldsptr_t)(sb), 16, lane * 16, c * 8192 + wid * 1024, 0, 0);
    __builtin_amdgcn_raw_ptr_buffer_load_lds(vrs, (ldsptr_t)(sb + 8192), 16, lane * 16, c * 8192 + wid * 1024, 0, 0);
  };
  const unsigned ldsb = (unsigned)(size_t)lds;
  const unsigned lk = ldsb + lane * 16;
  const unsigned lv = ldsb + 8192 + h * 256 + (l31 & 15) * 16;
  const unsigned goff = lane * 16;
  const unsigned m0k = ldsb + wid * 1024;
  int* flag = (int*)(lds + 3 * AT_SLOT);
  float mref = 0.f;
  f32x16 o;
  for (int attempt = 0; attempt < 2; ++attempt) {
    if (attempt == 1) {
      float mx = -__builtin_inff();
      for (int c = 0; c < 8; ++c) {
        issue(c, 0);
        WAIT_V(0); RAW_BARRIER();
        for (int t = 0; t < 8; ++t) {
          bf16x8 kf = *(const bf16x8*)(lds + t * 1024 + lane * 16);
          f32x16 z = {};
          f32x16 s = __builtin_amdgcn_mfma_f32_32x32x16_bf16(kf, qf, z, 0, 0, 0);
#pragma unroll
          for (int r = 0; r < 16; ++r) mx = fmaxf(mx, s[r]);
        }
        RAW_BARRIER();
      }
      mx = fmaxf(mx, __shfl_xor(mx, 32));
      mref = mx;
    }
    STAMP(2);
    issue(0, 0);
    issue(1, 1);
    STAMP(3);
    WAIT_V(2); RAW_BARRIER();
    STAMP(4);
    if (attempt == 0) {
      bf16x8 kf = *(const bf16x8*)(lds + lane * 16);
      f32x16 z = {};
      f32x16 s = __builtin_amdgcn_mfma_f32_32x32x16_bf16(kf, qf, z, 0, 0, 0);
      float mx = s[0];
#pragma unroll
      for (int r = 1; r < 16; ++r) mx = fmaxf(mx, s[r]);
      mx = fmaxf(mx, __shfl_xor(mx, 32));
      mref = mx;
    }
    STAMP(5);
    {
      float negm = -mref;
      unsigned scur, snext, sfree, stmp, cnt, ssrc = 2 * 8192 + wid * 1024;
      unsigned long long vmask = 0x0000FFFF0000FFFFull;
      asm volatile(ATTN_ASM
                   : "={v[52:67]}"(o), [scur] "=&s"(scur), [snext] "=&s"(snext), [sfree] "=&s"(sfree), [stmp] "=&s"(stmp),
                     [cnt] "=&s"(cnt), [ssrc] "+s"(ssrc)
                   : "{v[32:35]}"(qf), [negm] "v"(negm), [lk] "v"(lk), [lv] "v"(lv), [goff] "v"(goff), [rk] "s"(krs), [rv] "s"(vrs),
                     [m0k] "s"(m0k), [vmask] "s"(vmask)
                   : ATTN_CLOBBERS);
    }
    STAMP(40);
    bool bad = !(o[8] < 1.0e30f);
#pragma unroll
    for (int r = 0; r < 8; ++r) bad |= !(fabsf(o[r]) < 3.0e38f);
    if (tid == 0) *flag = 0;
    RAW_BARRIER();
    if (attempt == 0 && __any(bad)) { if (lane == 0) *flag = 1; }
    RAW_BARRIER();
    int f = *flag;
    RAW_BARRIER();
    if (f == 0) break;
  }
  float inv = 1.0f / o[8];
  u32x4 ov = widen(pk4(o[0] * inv, o[1] * inv, o[2] * inv, o[3] * inv), pk4(o[4] * inv, o[5] * inv, o[6] * inv, o[7] * inv));
  *(u32x4*)(O + (size_t)q * D_ + head * 16 + h * 8) = ov;
  STAMP(41); RSTAMP(42); STAMP_FLUSH;
#endif
}

#define SEL(i) true
extern "C" void kernel_launch(void* const* d_in, const int* in_sizes, int n_in, void* d_out, int out_size,
                              void* d_ws, size_t ws_size, hipStream_t stream) {
  const u16* x = (const u16*)d_in[0];
  const u16* wqkv = (const u16*)d_in[1];
  const u16* wo = (const u16*)d_in[2];
  const u16* w1 = (const u16*)d_in[4];
  const u16* w2 = (const u16*)d_in[5];
  const u16* w3 = (const u16*)d_in[6];
  const u16* w4 = (const u16*)d_in[7];
  u16* out = (u16*)d_out;
  char* ws = (char*)d_ws;
  const size_t MB = 1 << 20;
  u16* xn = (u16*)(ws + 0 * MB);
  u16* Q = (u16*)(ws + 2 * MB);
  u16* Kf = (u16*)(ws + 4 * MB);
  u16* Vf = (u16*)(ws + 6 * MB);
  u16* O = (u16*)(ws + 10 * MB);
  u16* hbuf = (u16*)(ws + 12 * MB);
  u16* x1 = (u16*)(ws + 16 * MB);
  u16* x1n = (u16*)(ws + 18 * MB);
  u16* h2 = (u16*)(ws + 20 * MB);
  u16* bout = (u16*)(ws + 28 * MB);

  if (SEL(0)) k_ln<<<512, 256, 0, stream>>>(x, xn);
  {
    size_t sh = 4 * (128 * 128 + 64 * 256);
    (void)hipFuncSetAttribute((const void*)k_qkv, hipFuncAttributeMaxDynamicSharedMemorySize, (int)sh);
    if (SEL(1)) k_qkv<<<16 * 12, 512, sh, stream>>>(xn, wqkv, Q, Kf, Vf);
  }
  size_t sh64 = 4 * (64 * 128 + 64 * 128);
  (void)hipFuncSetAttribute((const void*)k_gemm64<EPI_H>, hipFuncAttributeMaxDynamicSharedMemorySize, (int)sh64);
  (void)hipFuncSetAttribute((const void*)k_gemm64<EPI_X1>, hipFuncAttributeMaxDynamicSharedMemorySize, (int)sh64);
  (void)hipFuncSetAttribute((const void*)k_gemm64<EPI_H2>, hipFuncAttributeMaxDynamicSharedMemorySize, (int)sh64);
  (void)hipFuncSetAttribute((const void*)k_gemm64<EPI_B>, hipFuncAttributeMaxDynamicSharedMemorySize, (int)sh64);
  (void)hipFuncSetAttribute((const void*)k_gemm64<EPI_OUT>, hipFuncAttributeMaxDynamicSharedMemorySize, (int)sh64);
  if (SEL(2)) k_gemm64<EPI_H><<<32 * 8, 256, sh64, stream>>>(xn, w1, nullptr, nullptr, hbuf);
  {
    size_t sh = AT_LDS;
    (void)hipFuncSetAttribute((const void*)k_attn, hipFuncAttributeMaxDynamicSharedMemorySize, (int)sh);
    if (SEL(3)) k_attn<<<256, 512, sh, stream>>>(Q, Kf, Vf, O);
  }
  if (SEL(4)) k_gemm64<EPI_X1><<<32 * 8, 256, sh64, stream>>>(hbuf, w2, xn, nullptr, x1);
  if (SEL(5)) k_ln<<<512, 256, 0, stream>>>(x1, x1n);
  if (SEL(6)) k_gemm64<EPI_H2><<<32 * 22, 256, sh64, stream>>>(x1n, w3, nullptr, nullptr, h2);
  if (SEL(7)) k_gemm64<EPI_B><<<32 * 8, 256, sh64, stream>>>(h2, w4, x1, nullptr, bout);
  if (SEL(8)) k_gemm64<EPI_OUT><<<32 * 8, 256, sh64, stream>>>(O, wo, x, bout, out);
}
```

```cpp
#include <hip/hip_runtime.h>
#include <stdint.h>

typedef __attribute__((ext_vector_type(8))) __bf16 bf16x8;
typedef __attribute__((ext_vector_type(2))) __bf16 bf16x2;
typedef __attribute__((ext_vector_type(4))) short s16x4;
typedef __attribute__((ext_vector_type(16))) float f32x16;
typedef __attribute__((ext_vector_type(4))) float f32x4;
typedef __attribute__((ext_vector_type(2))) float f32x2;
typedef __attribute__((ext_vector_type(4))) unsigned u32x4;
typedef __attribute__((ext_vector_type(2))) unsigned u32x2;
typedef __attribute__((address_space(3))) void* ldsptr_t;
typedef unsigned short u16;

#define DEV __device__ __forceinline__

constexpr int S_ = 2048, D_ = 512, H_ = 32, F_ = 1376;
constexpr float LN_EPS = 1e-5f;
constexpr float QSCALE = 0.25f * 1.4426950408889634f;

constexpr size_t MB_ = 1 << 20, KB_ = 1 << 10;
constexpr size_t OFF_Q = 0, OFF_K = 2 * MB_, OFF_V = 4 * MB_, OFF_O = 6 * MB_, OFF_X1 = 8 * MB_, OFF_HB = 10 * MB_;
constexpr size_t OFF_ST0 = 12 * MB_ + 64 * KB_, OFF_CNT = 12 * MB_ + 96 * KB_, OFF_CS3 = 12 * MB_ + 128 * KB_;
constexpr size_t OFF_ST1 = 12 * MB_ + 256 * KB_, OFF_H2 = 13 * MB_, OFF_R = 19 * MB_;
constexpr int HB_ROWS = S_ + 4;
constexpr int CS3_P = 8, CS3_LD = 1408;

template <int N> struct IC { static constexpr int value = N; constexpr operator int() const { return N; } };
template <int I, int N, class F> DEV void static_for_impl(F&& f) {
  if constexpr (I < N) { f(IC<I>{}); static_for_impl<I + 1, N>(f); }
}
template <int N, class F> DEV void static_for(F&& f) { static_for_impl<0, N>(f); }

DEV unsigned pk_bf16(float lo, float hi) {
  f32x2 v = {lo, hi};
  return __builtin_bit_cast(unsigned, __builtin_convertvector(v, bf16x2));
}
DEV float bf_lo(unsigned u) { return __uint_as_float(u << 16); }
DEV float bf_hi(unsigned u) { return __uint_as_float(u & 0xffff0000u); }
DEV float gelu_tanh(float x) {
  const float c1 = -2.0f * 0.7978845608028654f * 1.4426950408889634f;
  const float c2 = c1 * 0.044715f;
  float t = x * (c1 + c2 * x * x);
  float e = __builtin_amdgcn_exp2f(t);
  return x * __builtin_amdgcn_rcpf(1.0f + e);
}
DEV float dot2(unsigned a, unsigned b, float c) {
  return __builtin_amdgcn_fdot2_f32_bf16(__builtin_bit_cast(bf16x2, a), __builtin_bit_cast(bf16x2, b), c, false);
}
DEV float xhalf_sum(float v) {
  auto r = __builtin_amdgcn_permlane32_swap(__float_as_uint(v), __float_as_uint(v), false, false);
  return __uint_as_float(r[0]) + __uint_as_float(r[1]);
}
#define STAMP(i)
#define RSTAMP(i)
#define STAMP_DECL
#define STAMP_ARG
#define STAMP_PASS
#define STAMP_FLUSH(w)
#define WAIT_V(n) asm volatile("s_waitcnt vmcnt(%0)" ::"n"(n) : "memory")
#define WAIT_L(n) asm volatile("s_waitcnt lgkmcnt(%0)" ::"n"(n) : "memory")
#define SCHED_FENCE() __builtin_amdgcn_sched_barrier(0)
#define RAW_BARRIER() do { WAIT_L(0); __builtin_amdgcn_s_barrier(); } while (0)
#define BARRIER_NOWAIT() do { SCHED_FENCE(); asm volatile("s_barrier" ::: "memory"); SCHED_FENCE(); } while (0)

DEV unsigned fresh_lane() {
  unsigned l;
  asm volatile("v_mbcnt_lo_u32_b32 %0, -1, 0\n\tv_mbcnt_hi_u32_b32 %0, -1, %0" : "=v"(l));
  return l;
}
DEV __amdgpu_buffer_rsrc_t mk_rsrc(const void* p, unsigned bytes) {
  return __builtin_amdgcn_make_buffer_rsrc((void*)p, 0, (int)bytes, 0x00020000);
}
DEV void dma16(__amdgpu_buffer_rsrc_t r, char* lds_dst, unsigned voff, unsigned soff) {
  __builtin_amdgcn_raw_ptr_buffer_load_lds(r, (ldsptr_t)lds_dst, 16, voff, soff, 0, 0);
}
DEV bf16x8 tr_frag(const char* p0, const char* p1) {
  s16x4 a = __builtin_amdgcn_ds_read_tr16_b64_v4i16((__attribute__((address_space(3))) s16x4*)p0);
  s16x4 b = __builtin_amdgcn_ds_read_tr16_b64_v4i16((__attribute__((address_space(3))) s16x4*)p1);
  typedef __attribute__((ext_vector_type(8))) short s16x8;
  s16x8 r = __builtin_shufflevector(a, b, 0, 1, 2, 3, 4, 5, 6, 7);
  return __builtin_bit_cast(bf16x8, r);
}
DEV f32x16 mfma32(bf16x8 a, bf16x8 b, f32x16 c) { return __builtin_amdgcn_mfma_f32_32x32x16_bf16(a, b, c, 0, 0, 0); }
DEV u32x2 pk4(float a, float b, float c, float d) { u32x2 r; r[0] = pk_bf16(a, b); r[1] = pk_bf16(c, d); return r; }
DEV u32x4 widen(u32x2 X, u32x2 Y) {
  u32x4 o;
  auto a = __builtin_amdgcn_permlane32_swap(X[0], Y[0], false, false);
  auto b = __builtin_amdgcn_permlane32_swap(X[1], Y[1], false, false);
  o[0] = a[0]; o[1] = b[0]; o[2] = a[1]; o[3] = b[1];
  return o;
}
DEV u32x4 pack8(const float* v) { return widen(pk4(v[0], v[1], v[2], v[3]), pk4(v[4], v[5], v[6], v[7])); }

constexpr int K1_A = 16384, K1_WQ = 12288, K1_ST = 32768;
constexpr int K1_X = 4 * K1_ST;
constexpr int K1_LDS = K1_X + 2048 + 2048 + 1536;

template <int WN, bool V0>
DEV void k1_main(const char* lds, int wm, int lane, f32x16& acc0, f32x16& acc1, float& s1, float& s2, float& c0, float& c1 STAMP_ARG) {
  const unsigned l31 = lane & 31, h = lane >> 5, blk = (lane >> 4) & 1, q = (lane & 15) >> 2, p = lane & 3;
  const unsigned row = wm * 32 + l31;
  unsigned ao[4];
#pragma unroll
  for (int s = 0; s < 4; ++s) ao[s] = row * 128 + (((2 * s + h) ^ ((row >> 1) & 7)) << 4);
  const unsigned cpart = (2 * blk + (p >> 1)) * 16 + 8 * (p & 1);
  const unsigned w0o = K1_A + (8 * h + q) * 192 + (2 * WN) * 64 + cpart;
  const unsigned w1o = WN == 0 ? K1_A + (8 * h + q) * 192 + 64 + cpart : K1_A + K1_WQ + (8 * h + q) * 64 + cpart;
  constexpr int RB1 = WN == 0 ? 192 : 64;
  bf16x8 xf[2], w0[2], w1[2];
  auto rd = [&](int t, int s, int b) {
    const char* sb = lds + (t & 3) * K1_ST;
    xf[b] = *(const bf16x8*)(sb + ao[s]);
    w0[b] = tr_frag(sb + w0o + s * 16 * 192, sb + w0o + s * 16 * 192 + 4 * 192);
    w1[b] = tr_frag(sb + w1o + s * 16 * RB1, sb + w1o + s * 16 * RB1 + 4 * RB1);
  };
  auto mm = [&](int s, int b) {
    acc0 = V0 ? mfma32(xf[b], w0[b], acc0) : mfma32(w0[b], xf[b], acc0);
    acc1 = (V0 && WN == 0) ? mfma32(xf[b], w1[b], acc1) : mfma32(w1[b], xf[b], acc1);
    if ((s >> 1) == WN) {
      u32x4 u = __builtin_bit_cast(u32x4, xf[b]);
#pragma unroll
      for (int i = 0; i < 4; ++i) { s1 = dot2(u[i], 0x3f803f80u, s1); s2 = dot2(u[i], u[i], s2); }
    }
    if (s == wm) {
      u32x4 u = __builtin_bit_cast(u32x4, w0[b]), v = __builtin_bit_cast(u32x4, w1[b]);
#pragma unroll
      for (int i = 0; i < 4; ++i) { c0 = dot2(u[i], 0x3f803f80u, c0); c1 = dot2(v[i], 0x3f803f80u, c1); }
    }
  };
#pragma unroll
  for (int t = 0; t < 8; ++t) {
    WAIT_L(0);
    BARRIER_NOWAIT();
    STAMP(4 + t);
    rd(t, 0, 0);
    if (t > 0) mm(3, 1);
    SCHED_FENCE();
    rd(t, 1, 1);
    mm(0, 0);
    SCHED_FENCE();
    rd(t, 2, 0);
    mm(1, 1);
    SCHED_FENCE();
    rd(t, 3, 1);
    mm(2, 0);
  }
  mm(3, 1);
}

DEV void k1_epi_s(const f32x16& a, const float* fin, int sub, int kind, int J, int m0, int wm, int lane, char* ws) {
  const int l31 = lane & 31, h = lane >> 5;
  const int rl = wm * 32 + l31, s = m0 + rl;
  float mu = fin[rl], rs = fin[128 + rl];
  if (kind == 0) rs *= QSCALE;
  if (kind == 2) {
    float r2 = rs * rs;
    rs *= __builtin_amdgcn_rsqf((1.0f - LN_EPS * r2) + LN_EPS);
  }
  float v[16];
#pragma unroll
  for (int j = 0; j < 4; ++j) {
    f32x4 c = *(const f32x4*)(fin + 256 + sub * 32 + 8 * j + 4 * h);
#pragma unroll
    for (int i = 0; i < 4; ++i) v[4 * j + i] = (a[4 * j + i] - mu * c[i]) * rs;
  }
  if (kind == 2) {
#pragma unroll
    for (int i = 0; i < 16; ++i) v[i] = gelu_tanh(v[i]);
  }
#pragma unroll
  for (int g = 0; g < 2; ++g) {
    u32x4 o = pack8(v + 8 * g);
    char* dst;
    if (kind == 0) dst = ws + OFF_Q + (size_t)(2 * J + g) * (S_ * 32) + s * 32 + h * 16;
    else if (kind == 1) dst = ws + OFF_K + (size_t)(2 * (J - 16) + g) * (S_ * 32) + (s >> 5) * 1024 + h * 512 + (s & 31) * 16;
    else dst = ws + OFF_HB + ((size_t)(4 * J + 2 * g + h) * HB_ROWS + s + 2) * 16;
    *(u32x4*)dst = o;
  }
}
DEV void k1_epi_v(const f32x16& a, const float* fin, int sub, int J, int m0, int wm, int lane, char* ws) {
  const int l31 = lane & 31, h = lane >> 5;
  const float cs = fin[256 + sub * 32 + l31];
  float v[16];
#pragma unroll
  for (int j = 0; j < 4; ++j) {
    f32x4 mu = *(const f32x4*)(fin + wm * 32 + 8 * j + 4 * h), rs = *(const f32x4*)(fin + 128 + wm * 32 + 8 * j + 4 * h);
#pragma unroll
    for (int i = 0; i < 4; ++i) v[4 * j + i] = (a[4 * j + i] - mu[i] * cs) * rs[i];
  }
  const int n = (J - 32) * 32 + l31, head = n >> 4, d = n & 15, tile = (m0 >> 5) + wm;
  char* base = ws + OFF_V + (size_t)head * (S_ * 32) + tile * 1024;
#pragma unroll
  for (int s = 0; s < 2; ++s) {
    u32x4 o;
#pragma unroll
    for (int j = 0; j < 4; ++j) o[j] = pk_bf16(v[8 * s + 2 * j], v[8 * s + 2 * j + 1]);
    *(u32x4*)(base + (s * 2 + h) * 256 + d * 16) = o;
  }
}

__global__ __launch_bounds__(768, 3) void k1(const u16* __restrict__ x, const u16* __restrict__ wqkv, const u16* __restrict__ w1,
                                             const u16* __restrict__ w3, const u16* __restrict__ w2, const u16* __restrict__ w4,
                                             const u16* __restrict__ wo, char* __restrict__ ws) {
#if defined(__HIP_DEVICE_COMPILE__)
  extern __shared__ __attribute__((aligned(16))) char lds[];
  STAMP_DECL
  RSTAMP(0); STAMP(1);
  const int b = blockIdx.x, xcd = b & 7, bi = b >> 3;
  const int mt = (xcd & 3) * 4 + (bi & 3), nt = (xcd >> 2) * 8 + (bi >> 2);
  const int m0 = mt * 128;
  const int tid = threadIdx.x, lane = tid & 63;
  const int wid = __builtin_amdgcn_readfirstlane(tid >> 6);
  if (wid >= 8) {
    const int lw = wid - 8;
    auto xrs = mk_rsrc(x, S_ * D_ * 2);
    auto qrs = mk_rsrc(wqkv, D_ * 3 * D_ * 2);
    auto w1rs = mk_rsrc(w1, D_ * D_ * 2);
    unsigned va[4], vq[3], v1;
#pragma unroll
    for (int i = 0; i < 4; ++i) {
      unsigned bb = (lw + 4 * i) * 1024 + lane * 16, row = bb >> 7, cp = (bb >> 4) & 7;
      va[i] = (m0 + row) * 1024 + ((cp ^ ((row >> 1) & 7)) << 4);
    }
#pragma unroll
    for (int i = 0; i < 3; ++i) {
      unsigned bb = (lw + 4 * i) * 1024 + lane * 16, row = bb / 192, col = bb % 192;
      vq[i] = row * 3072 + nt * 192 + col;
    }
    { unsigned bb = lw * 1024 + lane * 16; v1 = (bb >> 6) * 1024 + nt * 64 + (bb & 63); }
    const int rot = ((mt + nt) & 1) * 4;
    auto issue = [&](int t) {
      char* sb = lds + (t & 3) * K1_ST;
      const unsigned kt = (t + rot) & 7;
#pragma unroll
      for (int i = 0; i < 4; ++i) dma16(xrs, sb + (lw + 4 * i) * 1024, va[i], kt * 128);
#pragma unroll
      for (int i = 0; i < 3; ++i) dma16(qrs, sb + K1_A + (lw + 4 * i) * 1024, vq[i], kt * (64 * 3072));
      dma16(w1rs, sb + K1_A + K1_WQ + lw * 1024, v1, kt * (64 * 1024));
    };
    issue(0); issue(1); issue(2); issue(3);
    const int item = b * 4 + lw;
    const void* mp; unsigned mbytes, mv, mstride;
    if (item < 88) {
      int cg = item % 11, rg = item / 11;
      mp = w3; mbytes = D_ * F_ * 2; mstride = 4 * F_ * 2;
      mv = (rg * 64 + (lane >> 4)) * (F_ * 2) + cg * 256 + (lane & 15) * 16;
    } else if (item < 96) { mp = w2; mbytes = D_ * D_ * 2; mstride = 4096; mv = (item - 88) * 65536 + lane * 64; }
    else if (item < 104) { mp = wo; mbytes = D_ * D_ * 2; mstride = 4096; mv = (item - 96) * 65536 + lane * 64; }
    else if (item < 126) { mp = w4; mbytes = D_ * F_ * 2; mstride = 4096; mv = (item - 104) * 65536 + lane * 64; }
    else { mp = w2; mbytes = 64; mstride = 0; mv = 0; }
    auto mrs = mk_rsrc(mp, mbytes);
    u32x4 md[16];
#pragma unroll
    for (int i = 0; i < 16; ++i) md[i] = __builtin_amdgcn_raw_buffer_load_b128(mrs, mv, i * mstride, 0);
    WAIT_V(40); BARRIER_NOWAIT();
    WAIT_V(32); BARRIER_NOWAIT(); issue(4);
    WAIT_V(32); BARRIER_NOWAIT(); issue(5);
    WAIT_V(32); BARRIER_NOWAIT(); issue(6);
    WAIT_V(16); BARRIER_NOWAIT(); issue(7);
    WAIT_V(16); BARRIER_NOWAIT();
    WAIT_V(8);  BARRIER_NOWAIT();
    WAIT_V(0);  BARRIER_NOWAIT();
    if (item < 88) {
      float cs[8];
#pragma unroll
      for (int j = 0; j < 8; ++j) cs[j] = 0.f;
#pragma unroll
      for (int i = 0; i < 16; ++i)
#pragma unroll
        for (int j = 0; j < 4; ++j) { cs[2 * j] += bf_lo(md[i][j]); cs[2 * j + 1] += bf_hi(md[i][j]); }
#pragma unroll
      for (int j = 0; j < 8; ++j) { cs[j] += __shfl_xor(cs[j], 16); cs[j] = xhalf_sum(cs[j]); }
      if (lane < 16) {
        float* dst = (float*)(ws + OFF_CS3) + (item / 11) * CS3_LD + (item % 11) * 128 + lane * 8;
        *(f32x4*)dst = f32x4{cs[0], cs[1], cs[2], cs[3]};
        *(f32x4*)(dst + 4) = f32x4{cs[4], cs[5], cs[6], cs[7]};
      }
    } else {
      unsigned keep = 0;
#pragma unroll
      for (int i = 0; i < 16; ++i) asm volatile("" ::"v"(md[i]));
      (void)keep;
      if (item == 126) ((unsigned*)(ws + OFF_CNT))[lane * 64] = 0;
      if (item >= 128 && item < 132) {
        u32x4 z = {0, 0, 0, 0};
        int r = item - 128;
        *(u32x4*)(ws + OFF_HB + ((size_t)lane * HB_ROWS + (r < 2 ? r : S_ + r)) * 16) = z;
      }
    }
    return;
  }
  const int wm = wid & 3, wn = wid >> 2;
  f32x16 acc0 = {}, acc1 = {};
  float s1 = 0.f, s2 = 0.f, c0 = 0.f, c1 = 0.f;
  const bool v0 = wn == 0 ? (nt >= 11) : (nt >= 10);
  if (wn == 0) { if (v0) k1_main<0, true>(lds, wm, lane, acc0, acc1, s1, s2, c0, c1 STAMP_PASS); else k1_main<0, false>(lds, wm, lane, acc0, acc1, s1, s2, c0, c1 STAMP_PASS); }
  else         { if (v0) k1_main<1, true>(lds, wm, lane, acc0, acc1, s1, s2, c0, c1 STAMP_PASS); else k1_main<1, false>(lds, wm, lane, acc0, acc1, s1, s2, c0, c1 STAMP_PASS); }
  STAMP(40);
  float* rsb = (float*)(lds + K1_X);
  float* csb = rsb + 512;
  float* fin = csb + 512;
  s1 = xhalf_sum(s1); s2 = xhalf_sum(s2); c0 = xhalf_sum(c0); c1 = xhalf_sum(c1);
  if (lane < 32) {
    *(f32x2*)(rsb + (wn * 128 + wm * 32 + lane) * 2) = f32x2{s1, s2};
    csb[wm * 128 + wn * 64 + lane] = c0;
    csb[wm * 128 + wn * 64 + 32 + lane] = c1;
  }
  RAW_BARRIER();
  if (tid < 128) {
    f32x2 a = *(const f32x2*)(rsb + tid * 2), c = *(const f32x2*)(rsb + (128 + tid) * 2);
    float mu = (a[0] + c[0]) * (1.0f / D_);
    float var = fmaxf((a[1] + c[1]) * (1.0f / D_) - mu * mu, 0.f);
    float rs = __builtin_amdgcn_rsqf(var + LN_EPS);
    fin[tid] = mu; fin[128 + tid] = rs;
    if (nt == 0) *(f32x2*)(ws + OFF_ST0 + (size_t)(m0 + tid) * 8) = f32x2{mu, rs};
  } else if (tid < 256) {
    int c = tid - 128;
    fin[256 + c] = (csb[c] + csb[128 + c]) + (csb[256 + c] + csb[384 + c]);
  }
  RAW_BARRIER();
  const int J0 = 3 * nt + 2 * wn;
  if (wn == 0) {
    if (v0) { k1_epi_v(acc0, fin, 0, J0, m0, wm, lane, ws); k1_epi_v(acc1, fin, 1, J0 + 1, m0, wm, lane, ws); }
    else {
      if (J0 < 16) k1_epi_s(acc0, fin, 0, 0, J0, m0, wm, lane, ws); else k1_epi_s(acc0, fin, 0, 1, J0, m0, wm, lane, ws);
      if (J0 + 1 < 16) k1_epi_s(acc1, fin, 1, 0, J0 + 1, m0, wm, lane, ws); else k1_epi_s(acc1, fin, 1, 1, J0 + 1, m0, wm, lane, ws);
    }
  } else {
    if (v0) k1_epi_v(acc0, fin, 2, J0, m0, wm, lane, ws);
    else if (J0 < 16) k1_epi_s(acc0, fin, 2, 0, J0, m0, wm, lane, ws);
    else k1_epi_s(acc0, fin, 2, 1, J0, m0, wm, lane, ws);
    k1_epi_s(acc1, fin, 3, 2, nt, m0, wm, lane, ws);
  }
  STAMP(41); RSTAMP(42); STAMP_FLUSH(0);
#endif
}
#define ATTN_ASM \
  "v_mov_b32 v36, %[negm]\n\t" \
  "v_mov_b32 v37, %[negm]\n\t" \
  "v_mov_b32 v38, %[negm]\n\t" \
  "v_mov_b32 v39, %[negm]\n\t" \
  "v_mov_b32 v40, %[negm]\n\t" \
  "v_mov_b32 v41, %[negm]\n\t" \
  "v_mov_b32 v42, %[negm]\n\t" \
  "v_mov_b32 v43, %[negm]\n\t" \
  "v_mov_b32 v44, %[negm]\n\t" \
  "v_mov_b32 v45, %[negm]\n\t" \
  "v_mov_b32 v46, %[negm]\n\t" \
  "v_mov_b32 v47, %[negm]\n\t" \
  "v_mov_b32 v48, %[negm]\n\t" \
  "v_mov_b32 v49, %[negm]\n\t" \
  "v_mov_b32 v50, %[negm]\n\t" \
  "v_mov_b32 v51, %[negm]\n\t" \
  "v_mov_b32 v52, 0\n\t" \
  "v_mov_b32 v53, 0\n\t" \
  "v_mov_b32 v54, 0\n\t" \
  "v_mov_b32 v55, 0\n\t" \
  "v_mov_b32 v56, 0\n\t" \
  "v_mov_b32 v57, 0\n\t" \
  "v_mov_b32 v58, 0\n\t" \
  "v_mov_b32 v59, 0\n\t" \
  "v_mov_b32 v60, 0\n\t" \
  "v_mov_b32 v61, 0\n\t" \
  "v_mov_b32 v62, 0\n\t" \
  "v_mov_b32 v63, 0\n\t" \
  "v_mov_b32 v64, 0\n\t" \
  "v_mov_b32 v65, 0\n\t" \
  "v_mov_b32 v66, 0\n\t" \
  "v_mov_b32 v67, 0\n\t" \
  "v_mov_b32 v104, 0x3f803f80\n\t" \
  "v_mov_b32 v108, 0x3f803f80\n\t" \
  "v_mov_b32 v116, 0\n\t" \
  "v_mov_b32 v105, 0x3f803f80\n\t" \
  "v_mov_b32 v109, 0x3f803f80\n\t" \
  "v_mov_b32 v117, 0\n\t" \
  "v_mov_b32 v106, 0x3f803f80\n\t" \
  "v_mov_b32 v110, 0x3f803f80\n\t" \
  "v_mov_b32 v118, 0\n\t" \
  "v_mov_b32 v107, 0x3f803f80\n\t" \
  "v_mov_b32 v111, 0x3f803f80\n\t" \
  "v_mov_b32 v119, 0\n\t" \
  "s_mov_b32 %[scur], 0\n\t" \
  "s_mov_b32 %[snext], 0x4000\n\t" \
  "s_mov_b32 %[sfree], 0x8000\n\t" \
  "s_mov_b32 %[cnt], 8\n\t" \
  "ds_read_b128 v[100:103], %[lk]\n\t" \
  "s_waitcnt lgkmcnt(0)\n\t" \
  "v_mfma_f32_32x32x16_bf16 v[68:83], v[100:103], v[32:35], v[36:51]\n\t" \
  "ds_read_b128 v[100:103], %[lk] offset:1024\n\t" \
  "s_mov_b64 exec, %[vmask]\n\t" \
  "ds_read_b128 v[104:107], %[lv]\n\t" \
  "s_mov_b64 exec, -1\n\t" \
  "s_nop 7\n\t" \
  "1:\n\t" \
  "v_add_u32 v120, %[scur], %[lk]\n\t" \
  "v_add_u32 v121, %[snext], %[lk]\n\t" \
  "v_add_u32 v122, %[scur], %[lv]\n\t" \
  "v_add_u32 v123, %[snext], %[lv]\n\t" \
  "s_waitcnt lgkmcnt(1)\n\t" \
  "v_mfma_f32_32x32x16_bf16 v[84:99], v[100:103], v[32:35], v[36:51]\n\t" \
  "ds_read_b128 v[100:103], v120 offset:2048\n\t" \
  "v_mfma_f32_32x32x16_bf16 v[52:67], v[108:111], v[116:119], v[52:67]\n\t" \
  "s_mov_b64 exec, %[vmask]\n\t" \
  "ds_read_b128 v[108:111], v122 offset:512\n\t" \
  "s_mov_b64 exec, -1\n\t" \
  "v_exp_f32 v112, v69\n\t" \
  "v_exp_f32 v68, v68\n\t" \
  "v_exp_f32 v113, v71\n\t" \
  "v_mov_b32_sdwa v112, v68 dst_sel:WORD_0 dst_unused:UNUSED_PRESERVE src0_sel:WORD_1\n\t" \
  "v_exp_f32 v70, v70\n\t" \
  "v_exp_f32 v114, v73\n\t" \
  "v_mov_b32_sdwa v113, v70 dst_sel:WORD_0 dst_unused:UNUSED_PRESERVE src0_sel:WORD_1\n\t" \
  "v_exp_f32 v72, v72\n\t" \
  "v_exp_f32 v115, v75\n\t" \
  "v_mov_b32_sdwa v114, v72 dst_sel:WORD_0 dst_unused:UNUSED_PRESERVE src0_sel:WORD_1\n\t" \
  "v_exp_f32 v74, v74\n\t" \
  "v_exp_f32 v116, v77\n\t" \
  "v_mov_b32_sdwa v115, v74 dst_sel:WORD_0 dst_unused:UNUSED_PRESERVE src0_sel:WORD_1\n\t" \
  "v_exp_f32 v76, v76\n\t" \
  "v_exp_f32 v117, v79\n\t" \
  "s_waitcnt lgkmcnt(2)\n\t" \
  "v_mfma_f32_32x32x16_bf16 v[52:67], v[104:107], v[112:115], v[52:67]\n\t" \
  "s_mov_b64 exec, %[vmask]\n\t" \
  "ds_read_b128 v[104:107], v122 offset:1024\n\t" \
  "s_mov_b64 exec, -1\n\t" \
  "v_mov_b32_sdwa v116, v76 dst_sel:WORD_0 dst_unused:UNUSED_PRESERVE src0_sel:WORD_1\n\t" \
  "v_exp_f32 v78, v78\n\t" \
  "v_exp_f32 v118, v81\n\t" \
  "v_mov_b32_sdwa v117, v78 dst_sel:WORD_0 dst_unused:UNUSED_PRESERVE src0_sel:WORD_1\n\t" \
  "v_exp_f32 v80, v80\n\t" \
  "v_exp_f32 v119, v83\n\t" \
  "v_mov_b32_sdwa v118, v80 dst_sel:WORD_0 dst_unused:UNUSED_PRESERVE src0_sel:WORD_1\n\t" \
  "v_exp_f32 v82, v82\n\t" \
  "s_nop 0\n\t" \
  "v_mov_b32_sdwa v119, v82 dst_sel:WORD_0 dst_unused:UNUSED_PRESERVE src0_sel:WORD_1\n\t" \
  "s_waitcnt lgkmcnt(1)\n\t" \
  "v_mfma_f32_32x32x16_bf16 v[68:83], v[100:103], v[32:35], v[36:51]\n\t" \
  "ds_read_b128 v[100:103], v120 offset:3072\n\t" \
  "v_mfma_f32_32x32x16_bf16 v[52:67], v[108:111], v[116:119], v[52:67]\n\t" \
  "s_mov_b64 exec, %[vmask]\n\t" \
  "ds_read_b128 v[108:111], v122 offset:1536\n\t" \
  "s_mov_b64 exec, -1\n\t" \
  "v_exp_f32 v112, v85\n\t" \
  "v_exp_f32 v84, v84\n\t" \
  "v_exp_f32 v113, v87\n\t" \
  "v_mov_b32_sdwa v112, v84 dst_sel:WORD_0 dst_unused:UNUSED_PRESERVE src0_sel:WORD_1\n\t" \
  "v_exp_f32 v86, v86\n\t" \
  "v_exp_f32 v114, v89\n\t" \
  "v_mov_b32_sdwa v113, v86 dst_sel:WORD_0 dst_unused:UNUSED_PRESERVE src0_sel:WORD_1\n\t" \
  "v_exp_f32 v88, v88\n\t" \
  "v_exp_f32 v115, v91\n\t" \
  "v_mov_b32_sdwa v114, v88 dst_sel:WORD_0 dst_unused:UNUSED_PRESERVE src0_sel:WORD_1\n\t" \
  "v_exp_f32 v90, v90\n\t" \
  "v_exp_f32 v116, v93\n\t" \
  "v_mov_b32_sdwa v115, v90 dst_sel:WORD_0 dst_unused:UNUSED_PRESERVE src0_sel:WORD_1\n\t" \
  "v_exp_f32 v92, v92\n\t" \
  "v_exp_f32 v117, v95\n\t" \
  "s_waitcnt lgkmcnt(2)\n\t" \
  "v_mfma_f32_32x32x16_bf16 v[52:67], v[104:107], v[112:115], v[52:67]\n\t" \
  "s_mov_b64 exec, %[vmask]\n\t" \
  "ds_read_b128 v[104:107], v122 offset:2048\n\t" \
  "s_mov_b64 exec, -1\n\t" \
  "v_mov_b32_sdwa v116, v92 dst_sel:WORD_0 dst_unused:UNUSED_PRESERVE src0_sel:WORD_1\n\t" \
  "v_exp_f32 v94, v94\n\t" \
  "v_exp_f32 v118, v97\n\t" \
  "v_mov_b32_sdwa v117, v94 dst_sel:WORD_0 dst_unused:UNUSED_PRESERVE src0_sel:WORD_1\n\t" \
  "v_exp_f32 v96, v96\n\t" \
  "v_exp_f32 v119, v99\n\t" \
  "v_mov_b32_sdwa v118, v96 dst_sel:WORD_0 dst_unused:UNUSED_PRESERVE src0_sel:WORD_1\n\t" \
  "v_exp_f32 v98, v98\n\t" \
  "s_nop 0\n\t" \
  "v_mov_b32_sdwa v119, v98 dst_sel:WORD_0 dst_unused:UNUSED_PRESERVE src0_sel:WORD_1\n\t" \
  "s_waitcnt lgkmcnt(1)\n\t" \
  "v_mfma_f32_32x32x16_bf16 v[84:99], v[100:103], v[32:35], v[36:51]\n\t" \
  "ds_read_b128 v[100:103], v120 offset:4096\n\t" \
  "v_mfma_f32_32x32x16_bf16 v[52:67], v[108:111], v[116:119], v[52:67]\n\t" \
  "s_mov_b64 exec, %[vmask]\n\t" \
  "ds_read_b128 v[108:111], v122 offset:2560\n\t" \
  "s_mov_b64 exec, -1\n\t" \
  "v_exp_f32 v112, v69\n\t" \
  "v_exp_f32 v68, v68\n\t" \
  "v_exp_f32 v113, v71\n\t" \
  "v_mov_b32_sdwa v112, v68 dst_sel:WORD_0 dst_unused:UNUSED_PRESERVE src0_sel:WORD_1\n\t" \
  "v_exp_f32 v70, v70\n\t" \
  "v_exp_f32 v114, v73\n\t" \
  "v_mov_b32_sdwa v113, v70 dst_sel:WORD_0 dst_unused:UNUSED_PRESERVE src0_sel:WORD_1\n\t" \
  "v_exp_f32 v72, v72\n\t" \
  "v_exp_f32 v115, v75\n\t" \
  "v_mov_b32_sdwa v114, v72 dst_sel:WORD_0 dst_unused:UNUSED_PRESERVE src0_sel:WORD_1\n\t" \
  "v_exp_f32 v74, v74\n\t" \
  "v_exp_f32 v116, v77\n\t" \
  "v_mov_b32_sdwa v115, v74 dst_sel:WORD_0 dst_unused:UNUSED_PRESERVE src0_sel:WORD_1\n\t" \
  "v_exp_f32 v76, v76\n\t" \
  "v_exp_f32 v117, v79\n\t" \
  "s_waitcnt lgkmcnt(2)\n\t" \
  "v_mfma_f32_32x32x16_bf16 v[52:67], v[104:107], v[112:115], v[52:67]\n\t" \
  "s_mov_b64 exec, %[vmask]\n\t" \
  "ds_read_b128 v[104:107], v122 offset:3072\n\t" \
  "s_mov_b64 exec, -1\n\t" \
  "v_mov_b32_sdwa v116, v76 dst_sel:WORD_0 dst_unused:UNUSED_PRESERVE src0_sel:WORD_1\n\t" \
  "v_exp_f32 v78, v78\n\t" \
  "v_exp_f32 v118, v81\n\t" \
  "v_mov_b32_sdwa v117, v78 dst_sel:WORD_0 dst_unused:UNUSED_PRESERVE src0_sel:WORD_1\n\t" \
  "v_exp_f32 v80, v80\n\t" \
  "v_exp_f32 v119, v83\n\t" \
  "v_mov_b32_sdwa v118, v80 dst_sel:WORD_0 dst_unused:UNUSED_PRESERVE src0_sel:WORD_1\n\t" \
  "v_exp_f32 v82, v82\n\t" \
  "s_nop 0\n\t" \
  "v_mov_b32_sdwa v119, v82 dst_sel:WORD_0 dst_unused:UNUSED_PRESERVE src0_sel:WORD_1\n\t" \
  "s_waitcnt lgkmcnt(1)\n\t" \
  "v_mfma_f32_32x32x16_bf16 v[68:83], v[100:103], v[32:35], v[36:51]\n\t" \
  "ds_read_b128 v[100:103], v120 offset:5120\n\t" \
  "v_mfma_f32_32x32x16_bf16 v[52:67], v[108:111], v[116:119], v[52:67]\n\t" \
  "s_mov_b64 exec, %[vmask]\n\t" \
  "ds_read_b128 v[108:111], v122 offset:3584\n\t" \
  "s_mov_b64 exec, -1\n\t" \
  "v_exp_f32 v112, v85\n\t" \
  "v_exp_f32 v84, v84\n\t" \
  "v_exp_f32 v113, v87\n\t" \
  "v_mov_b32_sdwa v112, v84 dst_sel:WORD_0 dst_unused:UNUSED_PRESERVE src0_sel:WORD_1\n\t" \
  "v_exp_f32 v86, v86\n\t" \
  "v_exp_f32 v114, v89\n\t" \
  "v_mov_b32_sdwa v113, v86 dst_sel:WORD_0 dst_unused:UNUSED_PRESERVE src0_sel:WORD_1\n\t" \
  "v_exp_f32 v88, v88\n\t" \
  "v_exp_f32 v115, v91\n\t" \
  "v_mov_b32_sdwa v114, v88 dst_sel:WORD_0 dst_unused:UNUSED_PRESERVE src0_sel:WORD_1\n\t" \
  "v_exp_f32 v90, v90\n\t" \
  "v_exp_f32 v116, v93\n\t" \
  "v_mov_b32_sdwa v115, v90 dst_sel:WORD_0 dst_unused:UNUSED_PRESERVE src0_sel:WORD_1\n\t" \
  "v_exp_f32 v92, v92\n\t" \
  "v_exp_f32 v117, v95\n\t" \
  "s_waitcnt lgkmcnt(2)\n\t" \
  "v_mfma_f32_32x32x16_bf16 v[52:67], v[104:107], v[112:115], v[52:67]\n\t" \
  "s_mov_b64 exec, %[vmask]\n\t" \
  "ds_read_b128 v[104:107], v122 offset:4096\n\t" \
  "s_mov_b64 exec, -1\n\t" \
  "v_mov_b32_sdwa v116, v92 dst_sel:WORD_0 dst_unused:UNUSED_PRESERVE src0_sel:WORD_1\n\t" \
  "v_exp_f32 v94, v94\n\t" \
  "v_exp_f32 v118, v97\n\t" \
  "v_mov_b32_sdwa v117, v94 dst_sel:WORD_0 dst_unused:UNUSED_PRESERVE src0_sel:WORD_1\n\t" \
  "v_exp_f32 v96, v96\n\t" \
  "v_exp_f32 v119, v99\n\t" \
  "v_mov_b32_sdwa v118, v96 dst_sel:WORD_0 dst_unused:UNUSED_PRESERVE src0_sel:WORD_1\n\t" \
  "v_exp_f32 v98, v98\n\t" \
  "s_nop 0\n\t" \
  "v_mov_b32_sdwa v119, v98 dst_sel:WORD_0 dst_unused:UNUSED_PRESERVE src0_sel:WORD_1\n\t" \
  "s_waitcnt lgkmcnt(1)\n\t" \
  "v_mfma_f32_32x32x16_bf16 v[84:99], v[100:103], v[32:35], v[36:51]\n\t" \
  "ds_read_b128 v[100:103], v120 offset:6144\n\t" \
  "v_mfma_f32_32x32x16_bf16 v[52:67], v[108:111], v[116:119], v[52:67]\n\t" \
  "s_mov_b64 exec, %[vmask]\n\t" \
  "ds_read_b128 v[108:111], v122 offset:4608\n\t" \
  "s_mov_b64 exec, -1\n\t" \
  "v_exp_f32 v112, v69\n\t" \
  "v_exp_f32 v68, v68\n\t" \
  "v_exp_f32 v113, v71\n\t" \
  "v_mov_b32_sdwa v112, v68 dst_sel:WORD_0 dst_unused:UNUSED_PRESERVE src0_sel:WORD_1\n\t" \
  "v_exp_f32 v70, v70\n\t" \
  "v_exp_f32 v114, v73\n\t" \
  "v_mov_b32_sdwa v113, v70 dst_sel:WORD_0 dst_unused:UNUSED_PRESERVE src0_sel:WORD_1\n\t" \
  "v_exp_f32 v72, v72\n\t" \
  "v_exp_f32 v115, v75\n\t" \
  "v_mov_b32_sdwa v114, v72 dst_sel:WORD_0 dst_unused:UNUSED_PRESERVE src0_sel:WORD_1\n\t" \
  "v_exp_f32 v74, v74\n\t" \
  "v_exp_f32 v116, v77\n\t" \
  "v_mov_b32_sdwa v115, v74 dst_sel:WORD_0 dst_unused:UNUSED_PRESERVE src0_sel:WORD_1\n\t" \
  "v_exp_f32 v76, v76\n\t" \
  "v_exp_f32 v117, v79\n\t" \
  "s_waitcnt lgkmcnt(2)\n\t" \
  "v_mfma_f32_32x32x16_bf16 v[52:67], v[104:107], v[112:115], v[52:67]\n\t" \
  "s_mov_b64 exec, %[vmask]\n\t" \
  "ds_read_b128 v[104:107], v122 offset:5120\n\t" \
  "s_mov_b64 exec, -1\n\t" \
  "v_mov_b32_sdwa v116, v76 dst_sel:WORD_0 dst_unused:UNUSED_PRESERVE src0_sel:WORD_1\n\t" \
  "v_exp_f32 v78, v78\n\t" \
  "v_exp_f32 v118, v81\n\t" \
  "v_mov_b32_sdwa v117, v78 dst_sel:WORD_0 dst_unused:UNUSED_PRESERVE src0_sel:WORD_1\n\t" \
  "v_exp_f32 v80, v80\n\t" \
  "v_exp_f32 v119, v83\n\t" \
  "v_mov_b32_sdwa v118, v80 dst_sel:WORD_0 dst_unused:UNUSED_PRESERVE src0_sel:WORD_1\n\t" \
  "v_exp_f32 v82, v82\n\t" \
  "s_waitcnt vmcnt(0)\n\t" \
  "s_barrier\n\t" \
  "s_add_u32 m0, %[sfree], %[m0k]\n\t" \
  "s_nop 0\n\t" \
  "buffer_load_dwordx4 %[goff], %[rk], %[ssrc] offen lds\n\t" \
  "s_add_u32 m0, m0, 0x2000\n\t" \
  "s_nop 0\n\t" \
  "buffer_load_dwordx4 %[goff], %[rv], %[ssrc] offen lds\n\t" \
  "s_add_u32 %[ssrc], %[ssrc], 0x2000\n\t" \
  "v_mov_b32_sdwa v119, v82 dst_sel:WORD_0 dst_unused:UNUSED_PRESERVE src0_sel:WORD_1\n\t" \
  "s_waitcnt lgkmcnt(1)\n\t" \
  "v_mfma_f32_32x32x16_bf16 v[68:83], v[100:103], v[32:35], v[36:51]\n\t" \
  "ds_read_b128 v[100:103], v120 offset:7168\n\t" \
  "v_mfma_f32_32x32x16_bf16 v[52:67], v[108:111], v[116:119], v[52:67]\n\t" \
  "s_mov_b64 exec, %[vmask]\n\t" \
  "ds_read_b128 v[108:111], v122 offset:5632\n\t" \
  "s_mov_b64 exec, -1\n\t" \
  "v_exp_f32 v112, v85\n\t" \
  "v_exp_f32 v84, v84\n\t" \
  "v_exp_f32 v113, v87\n\t" \
  "v_mov_b32_sdwa v112, v84 dst_sel:WORD_0 dst_unused:UNUSED_PRESERVE src0_sel:WORD_1\n\t" \
  "v_exp_f32 v86, v86\n\t" \
  "v_exp_f32 v114, v89\n\t" \
  "v_mov_b32_sdwa v113, v86 dst_sel:WORD_0 dst_unused:UNUSED_PRESERVE src0_sel:WORD_1\n\t" \
  "v_exp_f32 v88, v88\n\t" \
  "v_exp_f32 v115, v91\n\t" \
  "v_mov_b32_sdwa v114, v88 dst_sel:WORD_0 dst_unused:UNUSED_PRESERVE src0_sel:WORD_1\n\t" \
  "v_exp_f32 v90, v90\n\t" \
  "v_exp_f32 v116, v93\n\t" \
  "v_mov_b32_sdwa v115, v90 dst_sel:WORD_0 dst_unused:UNUSED_PRESERVE src0_sel:WORD_1\n\t" \
  "v_exp_f32 v92, v92\n\t" \
  "v_exp_f32 v117, v95\n\t" \
  "s_waitcnt lgkmcnt(2)\n\t" \
  "v_mfma_f32_32x32x16_bf16 v[52:67], v[104:107], v[112:115], v[52:67]\n\t" \
  "s_mov_b64 exec, %[vmask]\n\t" \
  "ds_read_b128 v[104:107], v122 offset:6144\n\t" \
  "s_mov_b64 exec, -1\n\t" \
  "v_mov_b32_sdwa v116, v92 dst_sel:WORD_0 dst_unused:UNUSED_PRESERVE src0_sel:WORD_1\n\t" \
  "v_exp_f32 v94, v94\n\t" \
  "v_exp_f32 v118, v97\n\t" \
  "v_mov_b32_sdwa v117, v94 dst_sel:WORD_0 dst_unused:UNUSED_PRESERVE src0_sel:WORD_1\n\t" \
  "v_exp_f32 v96, v96\n\t" \
  "v_exp_f32 v119, v99\n\t" \
  "v_mov_b32_sdwa v118, v96 dst_sel:WORD_0 dst_unused:UNUSED_PRESERVE src0_sel:WORD_1\n\t" \
  "v_exp_f32 v98, v98\n\t" \
  "s_nop 0\n\t" \
  "v_mov_b32_sdwa v119, v98 dst_sel:WORD_0 dst_unused:UNUSED_PRESERVE src0_sel:WORD_1\n\t" \
  "s_waitcnt lgkmcnt(1)\n\t" \
  "v_mfma_f32_32x32x16_bf16 v[84:99], v[100:103], v[32:35], v[36:51]\n\t" \
  "ds_read_b128 v[100:103], v121 offset:0\n\t" \
  "v_mfma_f32_32x32x16_bf16 v[52:67], v[108:111], v[116:119], v[52:67]\n\t" \
  "s_mov_b64 exec, %[vmask]\n\t" \
  "ds_read_b128 v[108:111], v122 offset:6656\n\t" \
  "s_mov_b64 exec, -1\n\t" \
  "v_exp_f32 v112, v69\n\t" \
  "v_exp_f32 v68, v68\n\t" \
  "v_exp_f32 v113, v71\n\t" \
  "v_mov_b32_sdwa v112, v68 dst_sel:WORD_0 dst_unused:UNUSED_PRESERVE src0_sel:WORD_1\n\t" \
  "v_exp_f32 v70, v70\n\t" \
  "v_exp_f32 v114, v73\n\t" \
  "v_mov_b32_sdwa v113, v70 dst_sel:WORD_0 dst_unused:UNUSED_PRESERVE src0_sel:WORD_1\n\t" \
  "v_exp_f32 v72, v72\n\t" \
  "v_exp_f32 v115, v75\n\t" \
  "v_mov_b32_sdwa v114, v72 dst_sel:WORD_0 dst_unused:UNUSED_PRESERVE src0_sel:WORD_1\n\t" \
  "v_exp_f32 v74, v74\n\t" \
  "v_exp_f32 v116, v77\n\t" \
  "v_mov_b32_sdwa v115, v74 dst_sel:WORD_0 dst_unused:UNUSED_PRESERVE src0_sel:WORD_1\n\t" \
  "v_exp_f32 v76, v76\n\t" \
  "v_exp_f32 v117, v79\n\t" \
  "s_waitcnt lgkmcnt(2)\n\t" \
  "v_mfma_f32_32x32x16_bf16 v[52:67], v[104:107], v[112:115], v[52:67]\n\t" \
  "s_mov_b64 exec, %[vmask]\n\t" \
  "ds_read_b128 v[104:107], v122 offset:7168\n\t" \
  "s_mov_b64 exec, -1\n\t" \
  "v_mov_b32_sdwa v116, v76 dst_sel:WORD_0 dst_unused:UNUSED_PRESERVE src0_sel:WORD_1\n\t" \
  "v_exp_f32 v78, v78\n\t" \
  "v_exp_f32 v118, v81\n\t" \
  "v_mov_b32_sdwa v117, v78 dst_sel:WORD_0 dst_unused:UNUSED_PRESERVE src0_sel:WORD_1\n\t" \
  "v_exp_f32 v80, v80\n\t" \
  "v_exp_f32 v119, v83\n\t" \
  "v_mov_b32_sdwa v118, v80 dst_sel:WORD_0 dst_unused:UNUSED_PRESERVE src0_sel:WORD_1\n\t" \
  "v_exp_f32 v82, v82\n\t" \
  "s_nop 0\n\t" \
  "v_mov_b32_sdwa v119, v82 dst_sel:WORD_0 dst_unused:UNUSED_PRESERVE src0_sel:WORD_1\n\t" \
  "s_waitcnt lgkmcnt(1)\n\t" \
  "v_mfma_f32_32x32x16_bf16 v[68:83], v[100:103], v[32:35], v[36:51]\n\t" \
  "ds_read_b128 v[100:103], v121 offset:1024\n\t" \
  "v_mfma_f32_32x32x16_bf16 v[52:67], v[108:111], v[116:119], v[52:67]\n\t" \
  "s_mov_b64 exec, %[vmask]\n\t" \
  "ds_read_b128 v[108:111], v122 offset:7680\n\t" \
  "s_mov_b64 exec, -1\n\t" \
  "v_exp_f32 v112, v85\n\t" \
  "v_exp_f32 v84, v84\n\t" \
  "v_exp_f32 v113, v87\n\t" \
  "v_mov_b32_sdwa v112, v84 dst_sel:WORD_0 dst_unused:UNUSED_PRESERVE src0_sel:WORD_1\n\t" \
  "v_exp_f32 v86, v86\n\t" \
  "v_exp_f32 v114, v89\n\t" \
  "v_mov_b32_sdwa v113, v86 dst_sel:WORD_0 dst_unused:UNUSED_PRESERVE src0_sel:WORD_1\n\t" \
  "v_exp_f32 v88, v88\n\t" \
  "v_exp_f32 v115, v91\n\t" \
  "v_mov_b32_sdwa v114, v88 dst_sel:WORD_0 dst_unused:UNUSED_PRESERVE src0_sel:WORD_1\n\t" \
  "v_exp_f32 v90, v90\n\t" \
  "v_exp_f32 v116, v93\n\t" \
  "v_mov_b32_sdwa v115, v90 dst_sel:WORD_0 dst_unused:UNUSED_PRESERVE src0_sel:WORD_1\n\t" \
  "v_exp_f32 v92, v92\n\t" \
  "v_exp_f32 v117, v95\n\t" \
  "s_waitcnt lgkmcnt(2)\n\t" \
  "v_mfma_f32_32x32x16_bf16 v[52:67], v[104:107], v[112:115], v[52:67]\n\t" \
  "s_mov_b64 exec, %[vmask]\n\t" \
  "ds_read_b128 v[104:107], v123 offset:0\n\t" \
  "s_mov_b64 exec, -1\n\t" \
  "v_mov_b32_sdwa v116, v92 dst_sel:WORD_0 dst_unused:UNUSED_PRESERVE src0_sel:WORD_1\n\t" \
  "v_exp_f32 v94, v94\n\t" \
  "v_exp_f32 v118, v97\n\t" \
  "v_mov_b32_sdwa v117, v94 dst_sel:WORD_0 dst_unused:UNUSED_PRESERVE src0_sel:WORD_1\n\t" \
  "v_exp_f32 v96, v96\n\t" \
  "v_exp_f32 v119, v99\n\t" \
  "v_mov_b32_sdwa v118, v96 dst_sel:WORD_0 dst_unused:UNUSED_PRESERVE src0_sel:WORD_1\n\t" \
  "v_exp_f32 v98, v98\n\t" \
  "s_nop 0\n\t" \
  "v_mov_b32_sdwa v119, v98 dst_sel:WORD_0 dst_unused:UNUSED_PRESERVE src0_sel:WORD_1\n\t" \
  "s_mov_b32 %[stmp], %[scur]\n\t" \
  "s_mov_b32 %[scur], %[snext]\n\t" \
  "s_mov_b32 %[snext], %[sfree]\n\t" \
  "s_mov_b32 %[sfree], %[stmp]\n\t" \
  "s_sub_u32 %[cnt], %[cnt], 1\n\t" \
  "s_cmp_lg_u32 %[cnt], 0\n\t" \
  "s_cbranch_scc1 1b\n\t" \
  "s_waitcnt lgkmcnt(0)\n\t" \
  "v_mfma_f32_32x32x16_bf16 v[52:67], v[108:111], v[116:119], v[52:67]\n\t" \
  "s_waitcnt vmcnt(0)\n\t" \
  "s_nop 15\n\t"
#define ATTN_CLOBBERS "v36", "v37", "v38", "v39", "v40", "v41", "v42", "v43", "v44", "v45", "v46", "v47", "v48", "v49", "v50", "v51", "v68", "v69", "v70", "v71", "v72", "v73", "v74", "v75", "v76", "v77", "v78", "v79", "v80", "v81", "v82", "v83", "v84", "v85", "v86", "v87", "v88", "v89", "v90", "v91", "v92", "v93", "v94", "v95", "v96", "v97", "v98", "v99", "v100", "v101", "v102", "v103", "v104", "v105", "v106", "v107", "v108", "v109", "v110", "v111", "v112", "v113", "v114", "v115", "v116", "v117", "v118", "v119", "v120", "v121", "v122", "v123", "m0", "scc", "memory"

constexpr int AT_SLOT = 16384;
DEV void attn_role(char* lds, int a, char* ws) {
#if defined(__HIP_DEVICE_COMPILE__)
  STAMP_DECL
  RSTAMP(0); STAMP(1);
  const int head = (a & 7) * 4 + (a >> 6), qb = (a >> 3) & 7;
  const int tid = threadIdx.x, lane = tid & 63, l31 = lane & 31, h = lane >> 5;
  const int wid = __builtin_amdgcn_readfirstlane(tid >> 6);
  auto krs = mk_rsrc(ws + OFF_K + (size_t)head * (S_ * 32), S_ * 32);
  auto vrs = mk_rsrc(ws + OFF_V + (size_t)head * (S_ * 32), S_ * 32);
  const int q = qb * 256 + wid * 32 + l31;
  bf16x8 qf = *(const bf16x8*)(ws + OFF_Q + (size_t)head * (S_ * 32) + q * 32 + h * 16);
  auto issue = [&](int c, int slot) {
    char* sb = lds + slot * AT_SLOT + wid * 1024;
    dma16(krs, sb, lane * 16, c * 8192 + wid * 1024);
    dma16(vrs, sb + 8192, lane * 16, c * 8192 + wid * 1024);
  };
  const unsigned ldsb = (unsigned)(size_t)lds;
  const unsigned lk = ldsb + lane * 16;
  const unsigned lv = ldsb + 8192 + h * 256 + (l31 & 15) * 16;
  const unsigned goff = lane * 16;
  const unsigned m0k = ldsb + wid * 1024;
  int* flag = (int*)(lds + 3 * AT_SLOT);
  float mref = 0.f;
  f32x16 o;
  for (int attempt = 0; attempt < 2; ++attempt) {
    if (attempt == 1) {
      float mx = -__builtin_inff();
      for (int c = 0; c < 8; ++c) {
        issue(c, 0);
        WAIT_V(0); RAW_BARRIER();
        for (int t = 0; t < 8; ++t) {
          bf16x8 kf = *(const bf16x8*)(lds + t * 1024 + lane * 16);
          f32x16 z = {};
          f32x16 s = mfma32(kf, qf, z);
#pragma unroll
          for (int r = 0; r < 16; ++r) mx = fmaxf(mx, s[r]);
        }
        RAW_BARRIER();
      }
      mx = fmaxf(mx, __shfl_xor(mx, 32));
      mref = mx;
    }
    STAMP(2);
    issue(0, 0);
    issue(1, 1);
    STAMP(3);
    WAIT_V(2); RAW_BARRIER();
    STAMP(4);
    if (attempt == 0) {
      bf16x8 kf = *(const bf16x8*)(lds + lane * 16);
      f32x16 z = {};
      f32x16 s = mfma32(kf, qf, z);
      float mx = s[0];
#pragma unroll
      for (int r = 1; r < 16; ++r) mx = fmaxf(mx, s[r]);
      mx = fmaxf(mx, __shfl_xor(mx, 32));
      mref = mx;
    }
    STAMP(5);
    {
      float negm = -mref;
      unsigned scur, snext, sfree, stmp, cnt, ssrc = 2 * 8192 + wid * 1024;
      unsigned long long vmask = 0x0000FFFF0000FFFFull;
      asm volatile(ATTN_ASM
                   : "={v[52:67]}"(o), [scur] "=&s"(scur), [snext] "=&s"(snext), [sfree] "=&s"(sfree), [stmp] "=&s"(stmp),
                     [cnt] "=&s"(cnt), [ssrc] "+s"(ssrc)
                   : "{v[32:35]}"(qf), [negm] "v"(negm), [lk] "v"(lk), [lv] "v"(lv), [goff] "v"(goff), [rk] "s"(krs), [rv] "s"(vrs),
                     [m0k] "s"(m0k), [vmask] "s"(vmask)
                   : ATTN_CLOBBERS);
    }
    STAMP(40);
    bool bad = !(o[8] < 1.0e30f);
#pragma unroll
    for (int r = 0; r < 8; ++r) bad |= !(fabsf(o[r]) < 3.0e38f);
    if (tid == 0) *flag = 0;
    RAW_BARRIER();
    if (attempt == 0 && __any(bad)) { if (lane == 0) *flag = 1; }
    RAW_BARRIER();
    int f = *flag;
    RAW_BARRIER();
    if (f == 0) break;
  }
  float inv = 1.0f / o[8];
  u32x4 ov = widen(pk4(o[0] * inv, o[1] * inv, o[2] * inv, o[3] * inv), pk4(o[4] * inv, o[5] * inv, o[6] * inv, o[7] * inv));
  {
    const unsigned l2 = fresh_lane();
    auto ors = mk_rsrc(ws + OFF_O, 64 * S_ * 16);
    __builtin_amdgcn_raw_buffer_store_b128(ov, ors, ((l2 >> 5) * S_ + (l2 & 31)) * 16, (2 * head * S_ + qb * 256 + wid * 32) * 16, 0);
  }
  STAMP(41); RSTAMP(42); STAMP_FLUSH(0);
#endif
}

namespace bsched {
constexpr int NG = 46;
constexpr int pos(int g) {
  if (g < 8) return 8 * g;
  if (g < 24) return 12 * ((g - 8) % 6);
  int u = (g - 24) % 10;
  return u == 0 ? 72 : u == 1 ? 48 : u == 2 ? 56 : u == 3 ? 64 : 8 * (u - 4);
}
constexpr int npw(int g) { return g < 8 ? 2 : g < 24 ? 3 : g == 45 ? 1 : 2; }
constexpr int iss(int g) {
  if (g < 8) return -1;
  if (g < 24) { int t = g - 8; return t == 0 ? 3 : t == 1 ? 4 : t == 2 ? 6 : t == 3 ? 7 : t == 4 ? 8 : t == 5 ? 9 : t + 6; }
  int u = g - 24;
  return u == 0 ? 9 : u == 1 ? 22 : u == 2 ? 23 : u == 3 ? 23 : u == 4 ? 24 : u == 5 ? 25 : u == 6 ? 25 : u <= 9 ? 26 : u + 19;
}
constexpr int pub(int g) { return g < 8 ? g : g < 24 ? g + 2 : g + 3; }
constexpr int pubbed(int bi) { return bi < 8 ? bi : (bi >= 10 && bi <= 25) ? bi - 2 : (bi >= 27 && bi <= 48) ? bi - 3 : -1; }
constexpr bool before(int a, int b) { return iss(a) != iss(b) ? iss(a) < iss(b) : a < b; }
constexpr int waitcnt(int g) {
  int n = 0;
  for (int o = 0; o < NG; ++o) if (o != g && before(g, o) && iss(o) < pub(g)) n += npw(o);
  return n;
}
}

DEV u32x4 ld_sc1(__amdgpu_buffer_rsrc_t r, unsigned voff, unsigned soff) { return __builtin_amdgcn_raw_buffer_load_b128(r, voff, soff, 16); }
DEV void st_sc1(__amdgpu_buffer_rsrc_t r, u32x4 v, unsigned voff, unsigned soff) { __builtin_amdgcn_raw_buffer_store_b128(v, r, voff, soff, 16); }

DEV void b_signal_wait(unsigned* cnt, int lane) {
  if (lane == 0) {
    __hip_atomic_fetch_add(cnt, 1u, __ATOMIC_RELAXED, __HIP_MEMORY_SCOPE_AGENT);
    for (int it = 0; it < (1 << 22); ++it) {
      if (__hip_atomic_load(cnt, __ATOMIC_RELAXED, __HIP_MEMORY_SCOPE_AGENT) >= 8u) break;
      __builtin_amdgcn_s_sleep(2);
    }
  }
}

DEV void b_loader(char* lds, int lw, int lane, int grp, int j, int c0, const u16* w2, const u16* w3, const u16* w4, char* ws) {
#if defined(__HIP_DEVICE_COMPILE__)
  using namespace bsched;
  auto r2 = mk_rsrc(w2, D_ * D_ * 2);
  auto r3 = mk_rsrc(w3, D_ * F_ * 2);
  auto r4 = mk_rsrc(w4, F_ * D_ * 2);
  unsigned va[2], vb[3];
#pragma unroll
  for (int i = 0; i < 2; ++i) {
    unsigned bb = (lw + 4 * i) * 1024 + lane * 16, row = bb >> 7, cp = (bb >> 4) & 7;
    va[i] = row * 1024 + j * 128 + ((cp ^ (((row >> 1) & 1) << 2)) << 4);
  }
#pragma unroll
  for (int i = 0; i < 3; ++i) {
    unsigned bb = (lw + 4 * i) * 1024 + lane * 16, row = bb / 384, cp = (bb % 384) >> 4;
    vb[i] = row * (F_ * 2) + c0 * 2 + ((cp ^ (((row >> 1) & 1) << 2)) << 4);
  }
  auto issue = [&](auto G) {
    constexpr int g = G;
    char* sb = lds + pos(g) * 1024 + lw * 1024;
    if constexpr (g < 8) { dma16(r2, sb, va[0], g * 65536); dma16(r2, sb + 4096, va[1], g * 65536); }
    else if constexpr (g < 24) {
#pragma unroll
      for (int i = 0; i < 3; ++i) dma16(r3, sb + i * 4096, vb[i], (g - 8) * 32 * (F_ * 2));
    } else {
      dma16(r4, sb, va[0], (g - 24) * 65536);
      if constexpr (g != 45) dma16(r4, sb + 4096, va[1], (g - 24) * 65536);
    }
  };
  static_for<NG>([&](auto G) { if constexpr (iss(G) == -1) issue(G); });
  static_for<50>([&](auto BI) {
    constexpr int bi = BI;
    constexpr int pg = pubbed(bi);
    if constexpr (bi == 10 || bi == 27) {
      if (lw == 0) b_signal_wait((unsigned*)(ws + OFF_CNT) + (bi == 10 ? grp : 32 + grp) * 64, lane);
    }
    if constexpr (pg >= 0) WAIT_V(waitcnt(pg));
    BARRIER_NOWAIT();
    static_for<NG>([&](auto G) { if constexpr (iss(G) == bi) issue(G); });
  });
#endif
}

DEV void b_compute(char* lds, int wid, int lane, int grp, int j, int c0, int ntile, const u16* x, char* ws) {
#if defined(__HIP_DEVICE_COMPILE__)
  using namespace bsched;
  STAMP_DECL
  RSTAMP(0); STAMP(1);
  const int wm = wid & 1, kp = wid >> 1;
  const int m0 = grp * 64;
  auto hrs = mk_rsrc(ws + OFF_HB, 64 * HB_ROWS * 16);
  auto x1rs = mk_rsrc(ws + OFF_X1, 64 * S_ * 16);
  auto h2rs = mk_rsrc(ws + OFF_H2, 172 * S_ * 16);
  auto xrs = mk_rsrc(x, S_ * D_ * 2);
  const unsigned col0 = j * 64 + kp * 32;
  unsigned l31, h, blk, q, p, row, sw, cpart, wo13[2];
  auto lane_init = [&]() {
    lane = fresh_lane();
    l31 = lane & 31; h = lane >> 5; blk = (lane >> 4) & 1; q = (lane & 15) >> 2; p = lane & 3;
    row = m0 + wm * 32 + l31; sw = (q >> 1) & 1; cpart = 8 * (p & 1);
#pragma unroll
    for (int a = 0; a < 2; ++a) wo13[a] = (16 * kp + 8 * h + q) * 128 + (((4 * (a ^ kp) + 2 * blk + (p >> 1)) ^ (sw << 2)) << 4) + cpart;
  };
  lane_init();
  u32x2 xv[4];
#pragma unroll
  for (int i = 0; i < 4; ++i) xv[i] = __builtin_amdgcn_raw_buffer_load_b64(xrs, (row * D_ + col0 + 4 * h) * 2, i * 16, 0);
  const f32x2 st0 = *(const f32x2*)(ws + OFF_ST0 + (size_t)row * 8);

  auto phase13 = [&](auto G0_, auto NI_, auto QD_, auto loadA, f32x16 (&acc)[2], auto&& first_issue_done) {
    constexpr int G0 = G0_, NI = NI_, QD = QD_;
    u32x4 aq[QD];
    static_for<QD>([&](auto I) { if constexpr (I < NI) aq[I] = loadA(I); });
    first_issue_done();
    bf16x8 wf[2][2];
    auto rd = [&](auto I) {
      constexpr int i = I;
      const char* sb = lds + pos(G0 + (i >> 1)) * 1024 + (i & 1) * 4096;
      wf[i & 1][0] = tr_frag(sb + wo13[0], sb + wo13[0] + 512);
      wf[i & 1][1] = tr_frag(sb + wo13[1], sb + wo13[1] + 512);
    };
    auto mm = [&](auto I) {
      constexpr int i = I;
      bf16x8 a = __builtin_bit_cast(bf16x8, aq[i % QD]);
      acc[0] = mfma32(wf[i & 1][0], a, acc[0]);
      acc[1] = mfma32(wf[i & 1][1], a, acc[1]);
      if constexpr (i + QD < NI) aq[i % QD] = loadA(IC<i + QD>{});
    };
    static_for<NI>([&](auto I) {
      constexpr int i = I;
      if constexpr ((i & 1) == 0) { BARRIER_NOWAIT(); STAMP(2 + (G0 + (i >> 1))); }
      rd(I);
      if constexpr (i > 0) mm(IC<i - 1>{});
      SCHED_FENCE();
    });
    mm(IC<NI - 1>{});
  };
  auto exchange = [&](f32x16 (&acc)[2], int xpos) {
    char* xb = lds + xpos * 1024;
    char* mine = xb + (wm * 2 + (kp ^ 1)) * 4096 + lane * 16;
    const char* theirs = xb + (wm * 2 + kp) * 4096 + lane * 16;
#pragma unroll
    for (int i = 0; i < 4; ++i) *(f32x4*)(mine + i * 1024) = f32x4{acc[1][4 * i], acc[1][4 * i + 1], acc[1][4 * i + 2], acc[1][4 * i + 3]};
    RAW_BARRIER();
#pragma unroll
    for (int i = 0; i < 4; ++i) {
      f32x4 t = *(const f32x4*)(theirs + i * 1024);
#pragma unroll
      for (int c = 0; c < 4; ++c) acc[0][4 * i + c] += t[c];
    }
  };

  {
    f32x16 acc[2] = {};
    const unsigned va = (h * HB_ROWS + row) * 16 + kp * (2 * HB_ROWS * 16);
    auto loadA = [&](auto I) {
      constexpr int i = I;
      constexpr int kk0 = 4 * (i >> 1) + 2 * (i & 1), g = i >> 2;
      constexpr int d = g == 0 ? -1 : g == 1 ? 1 : g == 2 ? -2 : 2;
      return __builtin_amdgcn_raw_buffer_load_b128(hrs, va, kk0 * (2 * HB_ROWS * 16) + (2 + d) * 16, 0);
    };
    phase13(IC<0>{}, IC<16>{}, IC<10>{}, loadA, acc, [] {});
    exchange(acc, 64);
    STAMP(50);
    float v[16], a1 = 0.f, a2 = 0.f;
#pragma unroll
    for (int i = 0; i < 4; ++i) {
      v[4 * i + 0] = (bf_lo(xv[i][0]) - st0[0]) * st0[1] + acc[0][4 * i + 0];
      v[4 * i + 1] = (bf_hi(xv[i][0]) - st0[0]) * st0[1] + acc[0][4 * i + 1];
      v[4 * i + 2] = (bf_lo(xv[i][1]) - st0[0]) * st0[1] + acc[0][4 * i + 2];
      v[4 * i + 3] = (bf_hi(xv[i][1]) - st0[0]) * st0[1] + acc[0][4 * i + 3];
    }
#pragma unroll
    for (int i = 0; i < 16; ++i) { a1 += v[i]; a2 += v[i] * v[i]; }
    a1 = xhalf_sum(a1); a2 = xhalf_sum(a2);
#pragma unroll
    for (int g = 0; g < 2; ++g) st_sc1(x1rs, pack8(v + 8 * g), ((col0 / 8 + 2 * g + h) * S_ + row) * 16, 0);
    if (lane < 32) {
      auto srs = mk_rsrc(ws + OFF_ST1, 16 * S_ * 8);
      u32x2 sv = {__float_as_uint(a1), __float_as_uint(a2)};
      __builtin_amdgcn_raw_buffer_store_b64(sv, srs, ((col0 >> 5) * S_ + row) * 8, 0, 16);
    }
    WAIT_V(0);
    BARRIER_NOWAIT();
    STAMP(51);
  }
  {
    const int wn = kp;
    lane_init();
    float cs[3];
    {
      const float* cp = (const float*)(ws + OFF_CS3) + h * 4 * CS3_LD + c0 + wn * 96 + l31;
#pragma unroll
      for (int a = 0; a < 3; ++a) {
        float t = 0.f;
#pragma unroll
        for (int i = 0; i < 4; ++i) t += cp[i * CS3_LD + a * 32];
        cs[a] = xhalf_sum(t);
      }
    }
    unsigned wo2[3];
#pragma unroll
    for (int a = 0; a < 3; ++a) wo2[a] = (8 * h + q) * 384 + (((4 * (3 * wn + a) + 2 * blk + (p >> 1)) ^ (sw << 2)) << 4) + cpart;
    f32x16 acc[3] = {};
    constexpr int QD = 8;
    u32x4 aq[QD];
    const unsigned va = (h * S_ + row) * 16;
    BARRIER_NOWAIT();
    STAMP(52);
    u32x2 sp[8];
    {
      auto srs = mk_rsrc(ws + OFF_ST1, 16 * S_ * 8);
#pragma unroll
      for (int i = 0; i < 8; ++i) sp[i] = __builtin_amdgcn_raw_buffer_load_b64(srs, ((h * 8 + i) * S_ + row) * 8, 0, 16);
    }
    static_for<QD>([&](auto I) { aq[I] = ld_sc1(x1rs, va, I * 65536); });
    bf16x8 wf[2][3];
    auto rd = [&](auto I) {
      constexpr int i = I;
      const char* sb = lds + pos(8 + (i >> 1)) * 1024 + (i & 1) * (16 * 384);
#pragma unroll
      for (int a = 0; a < 3; ++a) wf[i & 1][a] = tr_frag(sb + wo2[a], sb + wo2[a] + 4 * 384);
    };
    auto mm = [&](auto I) {
      constexpr int i = I;
      bf16x8 a = __builtin_bit_cast(bf16x8, aq[i % QD]);
#pragma unroll
      for (int t = 0; t < 3; ++t) acc[t] = mfma32(wf[i & 1][t], a, acc[t]);
      if constexpr (i + QD < 32) aq[i % QD] = ld_sc1(x1rs, va, (i + QD) * 65536);
    };
    float mu, rs;
    {
      float a1 = 0.f, a2 = 0.f;
#pragma unroll
      for (int t = 0; t < 8; ++t) { a1 += __uint_as_float(sp[t][0]); a2 += __uint_as_float(sp[t][1]); }
      a1 = xhalf_sum(a1); a2 = xhalf_sum(a2);
      mu = a1 * (1.0f / D_);
      rs = __builtin_amdgcn_rsqf(fmaxf(a2 * (1.0f / D_) - mu * mu, 0.f) + LN_EPS);
      SCHED_FENCE();
    }
    static_for<32>([&](auto I) {
      constexpr int i = I;
      if constexpr ((i & 1) == 0 && i > 0) { BARRIER_NOWAIT(); STAMP(2 + 8 + (i >> 1)); }
      rd(I);
      if constexpr (i > 0) mm(IC<i - 1>{});
      SCHED_FENCE();
    });
    mm(IC<31>{});
    {
      unsigned mh = pk_bf16(-mu, -mu);
      float mlo = -mu - bf_lo(mh);
      u32x4 xe = {h ? 0u : mh, h ? 0u : (pk_bf16(mlo, 0.f)), 0u, 0u};
#pragma unroll
      for (int t = 0; t < 3; ++t) {
        unsigned ch = pk_bf16(cs[t], cs[t]);
        float clo = cs[t] - bf_lo(ch);
        u32x4 we = {h ? 0u : ((ch & 0xffffu) | (pk_bf16(clo, clo) << 16)), h ? 0u : (ch & 0xffffu), 0u, 0u};
        acc[t] = mfma32(__builtin_bit_cast(bf16x8, we), __builtin_bit_cast(bf16x8, xe), acc[t]);
      }
    }
    STAMP(53);
#pragma unroll
    for (int t = 0; t < 3; ++t) {
      if (3 * wn + t < ntile) {
        float v[16];
#pragma unroll
        for (int i = 0; i < 16; ++i) v[i] = gelu_tanh(acc[t][i] * rs);
        const unsigned kt8 = (c0 + (3 * wn + t) * 32) >> 3;
#pragma unroll
        for (int g = 0; g < 2; ++g) st_sc1(h2rs, pack8(v + 8 * g), ((kt8 + 2 * g + h) * S_ + row) * 16, 0);
      }
    }
    WAIT_V(0);
    BARRIER_NOWAIT();
    STAMP(54);
  }
  {
    lane_init();
    f32x16 acc[2] = {};
    const unsigned va = (h * S_ + row) * 16 + kp * 65536;
    auto loadA = [&](auto I) {
      constexpr int i = I;
      constexpr int kk0 = 4 * (i >> 1) + 2 * (i & 1);
      return ld_sc1(h2rs, va, kk0 * 65536);
    };
    BARRIER_NOWAIT();
    STAMP(55);
    constexpr int NI = 43, QD = 10;
    u32x4 aq[QD];
    static_for<QD>([&](auto I) { aq[I] = loadA(I); });
    bf16x8 wf[2][2];
    u32x2 xr[4], x1r[4];
    auto rd = [&](auto I) {
      constexpr int i = I;
      const char* sb = lds + pos(24 + (i >> 1)) * 1024 + (i & 1) * 4096;
      wf[i & 1][0] = tr_frag(sb + wo13[0], sb + wo13[0] + 512);
      wf[i & 1][1] = tr_frag(sb + wo13[1], sb + wo13[1] + 512);
    };
    auto mm = [&](auto I) {
      constexpr int i = I;
      bf16x8 a = __builtin_bit_cast(bf16x8, aq[i % QD]);
      acc[0] = mfma32(wf[i & 1][0], a, acc[0]);
      acc[1] = mfma32(wf[i & 1][1], a, acc[1]);
      if constexpr (i + QD < NI) aq[i % QD] = loadA(IC<i + QD>{});
      if constexpr (i + QD == NI) {
#pragma unroll
        for (int t = 0; t < 4; ++t) {
          xr[t] = __builtin_amdgcn_raw_buffer_load_b64(xrs, (row * D_ + col0 + 4 * h) * 2, t * 16, 0);
          x1r[t] = __builtin_amdgcn_raw_buffer_load_b64(x1rs, ((col0 / 8 + t) * S_ + row) * 16 + 8 * h, 0, 16);
        }
      }
    };
    static_for<NI>([&](auto I) {
      constexpr int i = I;
      if constexpr ((i & 1) == 0 && i > 0) { BARRIER_NOWAIT(); STAMP(2 + 24 + (i >> 1)); }
      rd(I);
      if constexpr (i > 0) mm(IC<i - 1>{});
      SCHED_FENCE();
    });
    mm(IC<NI - 1>{});
    exchange(acc, 32);
    STAMP(56);
    char* rb = ws + OFF_R + ((size_t)((m0 >> 5) + wm) * 16 + (col0 >> 5)) * 4096 + fresh_lane() * 16;
#pragma unroll
    for (int i = 0; i < 4; ++i) {
      f32x4 o;
      o[0] = acc[0][4 * i + 0] + bf_lo(xr[i][0]) + bf_lo(x1r[i][0]);
      o[1] = acc[0][4 * i + 1] + bf_hi(xr[i][0]) + bf_hi(x1r[i][0]);
      o[2] = acc[0][4 * i + 2] + bf_lo(xr[i][1]) + bf_lo(x1r[i][1]);
      o[3] = acc[0][4 * i + 3] + bf_hi(xr[i][1]) + bf_hi(x1r[i][1]);
      *(f32x4*)(rb + i * 1024) = o;
    }
  }
  STAMP(60); RSTAMP(61); STAMP_FLUSH(0);
#endif
}

constexpr int K2_LDS = 81920;
__global__ __launch_bounds__(512, 4) void k2(const u16* __restrict__ x, const u16* __restrict__ w2, const u16* __restrict__ w3,
                                             const u16* __restrict__ w4, char* __restrict__ ws) {
#if defined(__HIP_DEVICE_COMPILE__)
  extern __shared__ __attribute__((aligned(16))) char lds[];
  const int b = blockIdx.x;
  if (b >= 256) { attn_role(lds, b - 256, ws); return; }
  const int lane = threadIdx.x & 63;
  const int wid = __builtin_amdgcn_readfirstlane(threadIdx.x >> 6);
  const int grp = (b & 7) * 4 + (b >> 6), j = (b >> 3) & 7;
  const int c0 = j < 3 ? j * 192 : 576 + (j - 3) * 160;
  const int ntile = j < 3 ? 6 : 5;
  __builtin_amdgcn_s_setprio(2);
  if (wid >= 4) b_loader(lds, wid - 4, lane, grp, j, c0, w2, w3, w4, ws);
  else b_compute(lds, wid, lane, grp, j, c0, ntile, x, ws);
#endif
}

constexpr int K3_LDS = 131072;
__global__ __launch_bounds__(512, 2) void k3(const u16* __restrict__ wo, const char* __restrict__ ws, u16* __restrict__ out) {
#if defined(__HIP_DEVICE_COMPILE__)
  extern __shared__ __attribute__((aligned(16))) char lds[];
  STAMP_DECL
  RSTAMP(0); STAMP(1);
  const int b = blockIdx.x, xcd = b & 7, bi = b >> 3;
  const int mt = xcd * 4 + (bi >> 3), nt = bi & 7;
  const int m0 = mt * 64;
  const int tid = threadIdx.x, lane = tid & 63;
  const int wid = __builtin_amdgcn_readfirstlane(tid >> 6);
  auto ors = mk_rsrc(ws + OFF_O, 64 * S_ * 16);
  auto wrs = mk_rsrc(wo, D_ * D_ * 2);
  const unsigned va = (m0 + lane) * 16;
  unsigned vw;
  { unsigned row = lane >> 3, cp = lane & 7; vw = row * 1024 + nt * 128 + ((cp ^ (((row >> 1) & 1) << 2)) << 4); }
  const int rot = (nt & 3);
#pragma unroll
  for (int c = 0; c < 4; ++c) {
    const int cc = (c + rot) & 3;
#pragma unroll
    for (int i = 0; i < 2; ++i) {
      const int pc = 2 * wid + i;
      dma16(ors, lds + (c * 16 + pc) * 1024, va, (cc * 16 + pc) * (S_ * 16));
      dma16(wrs, lds + 65536 + (c * 16 + pc) * 1024, vw, (cc * 16 + pc) * 8192);
    }
  }
  const unsigned l31 = lane & 31, h = lane >> 5, blk = (lane >> 4) & 1, q = (lane & 15) >> 2, p = lane & 3;
  const int wm = wid & 1, wn = (wid >> 1) & 1;
  f32x4 r[4];
  if (wid < 4) {
    const char* rb = ws + OFF_R + ((size_t)((m0 >> 5) + wm) * 16 + nt * 2 + wn) * 4096 + lane * 16;
#pragma unroll
    for (int i = 0; i < 4; ++i) r[i] = *(const f32x4*)(rb + i * 1024);
  }
  const unsigned ao = h * 1024 + (wm * 32 + l31) * 16;
  const unsigned wo_ = 65536 + (8 * h + q) * 128 + (((4 * wn + 2 * blk + (p >> 1)) ^ (((q >> 1) & 1) << 2)) << 4) + 8 * (p & 1);
  f32x16 acc = {};
  static_for<4>([&](auto C) {
    constexpr int c = C;
    if (wid < 4) WAIT_V((3 - c) * 4 + 4); else WAIT_V((3 - c) * 4);
    BARRIER_NOWAIT();
    STAMP(4 + c);
    if (wid < 4) {
      bf16x8 xf[8], wf[8];
#pragma unroll
      for (int s = 0; s < 8; ++s) {
        xf[s] = *(const bf16x8*)(lds + c * 16384 + s * 2048 + ao);
        wf[s] = tr_frag(lds + c * 16384 + s * 2048 + wo_, lds + c * 16384 + s * 2048 + wo_ + 512);
      }
#pragma unroll
      for (int s = 0; s < 8; ++s) acc = mfma32(wf[s], xf[s], acc);
    }
  });
  STAMP(40);
  if (wid < 4) {
    float v[16];
#pragma unroll
    for (int i = 0; i < 4; ++i)
#pragma unroll
      for (int c = 0; c < 4; ++c) v[4 * i + c] = acc[4 * i + c] + r[i][c];
    u16* dst = out + (size_t)(m0 + wm * 32 + l31) * D_ + nt * 64 + wn * 32 + h * 8;
#pragma unroll
    for (int g = 0; g < 2; ++g) *(u32x4*)(dst + g * 16) = pack8(v + 8 * g);
  }
  STAMP(41); RSTAMP(42); STAMP_FLUSH(0);
#endif
}

#define SEL(i) true
extern "C" void kernel_launch(void* const* d_in, const int* in_sizes, int n_in, void* d_out, int out_size,
                              void* d_ws, size_t ws_size, hipStream_t stream) {
  const u16* x = (const u16*)d_in[0];
  const u16* wqkv = (const u16*)d_in[1];
  const u16* wo = (const u16*)d_in[2];
  const u16* w1 = (const u16*)d_in[4];
  const u16* w2 = (const u16*)d_in[5];
  const u16* w3 = (const u16*)d_in[6];
  const u16* w4 = (const u16*)d_in[7];
  char* ws = (char*)d_ws;
  (void)hipFuncSetAttribute((const void*)k1, hipFuncAttributeMaxDynamicSharedMemorySize, K1_LDS);
  (void)hipFuncSetAttribute((const void*)k2, hipFuncAttributeMaxDynamicSharedMemorySize, K2_LDS);
  (void)hipFuncSetAttribute((const void*)k3, hipFuncAttributeMaxDynamicSharedMemorySize, K3_LDS);
  if (SEL(0)) k1<<<256, 768, K1_LDS, stream>>>(x, wqkv, w1, w3, w2, w4, wo, ws);
  if (SEL(1)) k2<<<512, 512, K2_LDS, stream>>>(x, w2, w3, w4, ws);
  if (SEL(2)) k3<<<256, 512, K3_LDS, stream>>>(wo, ws, (u16*)d_out);
}
```

```cpp
#include <hip/hip_runtime.h>
#include <stdint.h>

typedef __attribute__((ext_vector_type(8))) __bf16 bf16x8;
typedef __attribute__((ext_vector_type(2))) __bf16 bf16x2;
typedef __attribute__((ext_vector_type(4))) short s16x4;
typedef __attribute__((ext_vector_type(16))) float f32x16;
typedef __attribute__((ext_vector_type(4))) float f32x4;
typedef __attribute__((ext_vector_type(2))) float f32x2;
typedef __attribute__((ext_vector_type(4))) unsigned u32x4;
typedef __attribute__((ext_vector_type(2))) unsigned u32x2;
typedef __attribute__((address_space(3))) void* ldsptr_t;
typedef unsigned short u16;

#define DEV __device__ __forceinline__

constexpr int S_ = 2048, D_ = 512, H_ = 32, F_ = 1376;
constexpr float LN_EPS = 1e-5f;
constexpr float QSCALE = 0.25f * 1.4426950408889634f;

constexpr size_t MB_ = 1 << 20, KB_ = 1 << 10;
constexpr size_t OFF_Q = 0, OFF_K = 2 * MB_, OFF_V = 4 * MB_, OFF_O = 6 * MB_, OFF_X1 = 8 * MB_, OFF_HB = 10 * MB_;
constexpr size_t OFF_ST0 = 12 * MB_ + 64 * KB_, OFF_CNT = 12 * MB_ + 96 * KB_, OFF_CS3 = 12 * MB_ + 128 * KB_;
constexpr size_t OFF_ST1 = 12 * MB_ + 256 * KB_, OFF_H2 = 13 * MB_, OFF_R = 19 * MB_;
constexpr int HB_ROWS = S_ + 4;
constexpr int CS3_P = 8, CS3_LD = 1408;

template <int N> struct IC { static constexpr int value = N; constexpr operator int() const { return N; } };
template <int I, int N, class F> DEV void static_for_impl(F&& f) {
  if constexpr (I < N) { f(IC<I>{}); static_for_impl<I + 1, N>(f); }
}
template <int N, class F> DEV void static_for(F&& f) { static_for_impl<0, N>(f); }

DEV unsigned pk_bf16(float lo, float hi) {
  f32x2 v = {lo, hi};
  return __builtin_bit_cast(unsigned, __builtin_convertvector(v, bf16x2));
}
DEV float bf_lo(unsigned u) { return __uint_as_float(u << 16); }
DEV float bf_hi(unsigned u) { return __uint_as_float(u & 0xffff0000u); }
DEV float gelu_tanh(float x) {
  const float c1 = -2.0f * 0.7978845608028654f * 1.4426950408889634f;
  const float c2 = c1 * 0.044715f;
  float t = x * (c1 + c2 * x * x);
  float e = __builtin_amdgcn_exp2f(t);
  return x * __builtin_amdgcn_rcpf(1.0f + e);
}
DEV float dot2(unsigned a, unsigned b, float c) {
  return __builtin_amdgcn_fdot2_f32_bf16(__builtin_bit_cast(bf16x2, a), __builtin_bit_cast(bf16x2, b), c, false);
}
DEV float xhalf_sum(float v) {
  auto r = __builtin_amdgcn_permlane32_swap(__float_as_uint(v), __float_as_uint(v), false, false);
  return __uint_as_float(r[0]) + __uint_as_float(r[1]);
}
#define STAMP(i)
#define RSTAMP(i)
#define STAMP_DECL
#define STAMP_ARG
#define STAMP_PASS
#define STAMP_FLUSH(w)
#define WAIT_V(n) asm volatile("s_waitcnt vmcnt(%0)" ::"n"(n) : "memory")
#define WAIT_L(n) asm volatile("s_waitcnt lgkmcnt(%0)" ::"n"(n) : "memory")
#define SCHED_FENCE() __builtin_amdgcn_sched_barrier(0)
#define RAW_BARRIER() do { WAIT_L(0); __builtin_amdgcn_s_barrier(); } while (0)
#define BARRIER_NOWAIT() do { SCHED_FENCE(); asm volatile("s_barrier" ::: "memory"); SCHED_FENCE(); } while (0)

DEV unsigned fresh_lane() {
  unsigned l;
  asm volatile("v_mbcnt_lo_u32_b32 %0, -1, 0\n\tv_mbcnt_hi_u32_b32 %0, -1, %0" : "=v"(l));
  return l;
}
DEV __amdgpu_buffer_rsrc_t mk_rsrc(const void* p, unsigned bytes) {
  return __builtin_amdgcn_make_buffer_rsrc((void*)p, 0, (int)bytes, 0x00020000);
}
DEV void dma16(__amdgpu_buffer_rsrc_t r, char* lds_dst, unsigned voff, unsigned soff) {
  __builtin_amdgcn_raw_ptr_buffer_load_lds(r, (ldsptr_t)lds_dst, 16, voff, soff, 0, 0);
}
DEV bf16x8 tr_frag(const char* p0, const char* p1) {
  s16x4 a = __builtin_amdgcn_ds_read_tr16_b64_v4i16((__attribute__((address_space(3))) s16x4*)p0);
  s16x4 b = __builtin_amdgcn_ds_read_tr16_b64_v4i16((__attribute__((address_space(3))) s16x4*)p1);
  typedef __attribute__((ext_vector_type(8))) short s16x8;
  s16x8 r = __builtin_shufflevector(a, b, 0, 1, 2, 3, 4, 5, 6, 7);
  return __builtin_bit_cast(bf16x8, r);
}
DEV f32x16 mfma32(bf16x8 a, bf16x8 b, f32x16 c) { return __builtin_amdgcn_mfma_f32_32x32x16_bf16(a, b, c, 0, 0, 0); }
DEV u32x2 pk4(float a, float b, float c, float d) { u32x2 r; r[0] = pk_bf16(a, b); r[1] = pk_bf16(c, d); return r; }
DEV u32x4 widen(u32x2 X, u32x2 Y) {
  u32x4 o;
  auto a = __builtin_amdgcn_permlane32_swap(X[0], Y[0], false, false);
  auto b = __builtin_amdgcn_permlane32_swap(X[1], Y[1], false, false);
  o[0] = a[0]; o[1] = b[0]; o[2] = a[1]; o[3] = b[1];
  return o;
}
DEV u32x4 pack8(const float* v) { return widen(pk4(v[0], v[1], v[2], v[3]), pk4(v[4], v[5], v[6], v[7])); }

constexpr int K1_A = 16384, K1_ST = 32768;
constexpr int K1_X = 4 * K1_ST;
constexpr int K1_P3 = K1_X + 2048 + 1024 + 1536;
constexpr int K1_LDS = K1_P3 + 2048 + 16;

template <int KIND>
DEV void k1_epi_s(const f32x16& a, const float* fin, int sub, int J, int m0, int rl0, int lane, char* ws) {
  const int l31 = lane & 31, h = lane >> 5;
  const int rl = rl0 + l31, s = m0 + rl;
  float mu = fin[rl], rs = fin[128 + rl];
  if (KIND == 0) rs *= QSCALE;
  if (KIND == 2) {
    float r2 = rs * rs;
    rs *= __builtin_amdgcn_rsqf((1.0f - LN_EPS * r2) + LN_EPS);
  }
  float v[16];
#pragma unroll
  for (int j = 0; j < 4; ++j) {
    f32x4 c = *(const f32x4*)(fin + 256 + sub * 32 + 8 * j + 4 * h);
#pragma unroll
    for (int i = 0; i < 4; ++i) v[4 * j + i] = (a[4 * j + i] - mu * c[i]) * rs;
  }
  if (KIND == 2) {
#pragma unroll
    for (int i = 0; i < 16; ++i) v[i] = gelu_tanh(v[i]);
  }
#pragma unroll
  for (int g = 0; g < 2; ++g) {
    u32x4 o = pack8(v + 8 * g);
    char* dst;
    if (KIND == 0) dst = ws + OFF_Q + (size_t)(2 * J + g) * (S_ * 32) + (unsigned)(s * 32 + h * 16);
    else if (KIND == 1) dst = ws + OFF_K + (size_t)(2 * J + g) * (S_ * 32) + (unsigned)((s >> 5) * 1024 + h * 512 + (s & 31) * 16);
    else dst = ws + OFF_HB + (size_t)(((4 * J + 2 * g + h) * HB_ROWS + s + 2) * 16);
    *(u32x4*)dst = o;
  }
}
DEV void k1_epi_v(const f32x16& a, const float* fin, int sub, int J, int m0, int rl0, int lane, char* ws) {
  const int l31 = lane & 31, h = lane >> 5;
  const float cs = fin[256 + sub * 32 + l31];
  float v[16];
#pragma unroll
  for (int j = 0; j < 4; ++j) {
    f32x4 mu = *(const f32x4*)(fin + rl0 + 8 * j + 4 * h), rs = *(const f32x4*)(fin + 128 + rl0 + 8 * j + 4 * h);
#pragma unroll
    for (int i = 0; i < 4; ++i) v[4 * j + i] = (a[4 * j + i] - mu[i] * cs) * rs[i];
  }
  const int n = J * 32 + l31, head = n >> 4, d = n & 15, tile = (m0 + rl0) >> 5;
  char* base = ws + OFF_V + (size_t)head * (S_ * 32) + tile * 1024;
#pragma unroll
  for (int s = 0; s < 2; ++s) {
    u32x4 o;
#pragma unroll
    for (int j = 0; j < 4; ++j) o[j] = pk_bf16(v[8 * s + 2 * j], v[8 * s + 2 * j + 1]);
    *(u32x4*)(base + (s * 2 + h) * 256 + d * 16) = o;
  }
}

template <int TYPE>
DEV void k1_compute(char* lds, int wm, int wn, int nt, int m0, char* ws STAMP_ARG) {
  const unsigned lane = fresh_lane();
  const unsigned l31 = lane & 31, h = lane >> 5, blk = (lane >> 4) & 1, q = (lane & 15) >> 2, p = lane & 3;
  const unsigned mask = wm ^ wn;
  const unsigned row = wm * 64 + l31;
  unsigned ao[4], wb[2][2];
#pragma unroll
  for (int j = 0; j < 4; ++j) ao[j] = row * 128 + (((2 * (j ^ mask) + h) ^ ((row >> 1) & 7)) << 4);
  const unsigned cpart = (2 * blk + (p >> 1)) * 16 + 8 * (p & 1);
#pragma unroll
  for (int a = 0; a < 2; ++a) {
    const unsigned w = K1_A + (8 * h + q) * 256 + (((wn + 2 * a) ^ q) << 6) + cpart;
    wb[0][a] = w + mask * 4096; wb[1][a] = w - mask * 4096;
  }
  f32x16 acc[2][2] = {};
  float s1[2][2] = {}, s2[2][2] = {}, cs[2][2] = {};
  bf16x8 xf[4][2], wf[4][2];
  auto rd = [&](int t, int j) {
    const char* sb = lds + (t & 3) * K1_ST;
    xf[j][0] = *(const bf16x8*)(sb + ao[j]);
    wf[j][0] = tr_frag(sb + wb[j & 1][0] + j * 4096, sb + wb[j & 1][0] + j * 4096 + 1024);
    wf[j][1] = tr_frag(sb + wb[j & 1][1] + j * 4096, sb + wb[j & 1][1] + j * 4096 + 1024);
    xf[j][1] = *(const bf16x8*)(sb + ao[j] + 4096);
  };
  auto mm = [&](int j) {
#pragma unroll
    for (int r = 0; r < 2; ++r)
#pragma unroll
      for (int a = 0; a < 2; ++a)
        acc[r][a] = TYPE == 2 ? mfma32(xf[j][r], wf[j][a], acc[r][a]) : mfma32(wf[j][a], xf[j][r], acc[r][a]);
    __builtin_amdgcn_sched_group_barrier(0x008, 1, 0);
    __builtin_amdgcn_sched_group_barrier(0x100, 3, 0);
    __builtin_amdgcn_sched_group_barrier(0x008, 1, 0);
    __builtin_amdgcn_sched_group_barrier(0x100, 3, 0);
    __builtin_amdgcn_sched_group_barrier(0x008, 2, 0);
  };
  auto dots = [&](int j) {
#pragma unroll
    for (int i = 0; i < 4; ++i) {
#pragma unroll
      for (int r = 0; r < 2; ++r) {
        unsigned u = __builtin_bit_cast(u32x4, xf[j][r])[i];
        s1[r][i & 1] = dot2(u, 0x3f803f80u, s1[r][i & 1]); s2[r][i & 1] = dot2(u, u, s2[r][i & 1]);
      }
#pragma unroll
      for (int a = 0; a < 2; ++a) cs[a][i & 1] = dot2(__builtin_bit_cast(u32x4, wf[j + 1][a])[i], 0x3f803f80u, cs[a][i & 1]);
    }
    asm volatile("" : "+v"(s1[0][0]), "+v"(s1[0][1]), "+v"(s1[1][0]), "+v"(s1[1][1]), "+v"(s2[0][0]), "+v"(s2[0][1]), "+v"(s2[1][0]), "+v"(s2[1][1]),
                      "+v"(cs[0][0]), "+v"(cs[0][1]), "+v"(cs[1][0]), "+v"(cs[1][1]));
  };
#pragma unroll
  for (int t = 0; t < 8; ++t) {
    WAIT_L(0);
    BARRIER_NOWAIT();
    STAMP(4 + t);
    rd(t, 0);
    if (t > 0) { mm(3); SCHED_FENCE(); dots(2); }
    SCHED_FENCE();
    rd(t, 1);
    mm(0);
    SCHED_FENCE();
    rd(t, 2);
    mm(1);
    SCHED_FENCE();
    dots(0);
    SCHED_FENCE();
    rd(t, 3);
    mm(2);
    SCHED_FENCE();
  }
  mm(3);
  SCHED_FENCE();
  dots(2);
  STAMP(40);
  float* rsb = (float*)(lds + K1_X);
  float* csb = rsb + 512;
  float* fin = csb + 256;
#pragma unroll
  for (int r = 0; r < 2; ++r) {
    float a = xhalf_sum(s1[r][0] + s1[r][1]), b = xhalf_sum(s2[r][0] + s2[r][1]), c = xhalf_sum(cs[r][0] + cs[r][1]);
    if (h == 0) *(f32x2*)(rsb + (wn * 128 + wm * 64 + r * 32 + l31) * 2) = f32x2{a, b};
    else csb[wm * 128 + (wn + 2 * r) * 32 + l31] = c;
  }
  RAW_BARRIER();
  {
    const int i = wm * 64 + lane;
    f32x2 a = *(const f32x2*)(rsb + i * 2), c = *(const f32x2*)(rsb + (128 + i) * 2);
    float mu = (a[0] + c[0]) * (1.0f / D_);
    float var = fmaxf((a[1] + c[1]) * (1.0f / D_) - mu * mu, 0.f);
    float rs = __builtin_amdgcn_rsqf(var + LN_EPS);
    float cc = csb[i] + csb[128 + i];
    fin[wn * 256 + i] = wn ? cc : mu;
    if (wn == 0) {
      fin[128 + i] = rs;
      if (nt == 0) *(f32x2*)(ws + OFF_ST0 + (size_t)(m0 + i) * 8) = f32x2{mu, rs};
    }
  }
  RAW_BARRIER();
  const int c = nt >> 1;
#pragma unroll
  for (int r = 0; r < 2; ++r) {
    const int rl0 = wm * 64 + r * 32;
    if (TYPE == 0) {
      k1_epi_s<0>(acc[r][0], fin, wn, 2 * c + wn, m0, rl0, lane, ws);
      k1_epi_s<2>(acc[r][1], fin, wn + 2, 2 * c + wn, m0, rl0, lane, ws);
    } else if (TYPE == 1) {
      k1_epi_s<1>(acc[r][0], fin, wn, 4 * c + wn, m0, rl0, lane, ws);
      k1_epi_s<1>(acc[r][1], fin, wn + 2, 4 * c + wn + 2, m0, rl0, lane, ws);
    } else {
      k1_epi_v(acc[r][0], fin, wn, 4 * (c - 4) + wn, m0, rl0, lane, ws);
      k1_epi_v(acc[r][1], fin, wn + 2, 4 * (c - 4) + wn + 2, m0, rl0, lane, ws);
    }
  }
  STAMP(41); RSTAMP(42); STAMP_FLUSH(4);
}

__global__ __launch_bounds__(512, 2) void k1(const u16* __restrict__ x, const u16* __restrict__ wqkv, const u16* __restrict__ w1,
                                             const u16* __restrict__ w3, const u16* __restrict__ w2, const u16* __restrict__ w4,
                                             const u16* __restrict__ wo, char* __restrict__ ws) {
#if defined(__HIP_DEVICE_COMPILE__)
  extern __shared__ __attribute__((aligned(16))) char lds[];
  STAMP_DECL
  RSTAMP(0); STAMP(1);
  const int b = blockIdx.x, xcd = b & 7, bi = b >> 3;
  const int mt = (xcd & 3) * 4 + (bi & 3), nt = (xcd >> 2) * 8 + (bi >> 2);
  const int m0 = mt * 128;
  const int tid = threadIdx.x, lane = tid & 63;
  const int wid = __builtin_amdgcn_readfirstlane(tid >> 6);
  if (__builtin_expect(wid < 4, 1)) {
    const int lw = wid;
    auto xrs = mk_rsrc(x, S_ * D_ * 2);
    unsigned va[4];
#pragma unroll
    for (int i = 0; i < 4; ++i) {
      unsigned bb = (lw + 4 * i) * 1024 + lane * 16, row = bb >> 7, cp = (bb >> 4) & 7;
      va[i] = (m0 + row) * 1024 + ((cp ^ ((row >> 1) & 7)) << 4);
    }
    const unsigned kl = lane >> 4, seg = ((lane >> 2) & 3) ^ kl;
    const int c = nt >> 1;
    const bool fromw1 = !(nt & 1) && seg >= 2;
    const unsigned wstride = fromw1 ? 1024u : 3072u;
    const char* wbase = fromw1 ? (const char*)w1 + c * 128 + (seg - 2) * 64
                               : (const char*)wqkv + ((nt & 1) ? 1024 + c * 256 : c * 128) + seg * 64;
    wbase += (lane & 3) * 16 + (lw * 4 + kl) * wstride;
    const int rot = nt + 2 * mt;
    auto issue = [&](int t) {
      char* sb = lds + (t & 3) * K1_ST;
      const unsigned kt = (t + rot) & 7;
#pragma unroll
      for (int i = 0; i < 4; ++i) {
        dma16(xrs, sb + (lw + 4 * i) * 1024, va[i], kt * 128);
        __builtin_amdgcn_global_load_lds((const unsigned*)(wbase + (size_t)((kt * 64 + i * 16) * wstride)),
                                         (__attribute__((address_space(3))) unsigned*)(sb + K1_A + (lw + 4 * i) * 1024), 16, 0, 0);
      }
    };
    STAMP(2);
    issue(0); STAMP(3); issue(1);
    if (tid == 0) *(unsigned*)(lds + K1_P3 + 2048) = 0;
    STAMP(4);
    WAIT_V(8);  STAMP(6); BARRIER_NOWAIT(); issue(2); issue(3);
    WAIT_V(16); STAMP(7); BARRIER_NOWAIT(); issue(4);
    WAIT_V(16); STAMP(8); BARRIER_NOWAIT(); issue(5);
    WAIT_V(16); STAMP(9); BARRIER_NOWAIT(); issue(6);
    WAIT_V(16); STAMP(10); BARRIER_NOWAIT(); issue(7);
    const void* mp; unsigned mbytes, mv, mstride;
    if (b < 88) {
      int cg = b % 11, rg = b / 11;
      mp = w3; mbytes = D_ * F_ * 2; mstride = 4 * F_ * 2;
      mv = (rg * 64 + lw * 16 + (lane >> 4)) * (F_ * 2) + cg * 256 + (lane & 15) * 16;
    } else {
      const int it = (b - 88) * 4 + lw;
      mstride = 8192;
      if (it < 16) { mp = w2; mbytes = D_ * D_ * 2; mv = it * 32768 + lane * 128; }
      else if (it < 32) { mp = wo; mbytes = D_ * D_ * 2; mv = (it - 16) * 32768 + lane * 128; }
      else { mp = w4; mbytes = D_ * F_ * 2; mv = (it - 32) * 32768 + lane * 128; }
    }
    auto mrs = mk_rsrc(mp, mbytes);
    u32x4 md[4];
#pragma unroll
    for (int i = 0; i < 4; ++i) md[i] = __builtin_amdgcn_raw_buffer_load_b128(mrs, mv, i * mstride, 0);
    WAIT_V(20); STAMP(11); BARRIER_NOWAIT();
    WAIT_V(12); STAMP(12); BARRIER_NOWAIT();
    WAIT_V(4);  STAMP(13); BARRIER_NOWAIT();
    if (b < 88) {
      float cs[8];
#pragma unroll
      for (int j = 0; j < 8; ++j) cs[j] = 0.f;
#pragma unroll
      for (int i = 0; i < 4; ++i)
#pragma unroll
        for (int j = 0; j < 4; ++j) { cs[2 * j] += bf_lo(md[i][j]); cs[2 * j + 1] += bf_hi(md[i][j]); }
#pragma unroll
      for (int j = 0; j < 8; ++j) { cs[j] += __shfl_xor(cs[j], 16); cs[j] = xhalf_sum(cs[j]); }
      float* p3 = (float*)(lds + K1_P3);
      if (lane < 16) {
        *(f32x4*)(p3 + lw * 128 + lane * 8) = f32x4{cs[0], cs[1], cs[2], cs[3]};
        *(f32x4*)(p3 + lw * 128 + lane * 8 + 4) = f32x4{cs[4], cs[5], cs[6], cs[7]};
      }
      WAIT_L(0);
      unsigned old = 0;
      if (lane == 0) old = __hip_atomic_fetch_add((unsigned*)(lds + K1_P3 + 2048), 1u, __ATOMIC_RELAXED, __HIP_MEMORY_SCOPE_WORKGROUP);
      old = __builtin_amdgcn_readfirstlane(old);
      if (old == 3) {
        f32x2 t = {0.f, 0.f};
#pragma unroll
        for (int w = 0; w < 4; ++w) t += *(const f32x2*)(p3 + w * 128 + lane * 2);
        *(f32x2*)((float*)(ws + OFF_CS3) + (b / 11) * CS3_LD + (b % 11) * 128 + lane * 2) = t;
      }
    } else {
#pragma unroll
      for (int i = 0; i < 4; ++i) asm volatile("" ::"v"(md[i]));
      if (b == 200 && lw == 0) ((unsigned*)(ws + OFF_CNT))[lane * 64] = 0;
      if (b == 201) {
        u32x4 z = {0, 0, 0, 0};
        *(u32x4*)(ws + OFF_HB + ((size_t)lane * HB_ROWS + (lw < 2 ? lw : S_ + lw)) * 16) = z;
      }
    }
    STAMP(41); RSTAMP(42); STAMP_FLUSH(0);
    return;
  }
  const int wm = wid & 1, wn = (wid >> 1) & 1;
  if (!(nt & 1)) k1_compute<0>(lds, wm, wn, nt, m0, ws STAMP_PASS);
  else if (nt < 8) k1_compute<1>(lds, wm, wn, nt, m0, ws STAMP_PASS);
  else k1_compute<2>(lds, wm, wn, nt, m0, ws STAMP_PASS);
#endif
}
#define ATTN_ASM \
  "v_mov_b32 v36, %[negm]\n\t" \
  "v_mov_b32 v37, %[negm]\n\t" \
  "v_mov_b32 v38, %[negm]\n\t" \
  "v_mov_b32 v39, %[negm]\n\t" \
  "v_mov_b32 v40, %[negm]\n\t" \
  "v_mov_b32 v41, %[negm]\n\t" \
  "v_mov_b32 v42, %[negm]\n\t" \
  "v_mov_b32 v43, %[negm]\n\t" \
  "v_mov_b32 v44, %[negm]\n\t" \
  "v_mov_b32 v45, %[negm]\n\t" \
  "v_mov_b32 v46, %[negm]\n\t" \
  "v_mov_b32 v47, %[negm]\n\t" \
  "v_mov_b32 v48, %[negm]\n\t" \
  "v_mov_b32 v49, %[negm]\n\t" \
  "v_mov_b32 v50, %[negm]\n\t" \
  "v_mov_b32 v51, %[negm]\n\t" \
  "v_mov_b32 v52, 0\n\t" \
  "v_mov_b32 v53, 0\n\t" \
  "v_mov_b32 v54, 0\n\t" \
  "v_mov_b32 v55, 0\n\t" \
  "v_mov_b32 v56, 0\n\t" \
  "v_mov_b32 v57, 0\n\t" \
  "v_mov_b32 v58, 0\n\t" \
  "v_mov_b32 v59, 0\n\t" \
  "v_mov_b32 v60, 0\n\t" \
  "v_mov_b32 v61, 0\n\t" \
  "v_mov_b32 v62, 0\n\t" \
  "v_mov_b32 v63, 0\n\t" \
  "v_mov_b32 v64, 0\n\t" \
  "v_mov_b32 v65, 0\n\t" \
  "v_mov_b32 v66, 0\n\t" \
  "v_mov_b32 v67, 0\n\t" \
  "v_mov_b32 v104, 0x3f803f80\n\t" \
  "v_mov_b32 v108, 0x3f803f80\n\t" \
  "v_mov_b32 v116, 0\n\t" \
  "v_mov_b32 v105, 0x3f803f80\n\t" \
  "v_mov_b32 v109, 0x3f803f80\n\t" \
  "v_mov_b32 v117, 0\n\t" \
  "v_mov_b32 v106, 0x3f803f80\n\t" \
  "v_mov_b32 v110, 0x3f803f80\n\t" \
  "v_mov_b32 v118, 0\n\t" \
  "v_mov_b32 v107, 0x3f803f80\n\t" \
  "v_mov_b32 v111, 0x3f803f80\n\t" \
  "v_mov_b32 v119, 0\n\t" \
  "s_mov_b32 %[scur], 0\n\t" \
  "s_mov_b32 %[snext], 0x4000\n\t" \
  "s_mov_b32 %[sfree], 0x8000\n\t" \
  "s_mov_b32 %[cnt], 8\n\t" \
  "ds_read_b128 v[100:103], %[lk]\n\t" \
  "s_waitcnt lgkmcnt(0)\n\t" \
  "v_mfma_f32_32x32x16_bf16 v[68:83], v[100:103], v[32:35], v[36:51]\n\t" \
  "ds_read_b128 v[100:103], %[lk] offset:1024\n\t" \
  "s_mov_b64 exec, %[vmask]\n\t" \
  "ds_read_b128 v[104:107], %[lv]\n\t" \
  "s_mov_b64 exec, -1\n\t" \
  "s_nop 7\n\t" \
  "1:\n\t" \
  "v_add_u32 v120, %[scur], %[lk]\n\t" \
  "v_add_u32 v121, %[snext], %[lk]\n\t" \
  "v_add_u32 v122, %[scur], %[lv]\n\t" \
  "v_add_u32 v123, %[snext], %[lv]\n\t" \
  "s_waitcnt lgkmcnt(1)\n\t" \
  "v_mfma_f32_32x32x16_bf16 v[84:99], v[100:103], v[32:35], v[36:51]\n\t" \
  "ds_read_b128 v[100:103], v120 offset:2048\n\t" \
  "v_mfma_f32_32x32x16_bf16 v[52:67], v[108:111], v[116:119], v[52:67]\n\t" \
  "s_mov_b64 exec, %[vmask]\n\t" \
  "ds_read_b128 v[108:111], v122 offset:512\n\t" \
  "s_mov_b64 exec, -1\n\t" \
  "v_exp_f32 v112, v69\n\t" \
  "v_exp_f32 v68, v68\n\t" \
  "v_exp_f32 v113, v71\n\t" \
  "v_mov_b32_sdwa v112, v68 dst_sel:WORD_0 dst_unused:UNUSED_PRESERVE src0_sel:WORD_1\n\t" \
  "v_exp_f32 v70, v70\n\t" \
  "v_exp_f32 v114, v73\n\t" \
  "v_mov_b32_sdwa v113, v70 dst_sel:WORD_0 dst_unused:UNUSED_PRESERVE src0_sel:WORD_1\n\t" \
  "v_exp_f32 v72, v72\n\t" \
  "v_exp_f32 v115, v75\n\t" \
  "v_mov_b32_sdwa v114, v72 dst_sel:WORD_0 dst_unused:UNUSED_PRESERVE src0_sel:WORD_1\n\t" \
  "v_exp_f32 v74, v74\n\t" \
  "v_exp_f32 v116, v77\n\t" \
  "v_mov_b32_sdwa v115, v74 dst_sel:WORD_0 dst_unused:UNUSED_PRESERVE src0_sel:WORD_1\n\t" \
  "v_exp_f32 v76, v76\n\t" \
  "v_exp_f32 v117, v79\n\t" \
  "s_waitcnt lgkmcnt(2)\n\t" \
  "v_mfma_f32_32x32x16_bf16 v[52:67], v[104:107], v[112:115], v[52:67]\n\t" \
  "s_mov_b64 exec, %[vmask]\n\t" \
  "ds_read_b128 v[104:107], v122 offset:1024\n\t" \
  "s_mov_b64 exec, -1\n\t" \
  "v_mov_b32_sdwa v116, v76 dst_sel:WORD_0 dst_unused:UNUSED_PRESERVE src0_sel:WORD_1\n\t" \
  "v_exp_f32 v78, v78\n\t" \
  "v_exp_f32 v118, v81\n\t" \
  "v_mov_b32_sdwa v117, v78 dst_sel:WORD_0 dst_unused:UNUSED_PRESERVE src0_sel:WORD_1\n\t" \
  "v_exp_f32 v80, v80\n\t" \
  "v_exp_f32 v119, v83\n\t" \
  "v_mov_b32_sdwa v118, v80 dst_sel:WORD_0 dst_unused:UNUSED_PRESERVE src0_sel:WORD_1\n\t" \
  "v_exp_f32 v82, v82\n\t" \
  "s_nop 0\n\t" \
  "v_mov_b32_sdwa v119, v82 dst_sel:WORD_0 dst_unused:UNUSED_PRESERVE src0_sel:WORD_1\n\t" \
  "s_waitcnt lgkmcnt(1)\n\t" \
  "v_mfma_f32_32x32x16_bf16 v[68:83], v[100:103], v[32:35], v[36:51]\n\t" \
  "ds_read_b128 v[100:103], v120 offset:3072\n\t" \
  "v_mfma_f32_32x32x16_bf16 v[52:67], v[108:111], v[116:119], v[52:67]\n\t" \
  "s_mov_b64 exec, %[vmask]\n\t" \
  "ds_read_b128 v[108:111], v122 offset:1536\n\t" \
  "s_mov_b64 exec, -1\n\t" \
  "v_exp_f32 v112, v85\n\t" \
  "v_exp_f32 v84, v84\n\t" \
  "v_exp_f32 v113, v87\n\t" \
  "v_mov_b32_sdwa v112, v84 dst_sel:WORD_0 dst_unused:UNUSED_PRESERVE src0_sel:WORD_1\n\t" \
  "v_exp_f32 v86, v86\n\t" \
  "v_exp_f32 v114, v89\n\t" \
  "v_mov_b32_sdwa v113, v86 dst_sel:WORD_0 dst_unused:UNUSED_PRESERVE src0_sel:WORD_1\n\t" \
  "v_exp_f32 v88, v88\n\t" \
  "v_exp_f32 v115, v91\n\t" \
  "v_mov_b32_sdwa v114, v88 dst_sel:WORD_0 dst_unused:UNUSED_PRESERVE src0_sel:WORD_1\n\t" \
  "v_exp_f32 v90, v90\n\t" \
  "v_exp_f32 v116, v93\n\t" \
  "v_mov_b32_sdwa v115, v90 dst_sel:WORD_0 dst_unused:UNUSED_PRESERVE src0_sel:WORD_1\n\t" \
  "v_exp_f32 v92, v92\n\t" \
  "v_exp_f32 v117, v95\n\t" \
  "s_waitcnt lgkmcnt(2)\n\t" \
  "v_mfma_f32_32x32x16_bf16 v[52:67], v[104:107], v[112:115], v[52:67]\n\t" \
  "s_mov_b64 exec, %[vmask]\n\t" \
  "ds_read_b128 v[104:107], v122 offset:2048\n\t" \
  "s_mov_b64 exec, -1\n\t" \
  "v_mov_b32_sdwa v116, v92 dst_sel:WORD_0 dst_unused:UNUSED_PRESERVE src0_sel:WORD_1\n\t" \
  "v_exp_f32 v94, v94\n\t" \
  "v_exp_f32 v118, v97\n\t" \
  "v_mov_b32_sdwa v117, v94 dst_sel:WORD_0 dst_unused:UNUSED_PRESERVE src0_sel:WORD_1\n\t" \
  "v_exp_f32 v96, v96\n\t" \
  "v_exp_f32 v119, v99\n\t" \
  "v_mov_b32_sdwa v118, v96 dst_sel:WORD_0 dst_unused:UNUSED_PRESERVE src0_sel:WORD_1\n\t" \
  "v_exp_f32 v98, v98\n\t" \
  "s_nop 0\n\t" \
  "v_mov_b32_sdwa v119, v98 dst_sel:WORD_0 dst_unused:UNUSED_PRESERVE src0_sel:WORD_1\n\t" \
  "s_waitcnt lgkmcnt(1)\n\t" \
  "v_mfma_f32_32x32x16_bf16 v[84:99], v[100:103], v[32:35], v[36:51]\n\t" \
  "ds_read_b128 v[100:103], v120 offset:4096\n\t" \
  "v_mfma_f32_32x32x16_bf16 v[52:67], v[108:111], v[116:119], v[52:67]\n\t" \
  "s_mov_b64 exec, %[vmask]\n\t" \
  "ds_read_b128 v[108:111], v122 offset:2560\n\t" \
  "s_mov_b64 exec, -1\n\t" \
  "v_exp_f32 v112, v69\n\t" \
  "v_exp_f32 v68, v68\n\t" \
  "v_exp_f32 v113, v71\n\t" \
  "v_mov_b32_sdwa v112, v68 dst_sel:WORD_0 dst_unused:UNUSED_PRESERVE src0_sel:WORD_1\n\t" \
  "v_exp_f32 v70, v70\n\t" \
  "v_exp_f32 v114, v73\n\t" \
  "v_mov_b32_sdwa v113, v70 dst_sel:WORD_0 dst_unused:UNUSED_PRESERVE src0_sel:WORD_1\n\t" \
  "v_exp_f32 v72, v72\n\t" \
  "v_exp_f32 v115, v75\n\t" \
  "v_mov_b32_sdwa v114, v72 dst_sel:WORD_0 dst_unused:UNUSED_PRESERVE src0_sel:WORD_1\n\t" \
  "v_exp_f32 v74, v74\n\t" \
  "v_exp_f32 v116, v77\n\t" \
  "v_mov_b32_sdwa v115, v74 dst_sel:WORD_0 dst_unused:UNUSED_PRESERVE src0_sel:WORD_1\n\t" \
  "v_exp_f32 v76, v76\n\t" \
  "v_exp_f32 v117, v79\n\t" \
  "s_waitcnt lgkmcnt(2)\n\t" \
  "v_mfma_f32_32x32x16_bf16 v[52:67], v[104:107], v[112:115], v[52:67]\n\t" \
  "s_mov_b64 exec, %[vmask]\n\t" \
  "ds_read_b128 v[104:107], v122 offset:3072\n\t" \
  "s_mov_b64 exec, -1\n\t" \
  "v_mov_b32_sdwa v116, v76 dst_sel:WORD_0 dst_unused:UNUSED_PRESERVE src0_sel:WORD_1\n\t" \
  "v_exp_f32 v78, v78\n\t" \
  "v_exp_f32 v118, v81\n\t" \
  "v_mov_b32_sdwa v117, v78 dst_sel:WORD_0 dst_unused:UNUSED_PRESERVE src0_sel:WORD_1\n\t" \
  "v_exp_f32 v80, v80\n\t" \
  "v_exp_f32 v119, v83\n\t" \
  "v_mov_b32_sdwa v118, v80 dst_sel:WORD_0 dst_unused:UNUSED_PRESERVE src0_sel:WORD_1\n\t" \
  "v_exp_f32 v82, v82\n\t" \
  "s_nop 0\n\t" \
  "v_mov_b32_sdwa v119, v82 dst_sel:WORD_0 dst_unused:UNUSED_PRESERVE src0_sel:WORD_1\n\t" \
  "s_waitcnt lgkmcnt(1)\n\t" \
  "v_mfma_f32_32x32x16_bf16 v[68:83], v[100:103], v[32:35], v[36:51]\n\t" \
  "ds_read_b128 v[100:103], v120 offset:5120\n\t" \
  "v_mfma_f32_32x32x16_bf16 v[52:67], v[108:111], v[116:119], v[52:67]\n\t" \
  "s_mov_b64 exec, %[vmask]\n\t" \
  "ds_read_b128 v[108:111], v122 offset:3584\n\t" \
  "s_mov_b64 exec, -1\n\t" \
  "v_exp_f32 v112, v85\n\t" \
  "v_exp_f32 v84, v84\n\t" \
  "v_exp_f32 v113, v87\n\t" \
  "v_mov_b32_sdwa v112, v84 dst_sel:WORD_0 dst_unused:UNUSED_PRESERVE src0_sel:WORD_1\n\t" \
  "v_exp_f32 v86, v86\n\t" \
  "v_exp_f32 v114, v89\n\t" \
  "v_mov_b32_sdwa v113, v86 dst_sel:WORD_0 dst_unused:UNUSED_PRESERVE src0_sel:WORD_1\n\t" \
  "v_exp_f32 v88, v88\n\t" \
  "v_exp_f32 v115, v91\n\t" \
  "v_mov_b32_sdwa v114, v88 dst_sel:WORD_0 dst_unused:UNUSED_PRESERVE src0_sel:WORD_1\n\t" \
  "v_exp_f32 v90, v90\n\t" \
  "v_exp_f32 v116, v93\n\t" \
  "v_mov_b32_sdwa v115, v90 dst_sel:WORD_0 dst_unused:UNUSED_PRESERVE src0_sel:WORD_1\n\t" \
  "v_exp_f32 v92, v92\n\t" \
  "v_exp_f32 v117, v95\n\t" \
  "s_waitcnt lgkmcnt(2)\n\t" \
  "v_mfma_f32_32x32x16_bf16 v[52:67], v[104:107], v[112:115], v[52:67]\n\t" \
  "s_mov_b64 exec, %[vmask]\n\t" \
  "ds_read_b128 v[104:107], v122 offset:4096\n\t" \
  "s_mov_b64 exec, -1\n\t" \
  "v_mov_b32_sdwa v116, v92 dst_sel:WORD_0 dst_unused:UNUSED_PRESERVE src0_sel:WORD_1\n\t" \
  "v_exp_f32 v94, v94\n\t" \
  "v_exp_f32 v118, v97\n\t" \
  "v_mov_b32_sdwa v117, v94 dst_sel:WORD_0 dst_unused:UNUSED_PRESERVE src0_sel:WORD_1\n\t" \
  "v_exp_f32 v96, v96\n\t" \
  "v_exp_f32 v119, v99\n\t" \
  "v_mov_b32_sdwa v118, v96 dst_sel:WORD_0 dst_unused:UNUSED_PRESERVE src0_sel:WORD_1\n\t" \
  "v_exp_f32 v98, v98\n\t" \
  "s_nop 0\n\t" \
  "v_mov_b32_sdwa v119, v98 dst_sel:WORD_0 dst_unused:UNUSED_PRESERVE src0_sel:WORD_1\n\t" \
  "s_waitcnt lgkmcnt(1)\n\t" \
  "v_mfma_f32_32x32x16_bf16 v[84:99], v[100:103], v[32:35], v[36:51]\n\t" \
  "ds_read_b128 v[100:103], v120 offset:6144\n\t" \
  "v_mfma_f32_32x32x16_bf16 v[52:67], v[108:111], v[116:119], v[52:67]\n\t" \
  "s_mov_b64 exec, %[vmask]\n\t" \
  "ds_read_b128 v[108:111], v122 offset:4608\n\t" \
  "s_mov_b64 exec, -1\n\t" \
  "v_exp_f32 v112, v69\n\t" \
  "v_exp_f32 v68, v68\n\t" \
  "v_exp_f32 v113, v71\n\t" \
  "v_mov_b32_sdwa v112, v68 dst_sel:WORD_0 dst_unused:UNUSED_PRESERVE src0_sel:WORD_1\n\t" \
  "v_exp_f32 v70, v70\n\t" \
  "v_exp_f32 v114, v73\n\t" \
  "v_mov_b32_sdwa v113, v70 dst_sel:WORD_0 dst_unused:UNUSED_PRESERVE src0_sel:WORD_1\n\t" \
  "v_exp_f32 v72, v72\n\t" \
  "v_exp_f32 v115, v75\n\t" \
  "v_mov_b32_sdwa v114, v72 dst_sel:WORD_0 dst_unused:UNUSED_PRESERVE src0_sel:WORD_1\n\t" \
  "v_exp_f32 v74, v74\n\t" \
  "v_exp_f32 v116, v77\n\t" \
  "v_mov_b32_sdwa v115, v74 dst_sel:WORD_0 dst_unused:UNUSED_PRESERVE src0_sel:WORD_1\n\t" \
  "v_exp_f32 v76, v76\n\t" \
  "v_exp_f32 v117, v79\n\t" \
  "s_waitcnt lgkmcnt(2)\n\t" \
  "v_mfma_f32_32x32x16_bf16 v[52:67], v[104:107], v[112:115], v[52:67]\n\t" \
  "s_mov_b64 exec, %[vmask]\n\t" \
  "ds_read_b128 v[104:107], v122 offset:5120\n\t" \
  "s_mov_b64 exec, -1\n\t" \
  "v_mov_b32_sdwa v116, v76 dst_sel:WORD_0 dst_unused:UNUSED_PRESERVE src0_sel:WORD_1\n\t" \
  "v_exp_f32 v78, v78\n\t" \
  "v_exp_f32 v118, v81\n\t" \
  "v_mov_b32_sdwa v117, v78 dst_sel:WORD_0 dst_unused:UNUSED_PRESERVE src0_sel:WORD_1\n\t" \
  "v_exp_f32 v80, v80\n\t" \
  "v_exp_f32 v119, v83\n\t" \
  "v_mov_b32_sdwa v118, v80 dst_sel:WORD_0 dst_unused:UNUSED_PRESERVE src0_sel:WORD_1\n\t" \
  "v_exp_f32 v82, v82\n\t" \
  "s_waitcnt vmcnt(0)\n\t" \
  "s_barrier\n\t" \
  "s_add_u32 m0, %[sfree], %[m0k]\n\t" \
  "s_nop 0\n\t" \
  "buffer_load_dwordx4 %[goff], %[rk], %[ssrc] offen lds\n\t" \
  "s_add_u32 m0, m0, 0x2000\n\t" \
  "s_nop 0\n\t" \
  "buffer_load_dwordx4 %[goff], %[rv], %[ssrc] offen lds\n\t" \
  "s_add_u32 %[ssrc], %[ssrc], 0x2000\n\t" \
  "v_mov_b32_sdwa v119, v82 dst_sel:WORD_0 dst_unused:UNUSED_PRESERVE src0_sel:WORD_1\n\t" \
  "s_waitcnt lgkmcnt(1)\n\t" \
  "v_mfma_f32_32x32x16_bf16 v[68:83], v[100:103], v[32:35], v[36:51]\n\t" \
  "ds_read_b128 v[100:103], v120 offset:7168\n\t" \
  "v_mfma_f32_32x32x16_bf16 v[52:67], v[108:111], v[116:119], v[52:67]\n\t" \
  "s_mov_b64 exec, %[vmask]\n\t" \
  "ds_read_b128 v[108:111], v122 offset:5632\n\t" \
  "s_mov_b64 exec, -1\n\t" \
  "v_exp_f32 v112, v85\n\t" \
  "v_exp_f32 v84, v84\n\t" \
  "v_exp_f32 v113, v87\n\t" \
  "v_mov_b32_sdwa v112, v84 dst_sel:WORD_0 dst_unused:UNUSED_PRESERVE src0_sel:WORD_1\n\t" \
  "v_exp_f32 v86, v86\n\t" \
  "v_exp_f32 v114, v89\n\t" \
  "v_mov_b32_sdwa v113, v86 dst_sel:WORD_0 dst_unused:UNUSED_PRESERVE src0_sel:WORD_1\n\t" \
  "v_exp_f32 v88, v88\n\t" \
  "v_exp_f32 v115, v91\n\t" \
  "v_mov_b32_sdwa v114, v88 dst_sel:WORD_0 dst_unused:UNUSED_PRESERVE src0_sel:WORD_1\n\t" \
  "v_exp_f32 v90, v90\n\t" \
  "v_exp_f32 v116, v93\n\t" \
  "v_mov_b32_sdwa v115, v90 dst_sel:WORD_0 dst_unused:UNUSED_PRESERVE src0_sel:WORD_1\n\t" \
  "v_exp_f32 v92, v92\n\t" \
  "v_exp_f32 v117, v95\n\t" \
  "s_waitcnt lgkmcnt(2)\n\t" \
  "v_mfma_f32_32x32x16_bf16 v[52:67], v[104:107], v[112:115], v[52:67]\n\t" \
  "s_mov_b64 exec, %[vmask]\n\t" \
  "ds_read_b128 v[104:107], v122 offset:6144\n\t" \
  "s_mov_b64 exec, -1\n\t" \
  "v_mov_b32_sdwa v116, v92 dst_sel:WORD_0 dst_unused:UNUSED_PRESERVE src0_sel:WORD_1\n\t" \
  "v_exp_f32 v94, v94\n\t" \
  "v_exp_f32 v118, v97\n\t" \
  "v_mov_b32_sdwa v117, v94 dst_sel:WORD_0 dst_unused:UNUSED_PRESERVE src0_sel:WORD_1\n\t" \
  "v_exp_f32 v96, v96\n\t" \
  "v_exp_f32 v119, v99\n\t" \
  "v_mov_b32_sdwa v118, v96 dst_sel:WORD_0 dst_unused:UNUSED_PRESERVE src0_sel:WORD_1\n\t" \
  "v_exp_f32 v98, v98\n\t" \
  "s_nop 0\n\t" \
  "v_mov_b32_sdwa v119, v98 dst_sel:WORD_0 dst_unused:UNUSED_PRESERVE src0_sel:WORD_1\n\t" \
  "s_waitcnt lgkmcnt(1)\n\t" \
  "v_mfma_f32_32x32x16_bf16 v[84:99], v[100:103], v[32:35], v[36:51]\n\t" \
  "ds_read_b128 v[100:103], v121 offset:0\n\t" \
  "v_mfma_f32_32x32x16_bf16 v[52:67], v[108:111], v[116:119], v[52:67]\n\t" \
  "s_mov_b64 exec, %[vmask]\n\t" \
  "ds_read_b128 v[108:111], v122 offset:6656\n\t" \
  "s_mov_b64 exec, -1\n\t" \
  "v_exp_f32 v112, v69\n\t" \
  "v_exp_f32 v68, v68\n\t" \
  "v_exp_f32 v113, v71\n\t" \
  "v_mov_b32_sdwa v112, v68 dst_sel:WORD_0 dst_unused:UNUSED_PRESERVE src0_sel:WORD_1\n\t" \
  "v_exp_f32 v70, v70\n\t" \
  "v_exp_f32 v114, v73\n\t" \
  "v_mov_b32_sdwa v113, v70 dst_sel:WORD_0 dst_unused:UNUSED_PRESERVE src0_sel:WORD_1\n\t" \
  "v_exp_f32 v72, v72\n\t" \
  "v_exp_f32 v115, v75\n\t" \
  "v_mov_b32_sdwa v114, v72 dst_sel:WORD_0 dst_unused:UNUSED_PRESERVE src0_sel:WORD_1\n\t" \
  "v_exp_f32 v74, v74\n\t" \
  "v_exp_f32 v116, v77\n\t" \
  "v_mov_b32_sdwa v115, v74 dst_sel:WORD_0 dst_unused:UNUSED_PRESERVE src0_sel:WORD_1\n\t" \
  "v_exp_f32 v76, v76\n\t" \
  "v_exp_f32 v117, v79\n\t" \
  "s_waitcnt lgkmcnt(2)\n\t" \
  "v_mfma_f32_32x32x16_bf16 v[52:67], v[104:107], v[112:115], v[52:67]\n\t" \
  "s_mov_b64 exec, %[vmask]\n\t" \
  "ds_read_b128 v[104:107], v122 offset:7168\n\t" \
  "s_mov_b64 exec, -1\n\t" \
  "v_mov_b32_sdwa v116, v76 dst_sel:WORD_0 dst_unused:UNUSED_PRESERVE src0_sel:WORD_1\n\t" \
  "v_exp_f32 v78, v78\n\t" \
  "v_exp_f32 v118, v81\n\t" \
  "v_mov_b32_sdwa v117, v78 dst_sel:WORD_0 dst_unused:UNUSED_PRESERVE src0_sel:WORD_1\n\t" \
  "v_exp_f32 v80, v80\n\t" \
  "v_exp_f32 v119, v83\n\t" \
  "v_mov_b32_sdwa v118, v80 dst_sel:WORD_0 dst_unused:UNUSED_PRESERVE src0_sel:WORD_1\n\t" \
  "v_exp_f32 v82, v82\n\t" \
  "s_nop 0\n\t" \
  "v_mov_b32_sdwa v119, v82 dst_sel:WORD_0 dst_unused:UNUSED_PRESERVE src0_sel:WORD_1\n\t" \
  "s_waitcnt lgkmcnt(1)\n\t" \
  "v_mfma_f32_32x32x16_bf16 v[68:83], v[100:103], v[32:35], v[36:51]\n\t" \
  "ds_read_b128 v[100:103], v121 offset:1024\n\t" \
  "v_mfma_f32_32x32x16_bf16 v[52:67], v[108:111], v[116:119], v[52:67]\n\t" \
  "s_mov_b64 exec, %[vmask]\n\t" \
  "ds_read_b128 v[108:111], v122 offset:7680\n\t" \
  "s_mov_b64 exec, -1\n\t" \
  "v_exp_f32 v112, v85\n\t" \
  "v_exp_f32 v84, v84\n\t" \
  "v_exp_f32 v113, v87\n\t" \
  "v_mov_b32_sdwa v112, v84 dst_sel:WORD_0 dst_unused:UNUSED_PRESERVE src0_sel:WORD_1\n\t" \
  "v_exp_f32 v86, v86\n\t" \
  "v_exp_f32 v114, v89\n\t" \
  "v_mov_b32_sdwa v113, v86 dst_sel:WORD_0 dst_unused:UNUSED_PRESERVE src0_sel:WORD_1\n\t" \
  "v_exp_f32 v88, v88\n\t" \
  "v_exp_f32 v115, v91\n\t" \
  "v_mov_b32_sdwa v114, v88 dst_sel:WORD_0 dst_unused:UNUSED_PRESERVE src0_sel:WORD_1\n\t" \
  "v_exp_f32 v90, v90\n\t" \
  "v_exp_f32 v116, v93\n\t" \
  "v_mov_b32_sdwa v115, v90 dst_sel:WORD_0 dst_unused:UNUSED_PRESERVE src0_sel:WORD_1\n\t" \
  "v_exp_f32 v92, v92\n\t" \
  "v_exp_f32 v117, v95\n\t" \
  "s_waitcnt lgkmcnt(2)\n\t" \
  "v_mfma_f32_32x32x16_bf16 v[52:67], v[104:107], v[112:115], v[52:67]\n\t" \
  "s_mov_b64 exec, %[vmask]\n\t" \
  "ds_read_b128 v[104:107], v123 offset:0\n\t" \
  "s_mov_b64 exec, -1\n\t" \
  "v_mov_b32_sdwa v116, v92 dst_sel:WORD_0 dst_unused:UNUSED_PRESERVE src0_sel:WORD_1\n\t" \
  "v_exp_f32 v94, v94\n\t" \
  "v_exp_f32 v118, v97\n\t" \
  "v_mov_b32_sdwa v117, v94 dst_sel:WORD_0 dst_unused:UNUSED_PRESERVE src0_sel:WORD_1\n\t" \
  "v_exp_f32 v96, v96\n\t" \
  "v_exp_f32 v119, v99\n\t" \
  "v_mov_b32_sdwa v118, v96 dst_sel:WORD_0 dst_unused:UNUSED_PRESERVE src0_sel:WORD_1\n\t" \
  "v_exp_f32 v98, v98\n\t" \
  "s_nop 0\n\t" \
  "v_mov_b32_sdwa v119, v98 dst_sel:WORD_0 dst_unused:UNUSED_PRESERVE src0_sel:WORD_1\n\t" \
  "s_mov_b32 %[stmp], %[scur]\n\t" \
  "s_mov_b32 %[scur], %[snext]\n\t" \
  "s_mov_b32 %[snext], %[sfree]\n\t" \
  "s_mov_b32 %[sfree], %[stmp]\n\t" \
  "s_sub_u32 %[cnt], %[cnt], 1\n\t" \
  "s_cmp_lg_u32 %[cnt], 0\n\t" \
  "s_cbranch_scc1 1b\n\t" \
  "s_waitcnt lgkmcnt(0)\n\t" \
  "v_mfma_f32_32x32x16_bf16 v[52:67], v[108:111], v[116:119], v[52:67]\n\t" \
  "s_waitcnt vmcnt(0)\n\t" \
  "s_nop 15\n\t"
#define ATTN_CLOBBERS "v36", "v37", "v38", "v39", "v40", "v41", "v42", "v43", "v44", "v45", "v46", "v47", "v48", "v49", "v50", "v51", "v68", "v69", "v70", "v71", "v72", "v73", "v74", "v75", "v76", "v77", "v78", "v79", "v80", "v81", "v82", "v83", "v84", "v85", "v86", "v87", "v88", "v89", "v90", "v91", "v92", "v93", "v94", "v95", "v96", "v97", "v98", "v99", "v100", "v101", "v102", "v103", "v104", "v105", "v106", "v107", "v108", "v109", "v110", "v111", "v112", "v113", "v114", "v115", "v116", "v117", "v118", "v119", "v120", "v121", "v122", "v123", "m0", "scc", "memory"

constexpr int AT_SLOT = 16384;
DEV void attn_role(char* lds, int a, char* ws) {
#if defined(__HIP_DEVICE_COMPILE__)
  STAMP_DECL
  RSTAMP(0); STAMP(1);
  const int head = (a & 7) * 4 + (a >> 6), qb = (a >> 3) & 7;
  const int tid = threadIdx.x, lane = tid & 63, l31 = lane & 31, h = lane >> 5;
  const int wid = __builtin_amdgcn_readfirstlane(tid >> 6);
  auto krs = mk_rsrc(ws + OFF_K + (size_t)head * (S_ * 32), S_ * 32);
  auto vrs = mk_rsrc(ws + OFF_V + (size_t)head * (S_ * 32), S_ * 32);
  const int q = qb * 256 + wid * 32 + l31;
  bf16x8 qf = *(const bf16x8*)(ws + OFF_Q + (size_t)head * (S_ * 32) + q * 32 + h * 16);
  auto issue = [&](int c, int slot) {
    char* sb = lds + slot * AT_SLOT + wid * 1024;
    dma16(krs, sb, lane * 16, c * 8192 + wid * 1024);
    dma16(vrs, sb + 8192, lane * 16, c * 8192 + wid * 1024);
  };
  const unsigned ldsb = (unsigned)(size_t)lds;
  const unsigned lk = ldsb + lane * 16;
  const unsigned lv = ldsb + 8192 + h * 256 + (l31 & 15) * 16;
  const unsigned goff = lane * 16;
  const unsigned m0k = ldsb + wid * 1024;
  int* flag = (int*)(lds + 3 * AT_SLOT);
  float mref = 0.f;
  f32x16 o;
  for (int attempt = 0; attempt < 2; ++attempt) {
    if (attempt == 1) {
      float mx = -__builtin_inff();
      for (int c = 0; c < 8; ++c) {
        issue(c, 0);
        WAIT_V(0); RAW_BARRIER();
        for (int t = 0; t < 8; ++t) {
          bf16x8 kf = *(const bf16x8*)(lds + t * 1024 + lane * 16);
          f32x16 z = {};
          f32x16 s = mfma32(kf, qf, z);
#pragma unroll
          for (int r = 0; r < 16; ++r) mx = fmaxf(mx, s[r]);
        }
        RAW_BARRIER();
      }
      mx = fmaxf(mx, __shfl_xor(mx, 32));
      mref = mx;
    }
    STAMP(2);
    issue(0, 0);
    issue(1, 1);
    STAMP(3);
    WAIT_V(2); RAW_BARRIER();
    STAMP(4);
    if (attempt == 0) {
      bf16x8 kf = *(const bf16x8*)(lds + lane * 16);
      f32x16 z = {};
      f32x16 s = mfma32(kf, qf, z);
      float mx = s[0];
#pragma unroll
      for (int r = 1; r < 16; ++r) mx = fmaxf(mx, s[r]);
      mx = fmaxf(mx, __shfl_xor(mx, 32));
      mref = mx;
    }
    STAMP(5);
    {
      float negm = -mref;
      unsigned scur, snext, sfree, stmp, cnt, ssrc = 2 * 8192 + wid * 1024;
      unsigned long long vmask = 0x0000FFFF0000FFFFull;
      asm volatile(ATTN_ASM
                   : "={v[52:67]}"(o), [scur] "=&s"(scur), [snext] "=&s"(snext), [sfree] "=&s"(sfree), [stmp] "=&s"(stmp),
                     [cnt] "=&s"(cnt), [ssrc] "+s"(ssrc)
                   : "{v[32:35]}"(qf), [negm] "v"(negm), [lk] "v"(lk), [lv] "v"(lv), [goff] "v"(goff), [rk] "s"(krs), [rv] "s"(vrs),
                     [m0k] "s"(m0k), [vmask] "s"(vmask)
                   : ATTN_CLOBBERS);
    }
    STAMP(40);
    bool bad = !(o[8] < 1.0e30f);
#pragma unroll
    for (int r = 0; r < 8; ++r) bad |= !(fabsf(o[r]) < 3.0e38f);
    if (tid == 0) *flag = 0;
    RAW_BARRIER();
    if (attempt == 0 && __any(bad)) { if (lane == 0) *flag = 1; }
    RAW_BARRIER();
    int f = *flag;
    RAW_BARRIER();
    if (f == 0) break;
  }
  float inv = 1.0f / o[8];
  u32x4 ov = widen(pk4(o[0] * inv, o[1] * inv, o[2] * inv, o[3] * inv), pk4(o[4] * inv, o[5] * inv, o[6] * inv, o[7] * inv));
  {
    const unsigned l2 = fresh_lane();
    auto ors = mk_rsrc(ws + OFF_O, 64 * S_ * 16);
    __builtin_amdgcn_raw_buffer_store_b128(ov, ors, ((l2 >> 5) * S_ + (l2 & 31)) * 16, (2 * head * S_ + qb * 256 + wid * 32) * 16, 0);
  }
  STAMP(41); RSTAMP(42); STAMP_FLUSH(0);
#endif
}

namespace bsched {
constexpr int NG = 46;
constexpr int pos(int g) {
  if (g < 8) return 8 * g;
  if (g < 24) return 12 * ((g - 8) % 6);
  int u = (g - 24) % 10;
  return u == 0 ? 72 : u == 1 ? 48 : u == 2 ? 56 : u == 3 ? 64 : 8 * (u - 4);
}
constexpr int npw(int g) { return g < 8 ? 2 : g < 24 ? 3 : g == 45 ? 1 : 2; }
constexpr int iss(int g) {
  if (g < 8) return -1;
  if (g < 24) { int t = g - 8; return t == 0 ? 3 : t == 1 ? 4 : t == 2 ? 6 : t == 3 ? 7 : t == 4 ? 8 : t == 5 ? 9 : t + 6; }
  int u = g - 24;
  return u == 0 ? 9 : u == 1 ? 22 : u == 2 ? 23 : u == 3 ? 23 : u == 4 ? 24 : u == 5 ? 25 : u == 6 ? 25 : u <= 9 ? 26 : u + 19;
}
constexpr int pub(int g) { return g < 8 ? g : g < 24 ? g + 2 : g + 3; }
constexpr int pubbed(int bi) { return bi < 8 ? bi : (bi >= 10 && bi <= 25) ? bi - 2 : (bi >= 27 && bi <= 48) ? bi - 3 : -1; }
constexpr bool before(int a, int b) { return iss(a) != iss(b) ? iss(a) < iss(b) : a < b; }
constexpr int waitcnt(int g) {
  int n = 0;
  for (int o = 0; o < NG; ++o) if (o != g && before(g, o) && iss(o) < pub(g)) n += npw(o);
  return n;
}
}

DEV u32x4 ld_sc1(__amdgpu_buffer_rsrc_t r, unsigned voff, unsigned soff) { return __builtin_amdgcn_raw_buffer_load_b128(r, voff, soff, 16); }
DEV void st_sc1(__amdgpu_buffer_rsrc_t r, u32x4 v, unsigned voff, unsigned soff) { __builtin_amdgcn_raw_buffer_store_b128(v, r, voff, soff, 16); }

DEV void b_signal_wait(unsigned* cnt, int lane) {
  if (lane == 0) {
    __hip_atomic_fetch_add(cnt, 1u, __ATOMIC_RELAXED, __HIP_MEMORY_SCOPE_AGENT);
    for (int it = 0; it < (1 << 22); ++it) {
      if (__hip_atomic_load(cnt, __ATOMIC_RELAXED, __HIP_MEMORY_SCOPE_AGENT) >= 8u) break;
      __builtin_amdgcn_s_sleep(2);
    }
  }
}

DEV void b_loader(char* lds, int lw, int lane, int grp, int j, int c0, const u16* w2, const u16* w3, const u16* w4, char* ws) {
#if defined(__HIP_DEVICE_COMPILE__)
  using namespace bsched;
  auto r2 = mk_rsrc(w2, D_ * D_ * 2);
  auto r3 = mk_rsrc(w3, D_ * F_ * 2);
  auto r4 = mk_rsrc(w4, F_ * D_ * 2);
  unsigned va[2], vb[3];
#pragma unroll
  for (int i = 0; i < 2; ++i) {
    unsigned bb = (lw + 4 * i) * 1024 + lane * 16, row = bb >> 7, cp = (bb >> 4) & 7;
    va[i] = row * 1024 + j * 128 + ((cp ^ (((row >> 1) & 1) << 2)) << 4);
  }
#pragma unroll
  for (int i = 0; i < 3; ++i) {
    unsigned bb = (lw + 4 * i) * 1024 + lane * 16, row = bb / 384, cp = (bb % 384) >> 4;
    vb[i] = row * (F_ * 2) + c0 * 2 + ((cp ^ (((row >> 1) & 1) << 2)) << 4);
  }
  auto issue = [&](auto G) {
    constexpr int g = G;
    char* sb = lds + pos(g) * 1024 + lw * 1024;
    if constexpr (g < 8) { dma16(r2, sb, va[0], g * 65536); dma16(r2, sb + 4096, va[1], g * 65536); }
    else if constexpr (g < 24) {
#pragma unroll
      for (int i = 0; i < 3; ++i) dma16(r3, sb + i * 4096, vb[i], (g - 8) * 32 * (F_ * 2));
    } else {
      dma16(r4, sb, va[0], (g - 24) * 65536);
      if constexpr (g != 45) dma16(r4, sb + 4096, va[1], (g - 24) * 65536);
    }
  };
  static_for<NG>([&](auto G) { if constexpr (iss(G) == -1) issue(G); });
  static_for<50>([&](auto BI) {
    constexpr int bi = BI;
    constexpr int pg = pubbed(bi);
    if constexpr (bi == 10 || bi == 27) {
      if (lw == 0) b_signal_wait((unsigned*)(ws + OFF_CNT) + (bi == 10 ? grp : 32 + grp) * 64, lane);
    }
    if constexpr (pg >= 0) WAIT_V(waitcnt(pg));
    BARRIER_NOWAIT();
    static_for<NG>([&](auto G) { if constexpr (iss(G) == bi) issue(G); });
  });
#endif
}

DEV void b_compute(char* lds, int wid, int lane, int grp, int j, int c0, int ntile, const u16* x, char* ws) {
#if defined(__HIP_DEVICE_COMPILE__)
  using namespace bsched;
  STAMP_DECL
  RSTAMP(0); STAMP(1);
  const int wm = wid & 1, kp = wid >> 1;
  const int m0 = grp * 64;
  auto hrs = mk_rsrc(ws + OFF_HB, 64 * HB_ROWS * 16);
  auto x1rs = mk_rsrc(ws + OFF_X1, 64 * S_ * 16);
  auto h2rs = mk_rsrc(ws + OFF_H2, 172 * S_ * 16);
  auto xrs = mk_rsrc(x, S_ * D_ * 2);
  const unsigned col0 = j * 64 + kp * 32;
  unsigned l31, h, blk, q, p, row, sw, cpart, wo13[2];
  auto lane_init = [&]() {
    lane = fresh_lane();
    l31 = lane & 31; h = lane >> 5; blk = (lane >> 4) & 1; q = (lane & 15) >> 2; p = lane & 3;
    row = m0 + wm * 32 + l31; sw = (q >> 1) & 1; cpart = 8 * (p & 1);
#pragma unroll
    for (int a = 0; a < 2; ++a) wo13[a] = (16 * kp + 8 * h + q) * 128 + (((4 * (a ^ kp) + 2 * blk + (p >> 1)) ^ (sw << 2)) << 4) + cpart;
  };
  lane_init();
  u32x2 xv[4];
#pragma unroll
  for (int i = 0; i < 4; ++i) xv[i] = __builtin_amdgcn_raw_buffer_load_b64(xrs, (row * D_ + col0 + 4 * h) * 2, i * 16, 0);
  const f32x2 st0 = *(const f32x2*)(ws + OFF_ST0 + (size_t)row * 8);

  auto phase13 = [&](auto G0_, auto NI_, auto QD_, auto loadA, f32x16 (&acc)[2], auto&& first_issue_done) {
    constexpr int G0 = G0_, NI = NI_, QD = QD_;
    u32x4 aq[QD];
    static_for<QD>([&](auto I) { if constexpr (I < NI) aq[I] = loadA(I); });
    first_issue_done();
    bf16x8 wf[2][2];
    auto rd = [&](auto I) {
      constexpr int i = I;
      const char* sb = lds + pos(G0 + (i >> 1)) * 1024 + (i & 1) * 4096;
      wf[i & 1][0] = tr_frag(sb + wo13[0], sb + wo13[0] + 512);
      wf[i & 1][1] = tr_frag(sb + wo13[1], sb + wo13[1] + 512);
    };
    auto mm = [&](auto I) {
      constexpr int i = I;
      bf16x8 a = __builtin_bit_cast(bf16x8, aq[i % QD]);
      acc[0] = mfma32(wf[i & 1][0], a, acc[0]);
      acc[1] = mfma32(wf[i & 1][1], a, acc[1]);
      if constexpr (i + QD < NI) aq[i % QD] = loadA(IC<i + QD>{});
    };
    static_for<NI>([&](auto I) {
      constexpr int i = I;
      if constexpr ((i & 1) == 0) { BARRIER_NOWAIT(); STAMP(2 + (G0 + (i >> 1))); }
      rd(I);
      if constexpr (i > 0) mm(IC<i - 1>{});
      SCHED_FENCE();
    });
    mm(IC<NI - 1>{});
  };
  auto exchange = [&](f32x16 (&acc)[2], int xpos) {
    char* xb = lds + xpos * 1024;
    char* mine = xb + (wm * 2 + (kp ^ 1)) * 4096 + lane * 16;
    const char* theirs = xb + (wm * 2 + kp) * 4096 + lane * 16;
#pragma unroll
    for (int i = 0; i < 4; ++i) *(f32x4*)(mine + i * 1024) = f32x4{acc[1][4 * i], acc[1][4 * i + 1], acc[1][4 * i + 2], acc[1][4 * i + 3]};
    RAW_BARRIER();
#pragma unroll
    for (int i = 0; i < 4; ++i) {
      f32x4 t = *(const f32x4*)(theirs + i * 1024);
#pragma unroll
      for (int c = 0; c < 4; ++c) acc[0][4 * i + c] += t[c];
    }
  };

  {
    f32x16 acc[2] = {};
    const unsigned va = (h * HB_ROWS + row) * 16 + kp * (2 * HB_ROWS * 16);
    auto loadA = [&](auto I) {
      constexpr int i = I;
      constexpr int kk0 = 4 * (i >> 1) + 2 * (i & 1), g = i >> 2;
      constexpr int d = g == 0 ? -1 : g == 1 ? 1 : g == 2 ? -2 : 2;
      return __builtin_amdgcn_raw_buffer_load_b128(hrs, va, kk0 * (2 * HB_ROWS * 16) + (2 + d) * 16, 0);
    };
    phase13(IC<0>{}, IC<16>{}, IC<10>{}, loadA, acc, [] {});
    exchange(acc, 64);
    STAMP(50);
    float v[16], a1 = 0.f, a2 = 0.f;
#pragma unroll
    for (int i = 0; i < 4; ++i) {
      v[4 * i + 0] = (bf_lo(xv[i][0]) - st0[0]) * st0[1] + acc[0][4 * i + 0];
      v[4 * i + 1] = (bf_hi(xv[i][0]) - st0[0]) * st0[1] + acc[0][4 * i + 1];
      v[4 * i + 2] = (bf_lo(xv[i][1]) - st0[0]) * st0[1] + acc[0][4 * i + 2];
      v[4 * i + 3] = (bf_hi(xv[i][1]) - st0[0]) * st0[1] + acc[0][4 * i + 3];
    }
#pragma unroll
    for (int i = 0; i < 16; ++i) { a1 += v[i]; a2 += v[i] * v[i]; }
    a1 = xhalf_sum(a1); a2 = xhalf_sum(a2);
#pragma unroll
    for (int g = 0; g < 2; ++g) st_sc1(x1rs, pack8(v + 8 * g), ((col0 / 8 + 2 * g + h) * S_ + row) * 16, 0);
    if (lane < 32) {
      auto srs = mk_rsrc(ws + OFF_ST1, 16 * S_ * 8);
      u32x2 sv = {__float_as_uint(a1), __float_as_uint(a2)};
      __builtin_amdgcn_raw_buffer_store_b64(sv, srs, ((col0 >> 5) * S_ + row) * 8, 0, 16);
    }
    WAIT_V(0);
    BARRIER_NOWAIT();
    STAMP(51);
  }
  {
    const int wn = kp;
    lane_init();
    float cs[3];
    {
      const float* cp = (const float*)(ws + OFF_CS3) + h * 4 * CS3_LD + c0 + wn * 96 + l31;
#pragma unroll
      for (int a = 0; a < 3; ++a) {
        float t = 0.f;
#pragma unroll
        for (int i = 0; i < 4; ++i) t += cp[i * CS3_LD + a * 32];
        cs[a] = xhalf_sum(t);
      }
    }
    unsigned wo2[3];
#pragma unroll
    for (int a = 0; a < 3; ++a) wo2[a] = (8 * h + q) * 384 + (((4 * (3 * wn + a) + 2 * blk + (p >> 1)) ^ (sw << 2)) << 4) + cpart;
    f32x16 acc[3] = {};
    constexpr int QD = 8;
    u32x4 aq[QD];
    const unsigned va = (h * S_ + row) * 16;
    BARRIER_NOWAIT();
    STAMP(52);
    u32x2 sp[8];
    {
      auto srs = mk_rsrc(ws + OFF_ST1, 16 * S_ * 8);
#pragma unroll
      for (int i = 0; i < 8; ++i) sp[i] = __builtin_amdgcn_raw_buffer_load_b64(srs, ((h * 8 + i) * S_ + row) * 8, 0, 16);
    }
    static_for<QD>([&](auto I) { aq[I] = ld_sc1(x1rs, va, I * 65536); });
    bf16x8 wf[2][3];
    auto rd = [&](auto I) {
      constexpr int i = I;
      const char* sb = lds + pos(8 + (i >> 1)) * 1024 + (i & 1) * (16 * 384);
#pragma unroll
      for (int a = 0; a < 3; ++a) wf[i & 1][a] = tr_frag(sb + wo2[a], sb + wo2[a] + 4 * 384);
    };
    auto mm = [&](auto I) {
      constexpr int i = I;
      bf16x8 a = __builtin_bit_cast(bf16x8, aq[i % QD]);
#pragma unroll
      for (int t = 0; t < 3; ++t) acc[t] = mfma32(wf[i & 1][t], a, acc[t]);
      if constexpr (i + QD < 32) aq[i % QD] = ld_sc1(x1rs, va, (i + QD) * 65536);
    };
    float mu, rs;
    {
      float a1 = 0.f, a2 = 0.f;
#pragma unroll
      for (int t = 0; t < 8; ++t) { a1 += __uint_as_float(sp[t][0]); a2 += __uint_as_float(sp[t][1]); }
      a1 = xhalf_sum(a1); a2 = xhalf_sum(a2);
      mu = a1 * (1.0f / D_);
      rs = __builtin_amdgcn_rsqf(fmaxf(a2 * (1.0f / D_) - mu * mu, 0.f) + LN_EPS);
      SCHED_FENCE();
    }
    static_for<32>([&](auto I) {
      constexpr int i = I;
      if constexpr ((i & 1) == 0 && i > 0) { BARRIER_NOWAIT(); STAMP(2 + 8 + (i >> 1)); }
      rd(I);
      if constexpr (i > 0) mm(IC<i - 1>{});
      SCHED_FENCE();
    });
    mm(IC<31>{});
    {
      unsigned mh = pk_bf16(-mu, -mu);
      float mlo = -mu - bf_lo(mh);
      u32x4 xe = {h ? 0u : mh, h ? 0u : (pk_bf16(mlo, 0.f)), 0u, 0u};
#pragma unroll
      for (int t = 0; t < 3; ++t) {
        unsigned ch = pk_bf16(cs[t], cs[t]);
        float clo = cs[t] - bf_lo(ch);
        u32x4 we = {h ? 0u : ((ch & 0xffffu) | (pk_bf16(clo, clo) << 16)), h ? 0u : (ch & 0xffffu), 0u, 0u};
        acc[t] = mfma32(__builtin_bit_cast(bf16x8, we), __builtin_bit_cast(bf16x8, xe), acc[t]);
      }
    }
    STAMP(53);
#pragma unroll
    for (int t = 0; t < 3; ++t) {
      if (3 * wn + t < ntile) {
        float v[16];
#pragma unroll
        for (int i = 0; i < 16; ++i) v[i] = gelu_tanh(acc[t][i] * rs);
        const unsigned kt8 = (c0 + (3 * wn + t) * 32) >> 3;
#pragma unroll
        for (int g = 0; g < 2; ++g) st_sc1(h2rs, pack8(v + 8 * g), ((kt8 + 2 * g + h) * S_ + row) * 16, 0);
      }
    }
    WAIT_V(0);
    BARRIER_NOWAIT();
    STAMP(54);
  }
  {
    lane_init();
    f32x16 acc[2] = {};
    const unsigned va = (h * S_ + row) * 16 + kp * 65536;
    auto loadA = [&](auto I) {
      constexpr int i = I;
      constexpr int kk0 = 4 * (i >> 1) + 2 * (i & 1);
      return ld_sc1(h2rs, va, kk0 * 65536);
    };
    BARRIER_NOWAIT();
    STAMP(55);
    constexpr int NI = 43, QD = 10;
    u32x4 aq[QD];
    static_for<QD>([&](auto I) { aq[I] = loadA(I); });
    bf16x8 wf[2][2];
    u32x2 xr[4], x1r[4];
    auto rd = [&](auto I) {
      constexpr int i = I;
      const char* sb = lds + pos(24 + (i >> 1)) * 1024 + (i & 1) * 4096;
      wf[i & 1][0] = tr_frag(sb + wo13[0], sb + wo13[0] + 512);
      wf[i & 1][1] = tr_frag(sb + wo13[1], sb + wo13[1] + 512);
    };
    auto mm = [&](auto I) {
      constexpr int i = I;
      bf16x8 a = __builtin_bit_cast(bf16x8, aq[i % QD]);
      acc[0] = mfma32(wf[i & 1][0], a, acc[0]);
      acc[1] = mfma32(wf[i & 1][1], a, acc[1]);
      if constexpr (i + QD < NI) aq[i % QD] = loadA(IC<i + QD>{});
      if constexpr (i + QD == NI) {
#pragma unroll
        for (int t = 0; t < 4; ++t) {
          xr[t] = __builtin_amdgcn_raw_buffer_load_b64(xrs, (row * D_ + col0 + 4 * h) * 2, t * 16, 0);
          x1r[t] = __builtin_amdgcn_raw_buffer_load_b64(x1rs, ((col0 / 8 + t) * S_ + row) * 16 + 8 * h, 0, 16);
        }
      }
    };
    static_for<NI>([&](auto I) {
      constexpr int i = I;
      if constexpr ((i & 1) == 0 && i > 0) { BARRIER_NOWAIT(); STAMP(2 + 24 + (i >> 1)); }
      rd(I);
      if constexpr (i > 0) mm(IC<i - 1>{});
      SCHED_FENCE();
    });
    mm(IC<NI - 1>{});
    exchange(acc, 32);
    STAMP(56);
    char* rb = ws + OFF_R + ((size_t)((m0 >> 5) + wm) * 16 + (col0 >> 5)) * 4096 + fresh_lane() * 16;
#pragma unroll
    for (int i = 0; i < 4; ++i) {
      f32x4 o;
      o[0] = acc[0][4 * i + 0] + bf_lo(xr[i][0]) + bf_lo(x1r[i][0]);
      o[1] = acc[0][4 * i + 1] + bf_hi(xr[i][0]) + bf_hi(x1r[i][0]);
      o[2] = acc[0][4 * i + 2] + bf_lo(xr[i][1]) + bf_lo(x1r[i][1]);
      o[3] = acc[0][4 * i + 3] + bf_hi(xr[i][1]) + bf_hi(x1r[i][1]);
      *(f32x4*)(rb + i * 1024) = o;
    }
  }
  STAMP(60); RSTAMP(61); STAMP_FLUSH(0);
#endif
}

constexpr int K2_LDS = 81920;
__global__ __launch_bounds__(512, 4) void k2(const u16* __restrict__ x, const u16* __restrict__ w2, const u16* __restrict__ w3,
                                             const u16* __restrict__ w4, char* __restrict__ ws) {
#if defined(__HIP_DEVICE_COMPILE__)
  extern __shared__ __attribute__((aligned(16))) char lds[];
  const int b = blockIdx.x;
  if (b >= 256) { attn_role(lds, b - 256, ws); return; }
  const int lane = threadIdx.x & 63;
  const int wid = __builtin_amdgcn_readfirstlane(threadIdx.x >> 6);
  const int grp = (b & 7) * 4 + (b >> 6), j = (b >> 3) & 7;
  const int c0 = j < 3 ? j * 192 : 576 + (j - 3) * 160;
  const int ntile = j < 3 ? 6 : 5;
  __builtin_amdgcn_s_setprio(2);
  if (wid >= 4) b_loader(lds, wid - 4, lane, grp, j, c0, w2, w3, w4, ws);
  else b_compute(lds, wid, lane, grp, j, c0, ntile, x, ws);
#endif
}

constexpr int K3_LDS = 131072;
__global__ __launch_bounds__(512, 2) void k3(const u16* __restrict__ wo, const char* __restrict__ ws, u16* __restrict__ out) {
#if defined(__HIP_DEVICE_COMPILE__)
  extern __shared__ __attribute__((aligned(16))) char lds[];
  STAMP_DECL
  RSTAMP(0); STAMP(1);
  const int b = blockIdx.x, xcd = b & 7, bi = b >> 3;
  const int mt = xcd * 4 + (bi >> 3), nt = bi & 7;
  const int m0 = mt * 64;
  const int tid = threadIdx.x, lane = tid & 63;
  const int wid = __builtin_amdgcn_readfirstlane(tid >> 6);
  auto ors = mk_rsrc(ws + OFF_O, 64 * S_ * 16);
  auto wrs = mk_rsrc(wo, D_ * D_ * 2);
  const unsigned va = (m0 + lane) * 16;
  unsigned vw;
  { unsigned row = lane >> 3, cp = lane & 7; vw = row * 1024 + nt * 128 + ((cp ^ (((row >> 1) & 1) << 2)) << 4); }
  const int rot = (nt & 3);
#pragma unroll
  for (int c = 0; c < 4; ++c) {
    const int cc = (c + rot) & 3;
#pragma unroll
    for (int i = 0; i < 2; ++i) {
      const int pc = 2 * wid + i;
      dma16(ors, lds + (c * 16 + pc) * 1024, va, (cc * 16 + pc) * (S_ * 16));
      dma16(wrs, lds + 65536 + (c * 16 + pc) * 1024, vw, (cc * 16 + pc) * 8192);
    }
  }
  const unsigned l31 = lane & 31, h = lane >> 5, blk = (lane >> 4) & 1, q = (lane & 15) >> 2, p = lane & 3;
  const int wm = wid & 1, wn = (wid >> 1) & 1;
  f32x4 r[4];
  if (wid < 4) {
    const char* rb = ws + OFF_R + ((size_t)((m0 >> 5) + wm) * 16 + nt * 2 + wn) * 4096 + lane * 16;
#pragma unroll
    for (int i = 0; i < 4; ++i) r[i] = *(const f32x4*)(rb + i * 1024);
  }
  const unsigned ao = h * 1024 + (wm * 32 + l31) * 16;
  const unsigned wo_ = 65536 + (8 * h + q) * 128 + (((4 * wn + 2 * blk + (p >> 1)) ^ (((q >> 1) & 1) << 2)) << 4) + 8 * (p & 1);
  f32x16 acc = {};
  static_for<4>([&](auto C) {
    constexpr int c = C;
    if (wid < 4) WAIT_V((3 - c) * 4 + 4); else WAIT_V((3 - c) * 4);
    BARRIER_NOWAIT();
    STAMP(4 + c);
    if (wid < 4) {
      bf16x8 xf[8], wf[8];
#pragma unroll
      for (int s = 0; s < 8; ++s) {
        xf[s] = *(const bf16x8*)(lds + c * 16384 + s * 2048 + ao);
        wf[s] = tr_frag(lds + c * 16384 + s * 2048 + wo_, lds + c * 16384 + s * 2048 + wo_ + 512);
      }
#pragma unroll
      for (int s = 0; s < 8; ++s) acc = mfma32(wf[s], xf[s], acc);
    }
  });
  STAMP(40);
  if (wid < 4) {
    float v[16];
#pragma unroll
    for (int i = 0; i < 4; ++i)
#pragma unroll
      for (int c = 0; c < 4; ++c) v[4 * i + c] = acc[4 * i + c] + r[i][c];
    u16* dst = out + (size_t)(m0 + wm * 32 + l31) * D_ + nt * 64 + wn * 32 + h * 8;
#pragma unroll
    for (int g = 0; g < 2; ++g) *(u32x4*)(dst + g * 16) = pack8(v + 8 * g);
  }
  STAMP(41); RSTAMP(42); STAMP_FLUSH(0);
#endif
}

#define SEL(i) true
extern "C" void kernel_launch(void* const* d_in, const int* in_sizes, int n_in, void* d_out, int out_size,
                              void* d_ws, size_t ws_size, hipStream_t stream) {
  const u16* x = (const u16*)d_in[0];
  const u16* wqkv = (const u16*)d_in[1];
  const u16* wo = (const u16*)d_in[2];
  const u16* w1 = (const u16*)d_in[4];
  const u16* w2 = (const u16*)d_in[5];
  const u16* w3 = (const u16*)d_in[6];
  const u16* w4 = (const u16*)d_in[7];
  char* ws = (char*)d_ws;
  (void)hipFuncSetAttribute((const void*)k1, hipFuncAttributeMaxDynamicSharedMemorySize, K1_LDS);
  (void)hipFuncSetAttribute((const void*)k2, hipFuncAttributeMaxDynamicSharedMemorySize, K2_LDS);
  (void)hipFuncSetAttribute((const void*)k3, hipFuncAttributeMaxDynamicSharedMemorySize, K3_LDS);
  if (SEL(0)) k1<<<256, 512, K1_LDS, stream>>>(x, wqkv, w1, w3, w2, w4, wo, ws);
  if (SEL(1)) k2<<<512, 512, K2_LDS, stream>>>(x, w2, w3, w4, ws);
  if (SEL(2)) k3<<<256, 512, K3_LDS, stream>>>(wo, ws, (u16*)d_out);
}
```

```cpp
#include <hip/hip_runtime.h>
#include <stdint.h>

typedef __attribute__((ext_vector_type(8))) __bf16 bf16x8;
typedef __attribute__((ext_vector_type(2))) __bf16 bf16x2;
typedef __attribute__((ext_vector_type(4))) short s16x4;
typedef __attribute__((ext_vector_type(16))) float f32x16;
typedef __attribute__((ext_vector_type(4))) float f32x4;
typedef __attribute__((ext_vector_type(2))) float f32x2;
typedef __attribute__((ext_vector_type(4))) unsigned u32x4;
typedef __attribute__((ext_vector_type(2))) unsigned u32x2;
typedef __attribute__((address_space(3))) void* ldsptr_t;
typedef unsigned short u16;

#define DEV __device__ __forceinline__

constexpr int S_ = 2048, D_ = 512, H_ = 32, F_ = 1376;
constexpr float LN_EPS = 1e-5f;
constexpr float QSCALE = 0.25f * 1.4426950408889634f;

constexpr size_t MB_ = 1 << 20, KB_ = 1 << 10;
constexpr size_t OFF_Q = 0, OFF_K = 2 * MB_, OFF_V = 4 * MB_, OFF_O = 6 * MB_, OFF_X1 = 8 * MB_, OFF_HB = 10 * MB_;
constexpr size_t OFF_ST0 = 12 * MB_ + 64 * KB_, OFF_CNT = 12 * MB_ + 96 * KB_, OFF_CS3 = 12 * MB_ + 128 * KB_;
constexpr size_t OFF_ST1 = 12 * MB_ + 256 * KB_, OFF_H2 = 13 * MB_, OFF_R = 19 * MB_;
constexpr int HB_ROWS = S_ + 4;
constexpr int CS3_P = 8, CS3_LD = 1408;

template <int N> struct IC { static constexpr int value = N; constexpr operator int() const { return N; } };
template <int I, int N, class F> DEV void static_for_impl(F&& f) {
  if constexpr (I < N) { f(IC<I>{}); static_for_impl<I + 1, N>(f); }
}
template <int N, class F> DEV void static_for(F&& f) { static_for_impl<0, N>(f); }

DEV unsigned pk_bf16(float lo, float hi) {
  f32x2 v = {lo, hi};
  return __builtin_bit_cast(unsigned, __builtin_convertvector(v, bf16x2));
}
DEV float bf_lo(unsigned u) { return __uint_as_float(u << 16); }
DEV float bf_hi(unsigned u) { return __uint_as_float(u & 0xffff0000u); }
DEV float gelu_tanh(float x) {
  const float c1 = -2.0f * 0.7978845608028654f * 1.4426950408889634f;
  const float c2 = c1 * 0.044715f;
  float t = x * (c1 + c2 * x * x);
  float e = __builtin_amdgcn_exp2f(t);
  return x * __builtin_amdgcn_rcpf(1.0f + e);
}
DEV float dot2(unsigned a, unsigned b, float c) {
  return __builtin_amdgcn_fdot2_f32_bf16(__builtin_bit_cast(bf16x2, a), __builtin_bit_cast(bf16x2, b), c, false);
}
DEV float xhalf_sum(float v) {
  auto r = __builtin_amdgcn_permlane32_swap(__float_as_uint(v), __float_as_uint(v), false, false);
  return __uint_as_float(r[0]) + __uint_as_float(r[1]);
}
#define STAMP(i)
#define RSTAMP(i)
#define STAMP_DECL
#define STAMP_ARG
#define STAMP_PASS
#define STAMP_FLUSH(w)
#define WAIT_V(n) asm volatile("s_waitcnt vmcnt(%0)" ::"n"(n) : "memory")
#define WAIT_L(n) asm volatile("s_waitcnt lgkmcnt(%0)" ::"n"(n) : "memory")
#define SCHED_FENCE() __builtin_amdgcn_sched_barrier(0)
#define RAW_BARRIER() do { WAIT_L(0); __builtin_amdgcn_s_barrier(); } while (0)
#define BARRIER_NOWAIT() do { SCHED_FENCE(); asm volatile("s_barrier" ::: "memory"); SCHED_FENCE(); } while (0)

DEV unsigned fresh_lane() {
  unsigned l;
  asm volatile("v_mbcnt_lo_u32_b32 %0, -1, 0\n\tv_mbcnt_hi_u32_b32 %0, -1, %0" : "=v"(l));
  return l;
}
DEV __amdgpu_buffer_rsrc_t mk_rsrc(const void* p, unsigned bytes) {
  return __builtin_amdgcn_make_buffer_rsrc((void*)p, 0, (int)bytes, 0x00020000);
}
DEV void dma16(__amdgpu_buffer_rsrc_t r, char* lds_dst, unsigned voff, unsigned soff) {
  __builtin_amdgcn_raw_ptr_buffer_load_lds(r, (ldsptr_t)lds_dst, 16, voff, soff, 0, 0);
}
DEV bf16x8 tr_frag(const char* p0, const char* p1) {
  s16x4 a = __builtin_amdgcn_ds_read_tr16_b64_v4i16((__attribute__((address_space(3))) s16x4*)p0);
  s16x4 b = __builtin_amdgcn_ds_read_tr16_b64_v4i16((__attribute__((address_space(3))) s16x4*)p1);
  typedef __attribute__((ext_vector_type(8))) short s16x8;
  s16x8 r = __builtin_shufflevector(a, b, 0, 1, 2, 3, 4, 5, 6, 7);
  return __builtin_bit_cast(bf16x8, r);
}
DEV f32x16 mfma32(bf16x8 a, bf16x8 b, f32x16 c) { return __builtin_amdgcn_mfma_f32_32x32x16_bf16(a, b, c, 0, 0, 0); }
DEV u32x2 pk4(float a, float b, float c, float d) { u32x2 r; r[0] = pk_bf16(a, b); r[1] = pk_bf16(c, d); return r; }
DEV u32x4 widen(u32x2 X, u32x2 Y) {
  u32x4 o;
  auto a = __builtin_amdgcn_permlane32_swap(X[0], Y[0], false, false);
  auto b = __builtin_amdgcn_permlane32_swap(X[1], Y[1], false, false);
  o[0] = a[0]; o[1] = b[0]; o[2] = a[1]; o[3] = b[1];
  return o;
}
DEV u32x4 pack8(const float* v) { return widen(pk4(v[0], v[1], v[2], v[3]), pk4(v[4], v[5], v[6], v[7])); }

constexpr int K1_A = 16384, K1_ST = 32768;
constexpr int K1_X = 4 * K1_ST;
constexpr int K1_P3 = K1_X + 2048 + 1024 + 1536;
constexpr int K1_LDS = K1_P3 + 2048 + 16;

template <int KIND>
DEV void k1_epi_s(const f32x16& a, const float* fin, int sub, int J, int m0, int rl0, int lane, char* ws) {
  const int l31 = lane & 31, h = lane >> 5;
  const int rl = rl0 + l31, s = m0 + rl;
  float mu = fin[rl], rs = fin[128 + rl];
  if (KIND == 0) rs *= QSCALE;
  if (KIND == 2) {
    float r2 = rs * rs;
    rs *= __builtin_amdgcn_rsqf((1.0f - LN_EPS * r2) + LN_EPS);
  }
  float v[16];
#pragma unroll
  for (int j = 0; j < 4; ++j) {
    f32x4 c = *(const f32x4*)(fin + 256 + sub * 32 + 8 * j + 4 * h);
#pragma unroll
    for (int i = 0; i < 4; ++i) v[4 * j + i] = (a[4 * j + i] - mu * c[i]) * rs;
  }
  if (KIND == 2) {
#pragma unroll
    for (int i = 0; i < 16; ++i) v[i] = gelu_tanh(v[i]);
  }
#pragma unroll
  for (int g = 0; g < 2; ++g) {
    u32x4 o = pack8(v + 8 * g);
    char* dst;
    if (KIND == 0) dst = ws + OFF_Q + (size_t)(2 * J + g) * (S_ * 32) + (unsigned)(s * 32 + h * 16);
    else if (KIND == 1) dst = ws + OFF_K + (size_t)(2 * J + g) * (S_ * 32) + (unsigned)((s >> 5) * 1024 + h * 512 + (s & 31) * 16);
    else dst = ws + OFF_HB + (size_t)(((4 * J + 2 * g + h) * HB_ROWS + s + 2) * 16);
    *(u32x4*)dst = o;
  }
}
DEV void k1_epi_v(const f32x16& a, const float* fin, int sub, int J, int m0, int rl0, int lane, char* ws) {
  const int l31 = lane & 31, h = lane >> 5;
  const float cs = fin[256 + sub * 32 + l31];
  float v[16];
#pragma unroll
  for (int j = 0; j < 4; ++j) {
    f32x4 mu = *(const f32x4*)(fin + rl0 + 8 * j + 4 * h), rs = *(const f32x4*)(fin + 128 + rl0 + 8 * j + 4 * h);
#pragma unroll
    for (int i = 0; i < 4; ++i) v[4 * j + i] = (a[4 * j + i] - mu[i] * cs) * rs[i];
  }
  const int n = J * 32 + l31, head = n >> 4, d = n & 15, tile = (m0 + rl0) >> 5;
  char* base = ws + OFF_V + (size_t)head * (S_ * 32) + tile * 1024;
#pragma unroll
  for (int s = 0; s < 2; ++s) {
    u32x4 o;
#pragma unroll
    for (int j = 0; j < 4; ++j) o[j] = pk_bf16(v[8 * s + 2 * j], v[8 * s + 2 * j + 1]);
    *(u32x4*)(base + (s * 2 + h) * 256 + d * 16) = o;
  }
}

template <int TYPE>
DEV void k1_compute(char* lds, int wm, int wn, int nt, int m0, char* ws STAMP_ARG) {
  const unsigned lane = fresh_lane();
  const unsigned l31 = lane & 31, h = lane >> 5, blk = (lane >> 4) & 1, q = (lane & 15) >> 2, p = lane & 3;
  const unsigned mask = wm ^ wn;
  const unsigned row = wm * 64 + l31;
  unsigned ao[4], wb[2][2];
#pragma unroll
  for (int j = 0; j < 4; ++j) ao[j] = row * 128 + (((2 * (j ^ mask) + h) ^ ((row >> 1) & 7)) << 4);
  const unsigned cpart = (2 * blk + (p >> 1)) * 16 + 8 * (p & 1);
#pragma unroll
  for (int a = 0; a < 2; ++a) {
    const unsigned w = K1_A + (8 * h + q) * 256 + (((wn + 2 * a) ^ q) << 6) + cpart;
    wb[0][a] = w + mask * 4096; wb[1][a] = w - mask * 4096;
  }
  f32x16 acc[2][2] = {};
  float s1[2][2] = {}, s2[2][2] = {}, cs[2][2] = {};
  bf16x8 xf[4][2], wf[4][2];
  auto rd = [&](int t, int j) {
    const char* sb = lds + (t & 3) * K1_ST;
    xf[j][0] = *(const bf16x8*)(sb + ao[j]);
    wf[j][0] = tr_frag(sb + wb[j & 1][0] + j * 4096, sb + wb[j & 1][0] + j * 4096 + 1024);
    wf[j][1] = tr_frag(sb + wb[j & 1][1] + j * 4096, sb + wb[j & 1][1] + j * 4096 + 1024);
    xf[j][1] = *(const bf16x8*)(sb + ao[j] + 4096);
  };
  auto mm = [&](int j) {
#pragma unroll
    for (int r = 0; r < 2; ++r)
#pragma unroll
      for (int a = 0; a < 2; ++a)
        acc[r][a] = TYPE == 2 ? mfma32(xf[j][r], wf[j][a], acc[r][a]) : mfma32(wf[j][a], xf[j][r], acc[r][a]);
    __builtin_amdgcn_sched_group_barrier(0x008, 1, 0);
    __builtin_amdgcn_sched_group_barrier(0x100, 3, 0);
    __builtin_amdgcn_sched_group_barrier(0x008, 1, 0);
    __builtin_amdgcn_sched_group_barrier(0x100, 3, 0);
    __builtin_amdgcn_sched_group_barrier(0x008, 2, 0);
  };
  auto dots = [&](int j) {
#pragma unroll
    for (int i = 0; i < 4; ++i) {
#pragma unroll
      for (int r = 0; r < 2; ++r) {
        unsigned u = __builtin_bit_cast(u32x4, xf[j][r])[i];
        s1[r][i & 1] = dot2(u, 0x3f803f80u, s1[r][i & 1]); s2[r][i & 1] = dot2(u, u, s2[r][i & 1]);
      }
#pragma unroll
      for (int a = 0; a < 2; ++a) cs[a][i & 1] = dot2(__builtin_bit_cast(u32x4, wf[j + 1][a])[i], 0x3f803f80u, cs[a][i & 1]);
    }
    asm volatile("" : "+v"(s1[0][0]), "+v"(s1[0][1]), "+v"(s1[1][0]), "+v"(s1[1][1]), "+v"(s2[0][0]), "+v"(s2[0][1]), "+v"(s2[1][0]), "+v"(s2[1][1]),
                      "+v"(cs[0][0]), "+v"(cs[0][1]), "+v"(cs[1][0]), "+v"(cs[1][1]));
  };
#pragma unroll
  for (int t = 0; t < 8; ++t) {
    WAIT_L(0);
    BARRIER_NOWAIT();
    STAMP(4 + t);
    rd(t, 0);
    if (t > 0) { mm(3); SCHED_FENCE(); dots(2); }
    SCHED_FENCE();
    rd(t, 1);
    mm(0);
    SCHED_FENCE();
    rd(t, 2);
    mm(1);
    SCHED_FENCE();
    dots(0);
    SCHED_FENCE();
    rd(t, 3);
    mm(2);
    SCHED_FENCE();
  }
  mm(3);
  SCHED_FENCE();
  dots(2);
  STAMP(40);
  float* rsb = (float*)(lds + K1_X);
  float* csb = rsb + 512;
  float* fin = csb + 256;
#pragma unroll
  for (int r = 0; r < 2; ++r) {
    float a = xhalf_sum(s1[r][0] + s1[r][1]), b = xhalf_sum(s2[r][0] + s2[r][1]), c = xhalf_sum(cs[r][0] + cs[r][1]);
    if (h == 0) *(f32x2*)(rsb + (wn * 128 + wm * 64 + r * 32 + l31) * 2) = f32x2{a, b};
    else csb[wm * 128 + (wn + 2 * r) * 32 + l31] = c;
  }
  RAW_BARRIER();
  {
    const int i = wm * 64 + lane;
    f32x2 a = *(const f32x2*)(rsb + i * 2), c = *(const f32x2*)(rsb + (128 + i) * 2);
    float mu = (a[0] + c[0]) * (1.0f / D_);
    float var = fmaxf((a[1] + c[1]) * (1.0f / D_) - mu * mu, 0.f);
    float rs = __builtin_amdgcn_rsqf(var + LN_EPS);
    float cc = csb[i] + csb[128 + i];
    fin[wn * 256 + i] = wn ? cc : mu;
    if (wn == 0) {
      fin[128 + i] = rs;
      if (nt == 0) *(f32x2*)(ws + OFF_ST0 + (size_t)(m0 + i) * 8) = f32x2{mu, rs};
    }
  }
  RAW_BARRIER();
  const int c = nt >> 1;
#pragma unroll
  for (int r = 0; r < 2; ++r) {
    const int rl0 = wm * 64 + r * 32;
    if (TYPE == 0) {
      k1_epi_s<0>(acc[r][0], fin, wn, 2 * c + wn, m0, rl0, lane, ws);
      k1_epi_s<2>(acc[r][1], fin, wn + 2, 2 * c + wn, m0, rl0, lane, ws);
    } else if (TYPE == 1) {
      k1_epi_s<1>(acc[r][0], fin, wn, 4 * c + wn, m0, rl0, lane, ws);
      k1_epi_s<1>(acc[r][1], fin, wn + 2, 4 * c + wn + 2, m0, rl0, lane, ws);
    } else {
      k1_epi_v(acc[r][0], fin, wn, 4 * (c - 4) + wn, m0, rl0, lane, ws);
      k1_epi_v(acc[r][1], fin, wn + 2, 4 * (c - 4) + wn + 2, m0, rl0, lane, ws);
    }
  }
  STAMP(41); RSTAMP(42); STAMP_FLUSH(4);
}

__global__ __launch_bounds__(512, 2) void k1(const u16* __restrict__ x, const u16* __restrict__ wqkv, const u16* __restrict__ w1,
                                             const u16* __restrict__ w3, const u16* __restrict__ w2, const u16* __restrict__ w4,
                                             const u16* __restrict__ wo, char* __restrict__ ws) {
#if defined(__HIP_DEVICE_COMPILE__)
  extern __shared__ __attribute__((aligned(16))) char lds[];
  STAMP_DECL
  RSTAMP(0); STAMP(1);
  const int b = blockIdx.x, xcd = b & 7, bi = b >> 3;
  const int mt = (xcd & 3) * 4 + (bi & 3), nt = (xcd >> 2) * 8 + (bi >> 2);
  const int m0 = mt * 128;
  const int tid = threadIdx.x, lane = tid & 63;
  const int wid = __builtin_amdgcn_readfirstlane(tid >> 6);
  if (__builtin_expect(wid < 4, 1)) {
    const int lw = wid;
    auto xrs = mk_rsrc(x, S_ * D_ * 2);
    unsigned va[4];
#pragma unroll
    for (int i = 0; i < 4; ++i) {
      unsigned bb = (lw + 4 * i) * 1024 + lane * 16, row = bb >> 7, cp = (bb >> 4) & 7;
      va[i] = (m0 + row) * 1024 + ((cp ^ ((row >> 1) & 7)) << 4);
    }
    const unsigned kl = lane >> 4, seg = ((lane >> 2) & 3) ^ kl;
    const int c = nt >> 1;
    const bool fromw1 = !(nt & 1) && seg >= 2;
    const unsigned wstride = fromw1 ? 1024u : 3072u;
    const char* wbase = fromw1 ? (const char*)w1 + c * 128 + (seg - 2) * 64
                               : (const char*)wqkv + ((nt & 1) ? 1024 + c * 256 : c * 128) + seg * 64;
    wbase += (lane & 3) * 16 + (lw * 4 + kl) * wstride;
#define K1_ROT (nt + 2 * mt)
    const int rot = K1_ROT;
    auto issue = [&](int t) {
      char* sb = lds + (t & 3) * K1_ST;
      const unsigned kt = (t + rot) & 7;
#pragma unroll
      for (int i = 0; i < 4; ++i) {
        dma16(xrs, sb + (lw + 4 * i) * 1024, va[i], kt * 128);
        __builtin_amdgcn_global_load_lds((const unsigned*)(wbase + (size_t)((kt * 64 + i * 16) * wstride)),
                                         (__attribute__((address_space(3))) unsigned*)(sb + K1_A + (lw + 4 * i) * 1024), 16, 0, 0);
      }
    };
    STAMP(2);
    issue(0); STAMP(3); issue(1);
    if (tid == 0) *(unsigned*)(lds + K1_P3 + 2048) = 0;
    STAMP(4);
    WAIT_V(8);  STAMP(6); BARRIER_NOWAIT(); issue(2); issue(3);
    WAIT_V(16); STAMP(7); BARRIER_NOWAIT(); issue(4);
    WAIT_V(16); STAMP(8); BARRIER_NOWAIT(); issue(5);
    WAIT_V(16); STAMP(9); BARRIER_NOWAIT(); issue(6);
    WAIT_V(16); STAMP(10); BARRIER_NOWAIT(); issue(7);
    const void* mp; unsigned mbytes, mv, mstride;
    if (b < 88) {
      int cg = b % 11, rg = b / 11;
      mp = w3; mbytes = D_ * F_ * 2; mstride = 4 * F_ * 2;
      mv = (rg * 64 + lw * 16 + (lane >> 4)) * (F_ * 2) + cg * 256 + (lane & 15) * 16;
    } else {
      const int it = (b - 88) * 4 + lw;
      mstride = 8192;
      if (it < 16) { mp = w2; mbytes = D_ * D_ * 2; mv = it * 32768 + lane * 128; }
      else if (it < 32) { mp = wo; mbytes = D_ * D_ * 2; mv = (it - 16) * 32768 + lane * 128; }
      else { mp = w4; mbytes = D_ * F_ * 2; mv = (it - 32) * 32768 + lane * 128; }
    }
    auto mrs = mk_rsrc(mp, mbytes);
    u32x4 md[4];
#pragma unroll
    for (int i = 0; i < 4; ++i) md[i] = __builtin_amdgcn_raw_buffer_load_b128(mrs, mv, i * mstride, 0);
    WAIT_V(20); STAMP(11); BARRIER_NOWAIT();
    WAIT_V(12); STAMP(12); BARRIER_NOWAIT();
    WAIT_V(4);  STAMP(13); BARRIER_NOWAIT();
    if (b < 88) {
      float cs[8];
#pragma unroll
      for (int j = 0; j < 8; ++j) cs[j] = 0.f;
#pragma unroll
      for (int i = 0; i < 4; ++i)
#pragma unroll
        for (int j = 0; j < 4; ++j) { cs[2 * j] += bf_lo(md[i][j]); cs[2 * j + 1] += bf_hi(md[i][j]); }
#pragma unroll
      for (int j = 0; j < 8; ++j) { cs[j] += __shfl_xor(cs[j], 16); cs[j] = xhalf_sum(cs[j]); }
      float* p3 = (float*)(lds + K1_P3);
      if (lane < 16) {
        *(f32x4*)(p3 + lw * 128 + lane * 8) = f32x4{cs[0], cs[1], cs[2], cs[3]};
        *(f32x4*)(p3 + lw * 128 + lane * 8 + 4) = f32x4{cs[4], cs[5], cs[6], cs[7]};
      }
      WAIT_L(0);
      unsigned old = 0;
      if (lane == 0) old = __hip_atomic_fetch_add((unsigned*)(lds + K1_P3 + 2048), 1u, __ATOMIC_RELAXED, __HIP_MEMORY_SCOPE_WORKGROUP);
      old = __builtin_amdgcn_readfirstlane(old);
      if (old == 3) {
        f32x2 t = {0.f, 0.f};
#pragma unroll
        for (int w = 0; w < 4; ++w) t += *(const f32x2*)(p3 + w * 128 + lane * 2);
        *(f32x2*)((float*)(ws + OFF_CS3) + (b / 11) * CS3_LD + (b % 11) * 128 + lane * 2) = t;
      }
    } else {
#pragma unroll
      for (int i = 0; i < 4; ++i) asm volatile("" ::"v"(md[i]));
      if (b == 200 && lw == 0) ((unsigned*)(ws + OFF_CNT))[lane * 64] = 0;
      if (b == 201) {
        u32x4 z = {0, 0, 0, 0};
        *(u32x4*)(ws + OFF_HB + ((size_t)lane * HB_ROWS + (lw < 2 ? lw : S_ + lw)) * 16) = z;
      }
    }
    STAMP(41); RSTAMP(42); STAMP_FLUSH(0);
    return;
  }
  const int wm = wid & 1, wn = (wid >> 1) & 1;
  if (!(nt & 1)) k1_compute<0>(lds, wm, wn, nt, m0, ws STAMP_PASS);
  else if (nt < 8) k1_compute<1>(lds, wm, wn, nt, m0, ws STAMP_PASS);
  else k1_compute<2>(lds, wm, wn, nt, m0, ws STAMP_PASS);
#endif
}
#define ATTN_ASM \
  "v_mov_b32 v36, %[negm]\n\t" \
  "v_mov_b32 v37, %[negm]\n\t" \
  "v_mov_b32 v38, %[negm]\n\t" \
  "v_mov_b32 v39, %[negm]\n\t" \
  "v_mov_b32 v40, %[negm]\n\t" \
  "v_mov_b32 v41, %[negm]\n\t" \
  "v_mov_b32 v42, %[negm]\n\t" \
  "v_mov_b32 v43, %[negm]\n\t" \
  "v_mov_b32 v44, %[negm]\n\t" \
  "v_mov_b32 v45, %[negm]\n\t" \
  "v_mov_b32 v46, %[negm]\n\t" \
  "v_mov_b32 v47, %[negm]\n\t" \
  "v_mov_b32 v48, %[negm]\n\t" \
  "v_mov_b32 v49, %[negm]\n\t" \
  "v_mov_b32 v50, %[negm]\n\t" \
  "v_mov_b32 v51, %[negm]\n\t" \
  "v_mov_b32 v52, 0\n\t" \
  "v_mov_b32 v53, 0\n\t" \
  "v_mov_b32 v54, 0\n\t" \
  "v_mov_b32 v55, 0\n\t" \
  "v_mov_b32 v56, 0\n\t" \
  "v_mov_b32 v57, 0\n\t" \
  "v_mov_b32 v58, 0\n\t" \
  "v_mov_b32 v59, 0\n\t" \
  "v_mov_b32 v60, 0\n\t" \
  "v_mov_b32 v61, 0\n\t" \
  "v_mov_b32 v62, 0\n\t" \
  "v_mov_b32 v63, 0\n\t" \
  "v_mov_b32 v64, 0\n\t" \
  "v_mov_b32 v65, 0\n\t" \
  "v_mov_b32 v66, 0\n\t" \
  "v_mov_b32 v67, 0\n\t" \
  "v_mov_b32 v104, %[ones]\n\t" \
  "v_mov_b32 v108, %[ones]\n\t" \
  "v_mov_b32 v116, 0\n\t" \
  "v_mov_b32 v105, %[ones]\n\t" \
  "v_mov_b32 v109, %[ones]\n\t" \
  "v_mov_b32 v117, 0\n\t" \
  "v_mov_b32 v106, %[ones]\n\t" \
  "v_mov_b32 v110, %[ones]\n\t" \
  "v_mov_b32 v118, 0\n\t" \
  "v_mov_b32 v107, %[ones]\n\t" \
  "v_mov_b32 v111, %[ones]\n\t" \
  "v_mov_b32 v119, 0\n\t" \
  "s_mov_b32 %[scur], 0\n\t" \
  "s_mov_b32 %[snext], 0x4000\n\t" \
  "s_mov_b32 %[sfree], 0x8000\n\t" \
  "s_mov_b32 %[cnt], 8\n\t" \
  "ds_read_b128 v[100:103], %[lk]\n\t" \
  "s_waitcnt lgkmcnt(0)\n\t" \
  "v_mfma_f32_32x32x16_bf16 v[68:83], v[100:103], v[32:35], v[36:51]\n\t" \
  "ds_read_b128 v[100:103], %[lk] offset:1024\n\t" \
  "s_mov_b64 exec, %[vmask]\n\t" \
  "ds_read_b128 v[104:107], %[lv]\n\t" \
  "s_mov_b64 exec, -1\n\t" \
  "s_nop 7\n\t" \
  "1:\n\t" \
  "v_add_u32 v120, %[scur], %[lk]\n\t" \
  "v_add_u32 v121, %[snext], %[lk]\n\t" \
  "v_add_u32 v122, %[scur], %[lv]\n\t" \
  "v_add_u32 v123, %[snext], %[lv]\n\t" \
  "s_waitcnt lgkmcnt(1)\n\t" \
  "v_mfma_f32_32x32x16_bf16 v[84:99], v[100:103], v[32:35], v[36:51]\n\t" \
  "ds_read_b128 v[100:103], v120 offset:2048\n\t" \
  "v_mfma_f32_32x32x16_bf16 v[52:67], v[108:111], v[116:119], v[52:67]\n\t" \
  "s_mov_b64 exec, %[vmask]\n\t" \
  "ds_read_b128 v[108:111], v122 offset:512\n\t" \
  "s_mov_b64 exec, -1\n\t" \
  "v_exp_f32 v112, v69\n\t" \
  "v_exp_f32 v68, v68\n\t" \
  "v_exp_f32 v113, v71\n\t" \
  "v_mov_b32_sdwa v112, v68 dst_sel:WORD_0 dst_unused:UNUSED_PRESERVE src0_sel:WORD_1\n\t" \
  "v_exp_f32 v70, v70\n\t" \
  "v_exp_f32 v114, v73\n\t" \
  "v_mov_b32_sdwa v113, v70 dst_sel:WORD_0 dst_unused:UNUSED_PRESERVE src0_sel:WORD_1\n\t" \
  "v_exp_f32 v72, v72\n\t" \
  "v_exp_f32 v115, v75\n\t" \
  "v_mov_b32_sdwa v114, v72 dst_sel:WORD_0 dst_unused:UNUSED_PRESERVE src0_sel:WORD_1\n\t" \
  "v_exp_f32 v74, v74\n\t" \
  "v_exp_f32 v116, v77\n\t" \
  "v_mov_b32_sdwa v115, v74 dst_sel:WORD_0 dst_unused:UNUSED_PRESERVE src0_sel:WORD_1\n\t" \
  "v_exp_f32 v76, v76\n\t" \
  "v_exp_f32 v117, v79\n\t" \
  "s_waitcnt lgkmcnt(2)\n\t" \
  "v_mfma_f32_32x32x16_bf16 v[52:67], v[104:107], v[112:115], v[52:67]\n\t" \
  "s_mov_b64 exec, %[vmask]\n\t" \
  "ds_read_b128 v[104:107], v122 offset:1024\n\t" \
  "s_mov_b64 exec, -1\n\t" \
  "v_mov_b32_sdwa v116, v76 dst_sel:WORD_0 dst_unused:UNUSED_PRESERVE src0_sel:WORD_1\n\t" \
  "v_exp_f32 v78, v78\n\t" \
  "v_exp_f32 v118, v81\n\t" \
  "v_mov_b32_sdwa v117, v78 dst_sel:WORD_0 dst_unused:UNUSED_PRESERVE src0_sel:WORD_1\n\t" \
  "v_exp_f32 v80, v80\n\t" \
  "v_exp_f32 v119, v83\n\t" \
  "v_mov_b32_sdwa v118, v80 dst_sel:WORD_0 dst_unused:UNUSED_PRESERVE src0_sel:WORD_1\n\t" \
  "v_exp_f32 v82, v82\n\t" \
  "s_nop 0\n\t" \
  "v_mov_b32_sdwa v119, v82 dst_sel:WORD_0 dst_unused:UNUSED_PRESERVE src0_sel:WORD_1\n\t" \
  "s_waitcnt lgkmcnt(1)\n\t" \
  "v_mfma_f32_32x32x16_bf16 v[68:83], v[100:103], v[32:35], v[36:51]\n\t" \
  "ds_read_b128 v[100:103], v120 offset:3072\n\t" \
  "v_mfma_f32_32x32x16_bf16 v[52:67], v[108:111], v[116:119], v[52:67]\n\t" \
  "s_mov_b64 exec, %[vmask]\n\t" \
  "ds_read_b128 v[108:111], v122 offset:1536\n\t" \
  "s_mov_b64 exec, -1\n\t" \
  "v_exp_f32 v112, v85\n\t" \
  "v_exp_f32 v84, v84\n\t" \
  "v_exp_f32 v113, v87\n\t" \
  "v_mov_b32_sdwa v112, v84 dst_sel:WORD_0 dst_unused:UNUSED_PRESERVE src0_sel:WORD_1\n\t" \
  "v_exp_f32 v86, v86\n\t" \
  "v_exp_f32 v114, v89\n\t" \
  "v_mov_b32_sdwa v113, v86 dst_sel:WORD_0 dst_unused:UNUSED_PRESERVE src0_sel:WORD_1\n\t" \
  "v_exp_f32 v88, v88\n\t" \
  "v_exp_f32 v115, v91\n\t" \
  "v_mov_b32_sdwa v114, v88 dst_sel:WORD_0 dst_unused:UNUSED_PRESERVE src0_sel:WORD_1\n\t" \
  "v_exp_f32 v90, v90\n\t" \
  "v_exp_f32 v116, v93\n\t" \
  "v_mov_b32_sdwa v115, v90 dst_sel:WORD_0 dst_unused:UNUSED_PRESERVE src0_sel:WORD_1\n\t" \
  "v_exp_f32 v92, v92\n\t" \
  "v_exp_f32 v117, v95\n\t" \
  "s_waitcnt lgkmcnt(2)\n\t" \
  "v_mfma_f32_32x32x16_bf16 v[52:67], v[104:107], v[112:115], v[52:67]\n\t" \
  "s_mov_b64 exec, %[vmask]\n\t" \
  "ds_read_b128 v[104:107], v122 offset:2048\n\t" \
  "s_mov_b64 exec, -1\n\t" \
  "v_mov_b32_sdwa v116, v92 dst_sel:WORD_0 dst_unused:UNUSED_PRESERVE src0_sel:WORD_1\n\t" \
  "v_exp_f32 v94, v94\n\t" \
  "v_exp_f32 v118, v97\n\t" \
  "v_mov_b32_sdwa v117, v94 dst_sel:WORD_0 dst_unused:UNUSED_PRESERVE src0_sel:WORD_1\n\t" \
  "v_exp_f32 v96, v96\n\t" \
  "v_exp_f32 v119, v99\n\t" \
  "v_mov_b32_sdwa v118, v96 dst_sel:WORD_0 dst_unused:UNUSED_PRESERVE src0_sel:WORD_1\n\t" \
  "v_exp_f32 v98, v98\n\t" \
  "s_nop 0\n\t" \
  "v_mov_b32_sdwa v119, v98 dst_sel:WORD_0 dst_unused:UNUSED_PRESERVE src0_sel:WORD_1\n\t" \
  "s_waitcnt lgkmcnt(1)\n\t" \
  "v_mfma_f32_32x32x16_bf16 v[84:99], v[100:103], v[32:35], v[36:51]\n\t" \
  "ds_read_b128 v[100:103], v120 offset:4096\n\t" \
  "v_mfma_f32_32x32x16_bf16 v[52:67], v[108:111], v[116:119], v[52:67]\n\t" \
  "s_mov_b64 exec, %[vmask]\n\t" \
  "ds_read_b128 v[108:111], v122 offset:2560\n\t" \
  "s_mov_b64 exec, -1\n\t" \
  "v_exp_f32 v112, v69\n\t" \
  "v_exp_f32 v68, v68\n\t" \
  "v_exp_f32 v113, v71\n\t" \
  "v_mov_b32_sdwa v112, v68 dst_sel:WORD_0 dst_unused:UNUSED_PRESERVE src0_sel:WORD_1\n\t" \
  "v_exp_f32 v70, v70\n\t" \
  "v_exp_f32 v114, v73\n\t" \
  "v_mov_b32_sdwa v113, v70 dst_sel:WORD_0 dst_unused:UNUSED_PRESERVE src0_sel:WORD_1\n\t" \
  "v_exp_f32 v72, v72\n\t" \
  "v_exp_f32 v115, v75\n\t" \
  "v_mov_b32_sdwa v114, v72 dst_sel:WORD_0 dst_unused:UNUSED_PRESERVE src0_sel:WORD_1\n\t" \
  "v_exp_f32 v74, v74\n\t" \
  "v_exp_f32 v116, v77\n\t" \
  "v_mov_b32_sdwa v115, v74 dst_sel:WORD_0 dst_unused:UNUSED_PRESERVE src0_sel:WORD_1\n\t" \
  "v_exp_f32 v76, v76\n\t" \
  "v_exp_f32 v117, v79\n\t" \
  "s_waitcnt lgkmcnt(2)\n\t" \
  "v_mfma_f32_32x32x16_bf16 v[52:67], v[104:107], v[112:115], v[52:67]\n\t" \
  "s_mov_b64 exec, %[vmask]\n\t" \
  "ds_read_b128 v[104:107], v122 offset:3072\n\t" \
  "s_mov_b64 exec, -1\n\t" \
  "v_mov_b32_sdwa v116, v76 dst_sel:WORD_0 dst_unused:UNUSED_PRESERVE src0_sel:WORD_1\n\t" \
  "v_exp_f32 v78, v78\n\t" \
  "v_exp_f32 v118, v81\n\t" \
  "v_mov_b32_sdwa v117, v78 dst_sel:WORD_0 dst_unused:UNUSED_PRESERVE src0_sel:WORD_1\n\t" \
  "v_exp_f32 v80, v80\n\t" \
  "v_exp_f32 v119, v83\n\t" \
  "v_mov_b32_sdwa v118, v80 dst_sel:WORD_0 dst_unused:UNUSED_PRESERVE src0_sel:WORD_1\n\t" \
  "v_exp_f32 v82, v82\n\t" \
  "s_nop 0\n\t" \
  "v_mov_b32_sdwa v119, v82 dst_sel:WORD_0 dst_unused:UNUSED_PRESERVE src0_sel:WORD_1\n\t" \
  "s_waitcnt lgkmcnt(1)\n\t" \
  "v_mfma_f32_32x32x16_bf16 v[68:83], v[100:103], v[32:35], v[36:51]\n\t" \
  "ds_read_b128 v[100:103], v120 offset:5120\n\t" \
  "v_mfma_f32_32x32x16_bf16 v[52:67], v[108:111], v[116:119], v[52:67]\n\t" \
  "s_mov_b64 exec, %[vmask]\n\t" \
  "ds_read_b128 v[108:111], v122 offset:3584\n\t" \
  "s_mov_b64 exec, -1\n\t" \
  "v_exp_f32 v112, v85\n\t" \
  "v_exp_f32 v84, v84\n\t" \
  "v_exp_f32 v113, v87\n\t" \
  "v_mov_b32_sdwa v112, v84 dst_sel:WORD_0 dst_unused:UNUSED_PRESERVE src0_sel:WORD_1\n\t" \
  "v_exp_f32 v86, v86\n\t" \
  "v_exp_f32 v114, v89\n\t" \
  "v_mov_b32_sdwa v113, v86 dst_sel:WORD_0 dst_unused:UNUSED_PRESERVE src0_sel:WORD_1\n\t" \
  "v_exp_f32 v88, v88\n\t" \
  "v_exp_f32 v115, v91\n\t" \
  "v_mov_b32_sdwa v114, v88 dst_sel:WORD_0 dst_unused:UNUSED_PRESERVE src0_sel:WORD_1\n\t" \
  "v_exp_f32 v90, v90\n\t" \
  "v_exp_f32 v116, v93\n\t" \
  "v_mov_b32_sdwa v115, v90 dst_sel:WORD_0 dst_unused:UNUSED_PRESERVE src0_sel:WORD_1\n\t" \
  "v_exp_f32 v92, v92\n\t" \
  "v_exp_f32 v117, v95\n\t" \
  "s_waitcnt lgkmcnt(2)\n\t" \
  "v_mfma_f32_32x32x16_bf16 v[52:67], v[104:107], v[112:115], v[52:67]\n\t" \
  "s_mov_b64 exec, %[vmask]\n\t" \
  "ds_read_b128 v[104:107], v122 offset:4096\n\t" \
  "s_mov_b64 exec, -1\n\t" \
  "v_mov_b32_sdwa v116, v92 dst_sel:WORD_0 dst_unused:UNUSED_PRESERVE src0_sel:WORD_1\n\t" \
  "v_exp_f32 v94, v94\n\t" \
  "v_exp_f32 v118, v97\n\t" \
  "v_mov_b32_sdwa v117, v94 dst_sel:WORD_0 dst_unused:UNUSED_PRESERVE src0_sel:WORD_1\n\t" \
  "v_exp_f32 v96, v96\n\t" \
  "v_exp_f32 v119, v99\n\t" \
  "v_mov_b32_sdwa v118, v96 dst_sel:WORD_0 dst_unused:UNUSED_PRESERVE src0_sel:WORD_1\n\t" \
  "v_exp_f32 v98, v98\n\t" \
  "s_nop 0\n\t" \
  "v_mov_b32_sdwa v119, v98 dst_sel:WORD_0 dst_unused:UNUSED_PRESERVE src0_sel:WORD_1\n\t" \
  "s_waitcnt lgkmcnt(1)\n\t" \
  "v_mfma_f32_32x32x16_bf16 v[84:99], v[100:103], v[32:35], v[36:51]\n\t" \
  "ds_read_b128 v[100:103], v120 offset:6144\n\t" \
  "v_mfma_f32_32x32x16_bf16 v[52:67], v[108:111], v[116:119], v[52:67]\n\t" \
  "s_mov_b64 exec, %[vmask]\n\t" \
  "ds_read_b128 v[108:111], v122 offset:4608\n\t" \
  "s_mov_b64 exec, -1\n\t" \
  "v_exp_f32 v112, v69\n\t" \
  "v_exp_f32 v68, v68\n\t" \
  "v_exp_f32 v113, v71\n\t" \
  "v_mov_b32_sdwa v112, v68 dst_sel:WORD_0 dst_unused:UNUSED_PRESERVE src0_sel:WORD_1\n\t" \
  "v_exp_f32 v70, v70\n\t" \
  "v_exp_f32 v114, v73\n\t" \
  "v_mov_b32_sdwa v113, v70 dst_sel:WORD_0 dst_unused:UNUSED_PRESERVE src0_sel:WORD_1\n\t" \
  "v_exp_f32 v72, v72\n\t" \
  "v_exp_f32 v115, v75\n\t" \
  "v_mov_b32_sdwa v114, v72 dst_sel:WORD_0 dst_unused:UNUSED_PRESERVE src0_sel:WORD_1\n\t" \
  "v_exp_f32 v74, v74\n\t" \
  "v_exp_f32 v116, v77\n\t" \
  "v_mov_b32_sdwa v115, v74 dst_sel:WORD_0 dst_unused:UNUSED_PRESERVE src0_sel:WORD_1\n\t" \
  "v_exp_f32 v76, v76\n\t" \
  "v_exp_f32 v117, v79\n\t" \
  "s_waitcnt lgkmcnt(2)\n\t" \
  "v_mfma_f32_32x32x16_bf16 v[52:67], v[104:107], v[112:115], v[52:67]\n\t" \
  "s_mov_b64 exec, %[vmask]\n\t" \
  "ds_read_b128 v[104:107], v122 offset:5120\n\t" \
  "s_mov_b64 exec, -1\n\t" \
  "v_mov_b32_sdwa v116, v76 dst_sel:WORD_0 dst_unused:UNUSED_PRESERVE src0_sel:WORD_1\n\t" \
  "v_exp_f32 v78, v78\n\t" \
  "v_exp_f32 v118, v81\n\t" \
  "v_mov_b32_sdwa v117, v78 dst_sel:WORD_0 dst_unused:UNUSED_PRESERVE src0_sel:WORD_1\n\t" \
  "v_exp_f32 v80, v80\n\t" \
  "v_exp_f32 v119, v83\n\t" \
  "v_mov_b32_sdwa v118, v80 dst_sel:WORD_0 dst_unused:UNUSED_PRESERVE src0_sel:WORD_1\n\t" \
  "v_exp_f32 v82, v82\n\t" \
  "s_waitcnt vmcnt(0)\n\t" \
  "s_barrier\n\t" \
  "s_add_u32 m0, %[sfree], %[m0k]\n\t" \
  "s_nop 0\n\t" \
  "buffer_load_dwordx4 %[goff], %[rk], %[ssrc] offen lds\n\t" \
  "s_add_u32 m0, m0, 0x2000\n\t" \
  "s_nop 0\n\t" \
  "buffer_load_dwordx4 %[goff], %[rv], %[ssrc] offen lds\n\t" \
  "s_add_u32 %[ssrc], %[ssrc], 0x2000\n\t" \
  "v_mov_b32_sdwa v119, v82 dst_sel:WORD_0 dst_unused:UNUSED_PRESERVE src0_sel:WORD_1\n\t" \
  "s_waitcnt lgkmcnt(1)\n\t" \
  "v_mfma_f32_32x32x16_bf16 v[68:83], v[100:103], v[32:35], v[36:51]\n\t" \
  "ds_read_b128 v[100:103], v120 offset:7168\n\t" \
  "v_mfma_f32_32x32x16_bf16 v[52:67], v[108:111], v[116:119], v[52:67]\n\t" \
  "s_mov_b64 exec, %[vmask]\n\t" \
  "ds_read_b128 v[108:111], v122 offset:5632\n\t" \
  "s_mov_b64 exec, -1\n\t" \
  "v_exp_f32 v112, v85\n\t" \
  "v_exp_f32 v84, v84\n\t" \
  "v_exp_f32 v113, v87\n\t" \
  "v_mov_b32_sdwa v112, v84 dst_sel:WORD_0 dst_unused:UNUSED_PRESERVE src0_sel:WORD_1\n\t" \
  "v_exp_f32 v86, v86\n\t" \
  "v_exp_f32 v114, v89\n\t" \
  "v_mov_b32_sdwa v113, v86 dst_sel:WORD_0 dst_unused:UNUSED_PRESERVE src0_sel:WORD_1\n\t" \
  "v_exp_f32 v88, v88\n\t" \
  "v_exp_f32 v115, v91\n\t" \
  "v_mov_b32_sdwa v114, v88 dst_sel:WORD_0 dst_unused:UNUSED_PRESERVE src0_sel:WORD_1\n\t" \
  "v_exp_f32 v90, v90\n\t" \
  "v_exp_f32 v116, v93\n\t" \
  "v_mov_b32_sdwa v115, v90 dst_sel:WORD_0 dst_unused:UNUSED_PRESERVE src0_sel:WORD_1\n\t" \
  "v_exp_f32 v92, v92\n\t" \
  "v_exp_f32 v117, v95\n\t" \
  "s_waitcnt lgkmcnt(2)\n\t" \
  "v_mfma_f32_32x32x16_bf16 v[52:67], v[104:107], v[112:115], v[52:67]\n\t" \
  "s_mov_b64 exec, %[vmask]\n\t" \
  "ds_read_b128 v[104:107], v122 offset:6144\n\t" \
  "s_mov_b64 exec, -1\n\t" \
  "v_mov_b32_sdwa v116, v92 dst_sel:WORD_0 dst_unused:UNUSED_PRESERVE src0_sel:WORD_1\n\t" \
  "v_exp_f32 v94, v94\n\t" \
  "v_exp_f32 v118, v97\n\t" \
  "v_mov_b32_sdwa v117, v94 dst_sel:WORD_0 dst_unused:UNUSED_PRESERVE src0_sel:WORD_1\n\t" \
  "v_exp_f32 v96, v96\n\t" \
  "v_exp_f32 v119, v99\n\t" \
  "v_mov_b32_sdwa v118, v96 dst_sel:WORD_0 dst_unused:UNUSED_PRESERVE src0_sel:WORD_1\n\t" \
  "v_exp_f32 v98, v98\n\t" \
  "s_nop 0\n\t" \
  "v_mov_b32_sdwa v119, v98 dst_sel:WORD_0 dst_unused:UNUSED_PRESERVE src0_sel:WORD_1\n\t" \
  "s_waitcnt lgkmcnt(1)\n\t" \
  "v_mfma_f32_32x32x16_bf16 v[84:99], v[100:103], v[32:35], v[36:51]\n\t" \
  "ds_read_b128 v[100:103], v121 offset:0\n\t" \
  "v_mfma_f32_32x32x16_bf16 v[52:67], v[108:111], v[116:119], v[52:67]\n\t" \
  "s_mov_b64 exec, %[vmask]\n\t" \
  "ds_read_b128 v[108:111], v122 offset:6656\n\t" \
  "s_mov_b64 exec, -1\n\t" \
  "v_exp_f32 v112, v69\n\t" \
  "v_exp_f32 v68, v68\n\t" \
  "v_exp_f32 v113, v71\n\t" \
  "v_mov_b32_sdwa v112, v68 dst_sel:WORD_0 dst_unused:UNUSED_PRESERVE src0_sel:WORD_1\n\t" \
  "v_exp_f32 v70, v70\n\t" \
  "v_exp_f32 v114, v73\n\t" \
  "v_mov_b32_sdwa v113, v70 dst_sel:WORD_0 dst_unused:UNUSED_PRESERVE src0_sel:WORD_1\n\t" \
  "v_exp_f32 v72, v72\n\t" \
  "v_exp_f32 v115, v75\n\t" \
  "v_mov_b32_sdwa v114, v72 dst_sel:WORD_0 dst_unused:UNUSED_PRESERVE src0_sel:WORD_1\n\t" \
  "v_exp_f32 v74, v74\n\t" \
  "v_exp_f32 v116, v77\n\t" \
  "v_mov_b32_sdwa v115, v74 dst_sel:WORD_0 dst_unused:UNUSED_PRESERVE src0_sel:WORD_1\n\t" \
  "v_exp_f32 v76, v76\n\t" \
  "v_exp_f32 v117, v79\n\t" \
  "s_waitcnt lgkmcnt(2)\n\t" \
  "v_mfma_f32_32x32x16_bf16 v[52:67], v[104:107], v[112:115], v[52:67]\n\t" \
  "s_mov_b64 exec, %[vmask]\n\t" \
  "ds_read_b128 v[104:107], v122 offset:7168\n\t" \
  "s_mov_b64 exec, -1\n\t" \
  "v_mov_b32_sdwa v116, v76 dst_sel:WORD_0 dst_unused:UNUSED_PRESERVE src0_sel:WORD_1\n\t" \
  "v_exp_f32 v78, v78\n\t" \
  "v_exp_f32 v118, v81\n\t" \
  "v_mov_b32_sdwa v117, v78 dst_sel:WORD_0 dst_unused:UNUSED_PRESERVE src0_sel:WORD_1\n\t" \
  "v_exp_f32 v80, v80\n\t" \
  "v_exp_f32 v119, v83\n\t" \
  "v_mov_b32_sdwa v118, v80 dst_sel:WORD_0 dst_unused:UNUSED_PRESERVE src0_sel:WORD_1\n\t" \
  "v_exp_f32 v82, v82\n\t" \
  "s_nop 0\n\t" \
  "v_mov_b32_sdwa v119, v82 dst_sel:WORD_0 dst_unused:UNUSED_PRESERVE src0_sel:WORD_1\n\t" \
  "s_waitcnt lgkmcnt(1)\n\t" \
  "v_mfma_f32_32x32x16_bf16 v[68:83], v[100:103], v[32:35], v[36:51]\n\t" \
  "ds_read_b128 v[100:103], v121 offset:1024\n\t" \
  "v_mfma_f32_32x32x16_bf16 v[52:67], v[108:111], v[116:119], v[52:67]\n\t" \
  "s_mov_b64 exec, %[vmask]\n\t" \
  "ds_read_b128 v[108:111], v122 offset:7680\n\t" \
  "s_mov_b64 exec, -1\n\t" \
  "v_exp_f32 v112, v85\n\t" \
  "v_exp_f32 v84, v84\n\t" \
  "v_exp_f32 v113, v87\n\t" \
  "v_mov_b32_sdwa v112, v84 dst_sel:WORD_0 dst_unused:UNUSED_PRESERVE src0_sel:WORD_1\n\t" \
  "v_exp_f32 v86, v86\n\t" \
  "v_exp_f32 v114, v89\n\t" \
  "v_mov_b32_sdwa v113, v86 dst_sel:WORD_0 dst_unused:UNUSED_PRESERVE src0_sel:WORD_1\n\t" \
  "v_exp_f32 v88, v88\n\t" \
  "v_exp_f32 v115, v91\n\t" \
  "v_mov_b32_sdwa v114, v88 dst_sel:WORD_0 dst_unused:UNUSED_PRESERVE src0_sel:WORD_1\n\t" \
  "v_exp_f32 v90, v90\n\t" \
  "v_exp_f32 v116, v93\n\t" \
  "v_mov_b32_sdwa v115, v90 dst_sel:WORD_0 dst_unused:UNUSED_PRESERVE src0_sel:WORD_1\n\t" \
  "v_exp_f32 v92, v92\n\t" \
  "v_exp_f32 v117, v95\n\t" \
  "s_waitcnt lgkmcnt(2)\n\t" \
  "v_mfma_f32_32x32x16_bf16 v[52:67], v[104:107], v[112:115], v[52:67]\n\t" \
  "s_mov_b64 exec, %[vmask]\n\t" \
  "ds_read_b128 v[104:107], v123 offset:0\n\t" \
  "s_mov_b64 exec, -1\n\t" \
  "v_mov_b32_sdwa v116, v92 dst_sel:WORD_0 dst_unused:UNUSED_PRESERVE src0_sel:WORD_1\n\t" \
  "v_exp_f32 v94, v94\n\t" \
  "v_exp_f32 v118, v97\n\t" \
  "v_mov_b32_sdwa v117, v94 dst_sel:WORD_0 dst_unused:UNUSED_PRESERVE src0_sel:WORD_1\n\t" \
  "v_exp_f32 v96, v96\n\t" \
  "v_exp_f32 v119, v99\n\t" \
  "v_mov_b32_sdwa v118, v96 dst_sel:WORD_0 dst_unused:UNUSED_PRESERVE src0_sel:WORD_1\n\t" \
  "v_exp_f32 v98, v98\n\t" \
  "s_nop 0\n\t" \
  "v_mov_b32_sdwa v119, v98 dst_sel:WORD_0 dst_unused:UNUSED_PRESERVE src0_sel:WORD_1\n\t" \
  "s_mov_b32 %[stmp], %[scur]\n\t" \
  "s_mov_b32 %[scur], %[snext]\n\t" \
  "s_mov_b32 %[snext], %[sfree]\n\t" \
  "s_mov_b32 %[sfree], %[stmp]\n\t" \
  "s_sub_u32 %[cnt], %[cnt], 1\n\t" \
  "s_cmp_lg_u32 %[cnt], 0\n\t" \
  "s_cbranch_scc1 1b\n\t" \
  "s_waitcnt lgkmcnt(0)\n\t" \
  "v_mfma_f32_32x32x16_bf16 v[52:67], v[108:111], v[116:119], v[52:67]\n\t" \
  "s_waitcnt vmcnt(0)\n\t" \
  "s_nop 15\n\t"
#define ATTN_CLOBBERS "v36", "v37", "v38", "v39", "v40", "v41", "v42", "v43", "v44", "v45", "v46", "v47", "v48", "v49", "v50", "v51", "v68", "v69", "v70", "v71", "v72", "v73", "v74", "v75", "v76", "v77", "v78", "v79", "v80", "v81", "v82", "v83", "v84", "v85", "v86", "v87", "v88", "v89", "v90", "v91", "v92", "v93", "v94", "v95", "v96", "v97", "v98", "v99", "v100", "v101", "v102", "v103", "v104", "v105", "v106", "v107", "v108", "v109", "v110", "v111", "v112", "v113", "v114", "v115", "v116", "v117", "v118", "v119", "v120", "v121", "v122", "v123", "m0", "scc", "memory"

constexpr int AT_SLOT = 16384;
DEV void attn_role(char* lds, int a, char* ws) {
#if defined(__HIP_DEVICE_COMPILE__)
  STAMP_DECL
  RSTAMP(0); STAMP(1);
  const int head = (a & 7) * 4 + (a >> 6), qb = (a >> 3) & 7;
  const int tid = threadIdx.x, lane = tid & 63, l31 = lane & 31, h = lane >> 5;
  const int wid = __builtin_amdgcn_readfirstlane(tid >> 6);
  auto krs = mk_rsrc(ws + OFF_K + (size_t)head * (S_ * 32), S_ * 32);
  auto vrs = mk_rsrc(ws + OFF_V + (size_t)head * (S_ * 32), S_ * 32);
  const int q = qb * 256 + wid * 32 + l31;
  bf16x8 qf = *(const bf16x8*)(ws + OFF_Q + (size_t)head * (S_ * 32) + q * 32 + h * 16);
  auto issue = [&](int c, int slot) {
    char* sb = lds + slot * AT_SLOT + wid * 1024;
    dma16(krs, sb, lane * 16, c * 8192 + wid * 1024);
    dma16(vrs, sb + 8192, lane * 16, c * 8192 + wid * 1024);
  };
  const unsigned ldsb = (unsigned)(size_t)lds;
  const unsigned lk = ldsb + lane * 16;
  const unsigned lv = ldsb + 8192 + h * 256 + (l31 & 15) * 16;
  const unsigned goff = lane * 16;
  const unsigned m0k = ldsb + wid * 1024;
  int* flag = (int*)(lds + 3 * AT_SLOT);
  float mref = 0.f;
  f32x16 o;
  for (int attempt = 0; attempt < 2; ++attempt) {
    if (attempt == 1) {
      float mx = -__builtin_inff();
      for (int c = 0; c < 8; ++c) {
        issue(c, 0);
        WAIT_V(0); RAW_BARRIER();
        for (int t = 0; t < 8; ++t) {
          bf16x8 kf = *(const bf16x8*)(lds + t * 1024 + lane * 16);
          f32x16 z = {};
          f32x16 s = mfma32(kf, qf, z);
#pragma unroll
          for (int r = 0; r < 16; ++r) mx = fmaxf(mx, s[r]);
        }
        RAW_BARRIER();
      }
      mx = fmaxf(mx, __shfl_xor(mx, 32));
      mref = mx;
    }
    STAMP(2);
    issue(0, 0);
    issue(1, 1);
    STAMP(3);
    WAIT_V(2); RAW_BARRIER();
    STAMP(4);
    if (attempt == 0) {
      bf16x8 kf = *(const bf16x8*)(lds + lane * 16);
      f32x16 z = {};
      f32x16 s = mfma32(kf, qf, z);
      float mx = s[0];
#pragma unroll
      for (int r = 1; r < 16; ++r) mx = fmaxf(mx, s[r]);
      mx = fmaxf(mx, __shfl_xor(mx, 32));
      mref = mx;
    }
    STAMP(5);
    {
      float negm = -mref;
      unsigned scur, snext, sfree, stmp, cnt, ssrc = 2 * 8192 + wid * 1024;
      unsigned long long vmask = 0x0000FFFF0000FFFFull;
      const unsigned ones = (l31 == 16 || l31 == 20) ? 0x3f803f80u : 0u;
      asm volatile(ATTN_ASM
                   : "={v[52:67]}"(o), [scur] "=&s"(scur), [snext] "=&s"(snext), [sfree] "=&s"(sfree), [stmp] "=&s"(stmp),
                     [cnt] "=&s"(cnt), [ssrc] "+s"(ssrc)
                   : "{v[32:35]}"(qf), [negm] "v"(negm), [lk] "v"(lk), [lv] "v"(lv), [goff] "v"(goff), [rk] "s"(krs), [rv] "s"(vrs),
                     [m0k] "s"(m0k), [vmask] "s"(vmask), [ones] "v"(ones)
                   : ATTN_CLOBBERS);
    }
    STAMP(40);
    bool bad = !(o[8] < 1.0e30f);
#pragma unroll
    for (int r = 0; r < 8; ++r) bad |= !(fabsf(o[r]) < 3.0e38f);
    if (tid == 0) *flag = 0;
    RAW_BARRIER();
    if (attempt == 0 && __any(bad)) { if (lane == 0) *flag = 1; }
    RAW_BARRIER();
    int f = *flag;
    RAW_BARRIER();
    if (f == 0) break;
  }
  float inv = 1.0f / o[8];
  u32x4 ov = widen(pk4(o[0] * inv, o[1] * inv, o[2] * inv, o[3] * inv), pk4(o[4] * inv, o[5] * inv, o[6] * inv, o[7] * inv));
  {
    const unsigned l2 = fresh_lane();
    auto ors = mk_rsrc(ws + OFF_O, 64 * S_ * 16);
    __builtin_amdgcn_raw_buffer_store_b128(ov, ors, ((l2 >> 5) * S_ + (l2 & 31)) * 16, (2 * head * S_ + qb * 256 + wid * 32) * 16, 0);
  }
  STAMP(41); RSTAMP(42); STAMP_FLUSH(0);
#endif
}

namespace bsched {
constexpr int NG = 46;
constexpr int pos(int g) {
  if (g < 8) return 8 * g;
  if (g < 24) return 12 * ((g - 8) % 6);
  int u = (g - 24) % 10;
  return u == 0 ? 72 : u == 1 ? 48 : u == 2 ? 56 : u == 3 ? 64 : 8 * (u - 4);
}
constexpr int npw(int g) { return g < 8 ? 2 : g < 24 ? 3 : g == 45 ? 1 : 2; }
constexpr int iss(int g) {
  if (g < 8) return -1;
  if (g < 24) { int t = g - 8; return t == 0 ? 3 : t == 1 ? 4 : t == 2 ? 6 : t == 3 ? 7 : t == 4 ? 8 : t == 5 ? 9 : t + 6; }
  int u = g - 24;
  return u == 0 ? 9 : u == 1 ? 22 : u == 2 ? 23 : u == 3 ? 23 : u == 4 ? 24 : u == 5 ? 25 : u == 6 ? 25 : u <= 9 ? 26 : u + 19;
}
constexpr int pub(int g) { return g < 8 ? g : g < 24 ? g + 2 : g + 3; }
constexpr int pubbed(int bi) { return bi < 8 ? bi : (bi >= 10 && bi <= 25) ? bi - 2 : (bi >= 27 && bi <= 48) ? bi - 3 : -1; }
constexpr bool before(int a, int b) { return iss(a) != iss(b) ? iss(a) < iss(b) : a < b; }
constexpr int waitcnt(int g) {
  int n = 0;
  for (int o = 0; o < NG; ++o) if (o != g && before(g, o) && iss(o) < pub(g)) n += npw(o);
  return n;
}
}

DEV u32x4 ld_sc1(__amdgpu_buffer_rsrc_t r, unsigned voff, unsigned soff) { return __builtin_amdgcn_raw_buffer_load_b128(r, voff, soff, 16); }
DEV void st_sc1(__amdgpu_buffer_rsrc_t r, u32x4 v, unsigned voff, unsigned soff) { __builtin_amdgcn_raw_buffer_store_b128(v, r, voff, soff, 16); }

DEV void b_signal_wait(unsigned* cnt, int lane) {
  if (lane == 0) {
    __hip_atomic_fetch_add(cnt, 1u, __ATOMIC_RELAXED, __HIP_MEMORY_SCOPE_AGENT);
    for (int it = 0; it < (1 << 22); ++it) {
      if (__hip_atomic_load(cnt, __ATOMIC_RELAXED, __HIP_MEMORY_SCOPE_AGENT) >= 8u) break;
      __builtin_amdgcn_s_sleep(2);
    }
  }
}

DEV void b_loader(char* lds, int lw, int lane, int grp, int j, int c0, const u16* w2, const u16* w3, const u16* w4, char* ws) {
#if defined(__HIP_DEVICE_COMPILE__)
  using namespace bsched;
  auto r2 = mk_rsrc(w2, D_ * D_ * 2);
  auto r3 = mk_rsrc(w3, D_ * F_ * 2);
  auto r4 = mk_rsrc(w4, F_ * D_ * 2);
  unsigned va[2], vb[3];
#pragma unroll
  for (int i = 0; i < 2; ++i) {
    unsigned bb = (lw + 4 * i) * 1024 + lane * 16, row = bb >> 7, cp = (bb >> 4) & 7;
    va[i] = row * 1024 + j * 128 + ((cp ^ (((row >> 1) & 1) << 2)) << 4);
  }
#pragma unroll
  for (int i = 0; i < 3; ++i) {
    unsigned bb = (lw + 4 * i) * 1024 + lane * 16, row = bb / 384, cp = (bb % 384) >> 4;
    vb[i] = row * (F_ * 2) + c0 * 2 + ((cp ^ (((row >> 1) & 1) << 2)) << 4);
  }
  auto issue = [&](auto G) {
    constexpr int g = G;
    char* sb = lds + pos(g) * 1024 + lw * 1024;
    if constexpr (g < 8) { dma16(r2, sb, va[0], g * 65536); dma16(r2, sb + 4096, va[1], g * 65536); }
    else if constexpr (g < 24) {
#pragma unroll
      for (int i = 0; i < 3; ++i) dma16(r3, sb + i * 4096, vb[i], (g - 8) * 32 * (F_ * 2));
    } else {
      dma16(r4, sb, va[0], (g - 24) * 65536);
      if constexpr (g != 45) dma16(r4, sb + 4096, va[1], (g - 24) * 65536);
    }
  };
  static_for<NG>([&](auto G) { if constexpr (iss(G) == -1) issue(G); });
  static_for<50>([&](auto BI) {
    constexpr int bi = BI;
    constexpr int pg = pubbed(bi);
    if constexpr (bi == 10 || bi == 27) {
      if (lw == 0) b_signal_wait((unsigned*)(ws + OFF_CNT) + (bi == 10 ? grp : 32 + grp) * 64, lane);
    }
    if constexpr (pg >= 0) WAIT_V(waitcnt(pg));
    BARRIER_NOWAIT();
    static_for<NG>([&](auto G) { if constexpr (iss(G) == bi) issue(G); });
  });
#endif
}

#define K2_PM1 0
#define K2_PM2 0
#define K2_PM3 0
#define K2_PE1 0
#define K2_PE2 2
#define K2_PE3 2
#define BPRIO(n) do { SCHED_FENCE(); __builtin_amdgcn_s_setprio(n); SCHED_FENCE(); } while (0)
DEV void b_compute(char* lds, int wid, int lane, int grp, int j, int c0, int ntile, const u16* x, char* ws) {
#if defined(__HIP_DEVICE_COMPILE__)
  using namespace bsched;
  STAMP_DECL
  RSTAMP(0); STAMP(1);
  const int wm = wid & 1, kp = wid >> 1;
  const int m0 = grp * 64;
  auto hrs = mk_rsrc(ws + OFF_HB, 64 * HB_ROWS * 16);
  auto x1rs = mk_rsrc(ws + OFF_X1, 64 * S_ * 16);
  auto h2rs = mk_rsrc(ws + OFF_H2, 172 * S_ * 16);
  auto xrs = mk_rsrc(x, S_ * D_ * 2);
  const unsigned col0 = j * 64 + kp * 32;
  unsigned l31, h, blk, q, p, row, sw, cpart, wo13[2];
  auto lane_init = [&]() {
    lane = fresh_lane();
    l31 = lane & 31; h = lane >> 5; blk = (lane >> 4) & 1; q = (lane & 15) >> 2; p = lane & 3;
    row = m0 + wm * 32 + l31; sw = (q >> 1) & 1; cpart = 8 * (p & 1);
#pragma unroll
    for (int a = 0; a < 2; ++a) wo13[a] = (16 * kp + 8 * h + q) * 128 + (((4 * (a ^ kp) + 2 * blk + (p >> 1)) ^ (sw << 2)) << 4) + cpart;
  };
  lane_init();
  u32x2 xv[4];
#pragma unroll
  for (int i = 0; i < 4; ++i) xv[i] = __builtin_amdgcn_raw_buffer_load_b64(xrs, (row * D_ + col0 + 4 * h) * 2, i * 16, 0);
  const f32x2 st0 = *(const f32x2*)(ws + OFF_ST0 + (size_t)row * 8);

  auto phase13 = [&](auto G0_, auto NI_, auto QD_, auto loadA, f32x16 (&acc)[2], auto&& first_issue_done) {
    constexpr int G0 = G0_, NI = NI_, QD = QD_;
    u32x4 aq[QD];
    static_for<QD>([&](auto I) { if constexpr (I < NI) aq[I] = loadA(I); });
    first_issue_done();
    bf16x8 wf[2][2];
    auto rd = [&](auto I) {
      constexpr int i = I;
      const char* sb = lds + pos(G0 + (i >> 1)) * 1024 + (i & 1) * 4096;
      wf[i & 1][0] = tr_frag(sb + wo13[0], sb + wo13[0] + 512);
      wf[i & 1][1] = tr_frag(sb + wo13[1], sb + wo13[1] + 512);
    };
    auto mm = [&](auto I) {
      constexpr int i = I;
      bf16x8 a = __builtin_bit_cast(bf16x8, aq[i % QD]);
      acc[0] = mfma32(wf[i & 1][0], a, acc[0]);
      acc[1] = mfma32(wf[i & 1][1], a, acc[1]);
      if constexpr (i + QD < NI) aq[i % QD] = loadA(IC<i + QD>{});
    };
    static_for<NI>([&](auto I) {
      constexpr int i = I;
      if constexpr ((i & 1) == 0) { BARRIER_NOWAIT(); STAMP(2 + (G0 + (i >> 1))); }
      rd(I);
      if constexpr (i > 0) mm(IC<i - 1>{});
      SCHED_FENCE();
    });
    mm(IC<NI - 1>{});
  };
  auto exchange = [&](f32x16 (&acc)[2], int xpos) {
    char* xb = lds + xpos * 1024;
    char* mine = xb + (wm * 2 + (kp ^ 1)) * 4096 + lane * 16;
    const char* theirs = xb + (wm * 2 + kp) * 4096 + lane * 16;
#pragma unroll
    for (int i = 0; i < 4; ++i) *(f32x4*)(mine + i * 1024) = f32x4{acc[1][4 * i], acc[1][4 * i + 1], acc[1][4 * i + 2], acc[1][4 * i + 3]};
    RAW_BARRIER();
#pragma unroll
    for (int i = 0; i < 4; ++i) {
      f32x4 t = *(const f32x4*)(theirs + i * 1024);
#pragma unroll
      for (int c = 0; c < 4; ++c) acc[0][4 * i + c] += t[c];
    }
  };

  {
    f32x16 acc[2] = {};
    const unsigned va = (h * HB_ROWS + row) * 16 + kp * (2 * HB_ROWS * 16);
    auto loadA = [&](auto I) {
      constexpr int i = I;
      constexpr int kk0 = 4 * (i >> 1) + 2 * (i & 1), g = i >> 2;
      constexpr int d = g == 0 ? -1 : g == 1 ? 1 : g == 2 ? -2 : 2;
      return __builtin_amdgcn_raw_buffer_load_b128(hrs, va, kk0 * (2 * HB_ROWS * 16) + (2 + d) * 16, 0);
    };
    BPRIO(K2_PM1);
    phase13(IC<0>{}, IC<16>{}, IC<10>{}, loadA, acc, [] {});
    BPRIO(K2_PE1);
    exchange(acc, 64);
    STAMP(50);
    float v[16], a1 = 0.f, a2 = 0.f;
#pragma unroll
    for (int i = 0; i < 4; ++i) {
      v[4 * i + 0] = (bf_lo(xv[i][0]) - st0[0]) * st0[1] + acc[0][4 * i + 0];
      v[4 * i + 1] = (bf_hi(xv[i][0]) - st0[0]) * st0[1] + acc[0][4 * i + 1];
      v[4 * i + 2] = (bf_lo(xv[i][1]) - st0[0]) * st0[1] + acc[0][4 * i + 2];
      v[4 * i + 3] = (bf_hi(xv[i][1]) - st0[0]) * st0[1] + acc[0][4 * i + 3];
    }
#pragma unroll
    for (int i = 0; i < 16; ++i) { a1 += v[i]; a2 += v[i] * v[i]; }
    a1 = xhalf_sum(a1); a2 = xhalf_sum(a2);
#pragma unroll
    for (int g = 0; g < 2; ++g) st_sc1(x1rs, pack8(v + 8 * g), ((col0 / 8 + 2 * g + h) * S_ + row) * 16, 0);
    if (lane < 32) {
      auto srs = mk_rsrc(ws + OFF_ST1, 16 * S_ * 8);
      u32x2 sv = {__float_as_uint(a1), __float_as_uint(a2)};
      __builtin_amdgcn_raw_buffer_store_b64(sv, srs, ((col0 >> 5) * S_ + row) * 8, 0, 16);
    }
    WAIT_V(0);
    BARRIER_NOWAIT();
    STAMP(51);
  }
  {
    const int wn = kp;
    lane_init();
    float cs[3];
    {
      const float* cp = (const float*)(ws + OFF_CS3) + h * 4 * CS3_LD + c0 + wn * 96 + l31;
#pragma unroll
      for (int a = 0; a < 3; ++a) {
        float t = 0.f;
#pragma unroll
        for (int i = 0; i < 4; ++i) t += cp[i * CS3_LD + a * 32];
        cs[a] = xhalf_sum(t);
      }
    }
    unsigned wo2[3];
#pragma unroll
    for (int a = 0; a < 3; ++a) wo2[a] = (8 * h + q) * 384 + (((4 * (3 * wn + a) + 2 * blk + (p >> 1)) ^ (sw << 2)) << 4) + cpart;
    f32x16 acc[3] = {};
    constexpr int QD = 8;
    u32x4 aq[QD];
    const unsigned va = (h * S_ + row) * 16;
    BARRIER_NOWAIT();
    STAMP(52);
    u32x2 sp[8];
    {
      auto srs = mk_rsrc(ws + OFF_ST1, 16 * S_ * 8);
#pragma unroll
      for (int i = 0; i < 8; ++i) sp[i] = __builtin_amdgcn_raw_buffer_load_b64(srs, ((h * 8 + i) * S_ + row) * 8, 0, 16);
    }
    static_for<QD>([&](auto I) { aq[I] = ld_sc1(x1rs, va, I * 65536); });
    bf16x8 wf[2][3];
    auto rd = [&](auto I) {
      constexpr int i = I;
      const char* sb = lds + pos(8 + (i >> 1)) * 1024 + (i & 1) * (16 * 384);
#pragma unroll
      for (int a = 0; a < 3; ++a) wf[i & 1][a] = tr_frag(sb + wo2[a], sb + wo2[a] + 4 * 384);
    };
    auto mm = [&](auto I) {
      constexpr int i = I;
      bf16x8 a = __builtin_bit_cast(bf16x8, aq[i % QD]);
#pragma unroll
      for (int t = 0; t < 3; ++t) acc[t] = mfma32(wf[i & 1][t], a, acc[t]);
      if constexpr (i + QD < 32) aq[i % QD] = ld_sc1(x1rs, va, (i + QD) * 65536);
    };
    float mu, rs;
    {
      float a1 = 0.f, a2 = 0.f;
#pragma unroll
      for (int t = 0; t < 8; ++t) { a1 += __uint_as_float(sp[t][0]); a2 += __uint_as_float(sp[t][1]); }
      a1 = xhalf_sum(a1); a2 = xhalf_sum(a2);
      mu = a1 * (1.0f / D_);
      rs = __builtin_amdgcn_rsqf(fmaxf(a2 * (1.0f / D_) - mu * mu, 0.f) + LN_EPS);
      SCHED_FENCE();
    }
    BPRIO(K2_PM2);
    static_for<32>([&](auto I) {
      constexpr int i = I;
      if constexpr ((i & 1) == 0 && i > 0) { BARRIER_NOWAIT(); STAMP(2 + 8 + (i >> 1)); }
      rd(I);
      if constexpr (i > 0) mm(IC<i - 1>{});
      SCHED_FENCE();
    });
    mm(IC<31>{});
    BPRIO(K2_PE2);
    {
      unsigned mh = pk_bf16(-mu, -mu);
      float mlo = -mu - bf_lo(mh);
      u32x4 xe = {h ? 0u : mh, h ? 0u : (pk_bf16(mlo, 0.f)), 0u, 0u};
#pragma unroll
      for (int t = 0; t < 3; ++t) {
        unsigned ch = pk_bf16(cs[t], cs[t]);
        float clo = cs[t] - bf_lo(ch);
        u32x4 we = {h ? 0u : ((ch & 0xffffu) | (pk_bf16(clo, clo) << 16)), h ? 0u : (ch & 0xffffu), 0u, 0u};
        acc[t] = mfma32(__builtin_bit_cast(bf16x8, we), __builtin_bit_cast(bf16x8, xe), acc[t]);
      }
    }
    STAMP(53);
#pragma unroll
    for (int t = 0; t < 3; ++t) {
      if (3 * wn + t < ntile) {
        float v[16];
#pragma unroll
        for (int i = 0; i < 16; ++i) v[i] = gelu_tanh(acc[t][i] * rs);
        const unsigned kt8 = (c0 + (3 * wn + t) * 32) >> 3;
#pragma unroll
        for (int g = 0; g < 2; ++g) st_sc1(h2rs, pack8(v + 8 * g), ((kt8 + 2 * g + h) * S_ + row) * 16, 0);
      }
    }
    WAIT_V(0);
    BARRIER_NOWAIT();
    STAMP(54);
  }
  {
    lane_init();
    f32x16 acc[2] = {};
    const unsigned va = (h * S_ + row) * 16 + kp * 65536;
    auto loadA = [&](auto I) {
      constexpr int i = I;
      constexpr int kk0 = 4 * (i >> 1) + 2 * (i & 1);
      return ld_sc1(h2rs, va, kk0 * 65536);
    };
    BARRIER_NOWAIT();
    STAMP(55);
    constexpr int NI = 43, QD = 10;
    u32x4 aq[QD];
    static_for<QD>([&](auto I) { aq[I] = loadA(I); });
    BPRIO(K2_PM3);
    bf16x8 wf[2][2];
    u32x2 xr[4], x1r[4];
    auto rd = [&](auto I) {
      constexpr int i = I;
      const char* sb = lds + pos(24 + (i >> 1)) * 1024 + (i & 1) * 4096;
      wf[i & 1][0] = tr_frag(sb + wo13[0], sb + wo13[0] + 512);
      wf[i & 1][1] = tr_frag(sb + wo13[1], sb + wo13[1] + 512);
    };
    auto mm = [&](auto I) {
      constexpr int i = I;
      bf16x8 a = __builtin_bit_cast(bf16x8, aq[i % QD]);
      acc[0] = mfma32(wf[i & 1][0], a, acc[0]);
      acc[1] = mfma32(wf[i & 1][1], a, acc[1]);
      if constexpr (i + QD < NI) aq[i % QD] = loadA(IC<i + QD>{});
      if constexpr (i + QD == NI) {
#pragma unroll
        for (int t = 0; t < 4; ++t) {
          xr[t] = __builtin_amdgcn_raw_buffer_load_b64(xrs, (row * D_ + col0 + 4 * h) * 2, t * 16, 0);
          x1r[t] = __builtin_amdgcn_raw_buffer_load_b64(x1rs, ((col0 / 8 + t) * S_ + row) * 16 + 8 * h, 0, 16);
        }
      }
    };
    static_for<NI>([&](auto I) {
      constexpr int i = I;
      if constexpr ((i & 1) == 0 && i > 0) { BARRIER_NOWAIT(); STAMP(2 + 24 + (i >> 1)); }
      rd(I);
      if constexpr (i > 0) mm(IC<i - 1>{});
      SCHED_FENCE();
    });
    mm(IC<NI - 1>{});
    BPRIO(K2_PE3);
    exchange(acc, 32);
    STAMP(56);
    char* rb = ws + OFF_R + ((size_t)((m0 >> 5) + wm) * 16 + (col0 >> 5)) * 4096 + fresh_lane() * 16;
#pragma unroll
    for (int i = 0; i < 4; ++i) {
      f32x4 o;
      o[0] = acc[0][4 * i + 0] + bf_lo(xr[i][0]) + bf_lo(x1r[i][0]);
      o[1] = acc[0][4 * i + 1] + bf_hi(xr[i][0]) + bf_hi(x1r[i][0]);
      o[2] = acc[0][4 * i + 2] + bf_lo(xr[i][1]) + bf_lo(x1r[i][1]);
      o[3] = acc[0][4 * i + 3] + bf_hi(xr[i][1]) + bf_hi(x1r[i][1]);
      *(f32x4*)(rb + i * 1024) = o;
    }
  }
  STAMP(60); RSTAMP(61); STAMP_FLUSH(0);
#endif
}

constexpr int K2_LDS = 81920;
__global__ __launch_bounds__(512, 4) void k2(const u16* __restrict__ x, const u16* __restrict__ w2, const u16* __restrict__ w3,
                                             const u16* __restrict__ w4, char* __restrict__ ws) {
#if defined(__HIP_DEVICE_COMPILE__)
  extern __shared__ __attribute__((aligned(16))) char lds[];
#define K2_SWAP 1
#define K2_BPRIO 2
#define K2_APRIO 1
  const int b = blockIdx.x ^ (K2_SWAP * 256);
  if (b >= 256) { __builtin_amdgcn_s_setprio(K2_APRIO); attn_role(lds, b - 256, ws); return; }
  const int lane = threadIdx.x & 63;
  const int wid = __builtin_amdgcn_readfirstlane(threadIdx.x >> 6);
  const int grp = (b & 7) * 4 + (b >> 6), j = (b >> 3) & 7;
  const int c0 = j < 3 ? j * 192 : 576 + (j - 3) * 160;
  const int ntile = j < 3 ? 6 : 5;
#define K2_BDELAY 24
  __builtin_amdgcn_s_sleep(K2_BDELAY);
  __builtin_amdgcn_s_setprio(K2_BPRIO);
  if (wid >= 4) b_loader(lds, wid - 4, lane, grp, j, c0, w2, w3, w4, ws);
  else b_compute(lds, wid, lane, grp, j, c0, ntile, x, ws);
#endif
}

constexpr int K3_LDS = 131072;
__global__ __launch_bounds__(512, 2) void k3(const u16* __restrict__ wo, const char* __restrict__ ws, u16* __restrict__ out) {
#if defined(__HIP_DEVICE_COMPILE__)
  extern __shared__ __attribute__((aligned(16))) char lds[];
  STAMP_DECL
  RSTAMP(0); STAMP(1);
  const int b = blockIdx.x, xcd = b & 7, bi = b >> 3;
  const int mt = xcd * 4 + (bi >> 3), nt = bi & 7;
  const int m0 = mt * 64;
  const int tid = threadIdx.x, lane = tid & 63;
  const int wid = __builtin_amdgcn_readfirstlane(tid >> 6);
  auto ors = mk_rsrc(ws + OFF_O, 64 * S_ * 16);
  auto wrs = mk_rsrc(wo, D_ * D_ * 2);
  const unsigned va = (m0 + lane) * 16;
  unsigned vw;
  { unsigned row = lane >> 3, cp = lane & 7; vw = row * 1024 + nt * 128 + ((cp ^ (((row >> 1) & 1) << 2)) << 4); }
  const int rot = (nt & 3);
#pragma unroll
  for (int c = 0; c < 4; ++c) {
    const int cc = (c + rot) & 3;
#pragma unroll
    for (int i = 0; i < 2; ++i) {
      const int pc = 2 * wid + i;
      dma16(ors, lds + (c * 16 + pc) * 1024, va, (cc * 16 + pc) * (S_ * 16));
      dma16(wrs, lds + 65536 + (c * 16 + pc) * 1024, vw, (cc * 16 + pc) * 8192);
    }
  }
  const unsigned l31 = lane & 31, h = lane >> 5, blk = (lane >> 4) & 1, q = (lane & 15) >> 2, p = lane & 3;
  const int wm = wid & 1, wn = (wid >> 1) & 1;
  f32x4 r[4];
  if (wid < 4) {
    const char* rb = ws + OFF_R + ((size_t)((m0 >> 5) + wm) * 16 + nt * 2 + wn) * 4096 + lane * 16;
#pragma unroll
    for (int i = 0; i < 4; ++i) r[i] = *(const f32x4*)(rb + i * 1024);
  }
  const unsigned ao = h * 1024 + (wm * 32 + l31) * 16;
  const unsigned wo_ = 65536 + (8 * h + q) * 128 + (((4 * wn + 2 * blk + (p >> 1)) ^ (((q >> 1) & 1) << 2)) << 4) + 8 * (p & 1);
  f32x16 acc = {};
  static_for<4>([&](auto C) {
    constexpr int c = C;
    if (wid < 4) WAIT_V((3 - c) * 4 + 4); else WAIT_V((3 - c) * 4);
    BARRIER_NOWAIT();
    STAMP(4 + c);
    if (wid < 4) {
      bf16x8 xf[8], wf[8];
#pragma unroll
      for (int s = 0; s < 8; ++s) {
        xf[s] = *(const bf16x8*)(lds + c * 16384 + s * 2048 + ao);
        wf[s] = tr_frag(lds + c * 16384 + s * 2048 + wo_, lds + c * 16384 + s * 2048 + wo_ + 512);
      }
#pragma unroll
      for (int s = 0; s < 8; ++s) acc = mfma32(wf[s], xf[s], acc);
    }
  });
  STAMP(40);
  if (wid < 4) {
    float v[16];
#pragma unroll
    for (int i = 0; i < 4; ++i)
#pragma unroll
      for (int c = 0; c < 4; ++c) v[4 * i + c] = acc[4 * i + c] + r[i][c];
    u16* dst = out + (size_t)(m0 + wm * 32 + l31) * D_ + nt * 64 + wn * 32 + h * 8;
#pragma unroll
    for (int g = 0; g < 2; ++g) *(u32x4*)(dst + g * 16) = pack8(v + 8 * g);
  }
  STAMP(41); RSTAMP(42); STAMP_FLUSH(0);
#endif
}

#define SEL(i) true
extern "C" void kernel_launch(void* const* d_in, const int* in_sizes, int n_in, void* d_out, int out_size,
                              void* d_ws, size_t ws_size, hipStream_t stream) {
  const u16* x = (const u16*)d_in[0];
  const u16* wqkv = (const u16*)d_in[1];
  const u16* wo = (const u16*)d_in[2];
  const u16* w1 = (const u16*)d_in[4];
  const u16* w2 = (const u16*)d_in[5];
  const u16* w3 = (const u16*)d_in[6];
  const u16* w4 = (const u16*)d_in[7];
  char* ws = (char*)d_ws;
  (void)hipFuncSetAttribute((const void*)k1, hipFuncAttributeMaxDynamicSharedMemorySize, K1_LDS);
  (void)hipFuncSetAttribute((const void*)k2, hipFuncAttributeMaxDynamicSharedMemorySize, K2_LDS);
  (void)hipFuncSetAttribute((const void*)k3, hipFuncAttributeMaxDynamicSharedMemorySize, K3_LDS);
  if (SEL(0)) k1<<<256, 512, K1_LDS, stream>>>(x, wqkv, w1, w3, w2, w4, wo, ws);
  if (SEL(1)) k2<<<512, 512, K2_LDS, stream>>>(x, w2, w3, w4, ws);
  if (SEL(2)) k3<<<256, 512, K3_LDS, stream>>>(wo, ws, (u16*)d_out);
}
```

```cpp
#include <hip/hip_runtime.h>
#include <stdint.h>

typedef __attribute__((ext_vector_type(8))) __bf16 bf16x8;
typedef __attribute__((ext_vector_type(2))) __bf16 bf16x2;
typedef __attribute__((ext_vector_type(4))) short s16x4;
typedef __attribute__((ext_vector_type(16))) float f32x16;
typedef __attribute__((ext_vector_type(4))) float f32x4;
typedef __attribute__((ext_vector_type(2))) float f32x2;
typedef __attribute__((ext_vector_type(4))) unsigned u32x4;
typedef __attribute__((ext_vector_type(2))) unsigned u32x2;
typedef __attribute__((address_space(3))) void* ldsptr_t;
typedef unsigned short u16;

#define DEV __device__ __forceinline__

constexpr int S_ = 2048, D_ = 512, H_ = 32, F_ = 1376;
constexpr float LN_EPS = 1e-5f;
constexpr float QSCALE = 0.25f * 1.4426950408889634f;

constexpr size_t MB_ = 1 << 20, KB_ = 1 << 10;
constexpr size_t OFF_Q = 0, OFF_K = 2 * MB_, OFF_V = 4 * MB_, OFF_O = 6 * MB_, OFF_X1 = 8 * MB_, OFF_HB = 10 * MB_;
constexpr size_t OFF_ST0 = 12 * MB_ + 64 * KB_, OFF_CNT = 12 * MB_ + 96 * KB_, OFF_CS3 = 12 * MB_ + 128 * KB_;
constexpr size_t OFF_ST1 = 12 * MB_ + 256 * KB_, OFF_H2 = 13 * MB_, OFF_R = 19 * MB_;
constexpr int HB_ROWS = S_ + 4;
constexpr int CS3_P = 8, CS3_LD = 1408;

template <int N> struct IC { static constexpr int value = N; constexpr operator int() const { return N; } };
template <int I, int N, class F> DEV void static_for_impl(F&& f) {
  if constexpr (I < N) { f(IC<I>{}); static_for_impl<I + 1, N>(f); }
}
template <int N, class F> DEV void static_for(F&& f) { static_for_impl<0, N>(f); }

DEV unsigned pk_bf16(float lo, float hi) {
  f32x2 v = {lo, hi};
  return __builtin_bit_cast(unsigned, __builtin_convertvector(v, bf16x2));
}
DEV float bf_lo(unsigned u) { return __uint_as_float(u << 16); }
DEV float bf_hi(unsigned u) { return __uint_as_float(u & 0xffff0000u); }
DEV float gelu_tanh(float x) {
  const float c1 = -2.0f * 0.7978845608028654f * 1.4426950408889634f;
  const float c2 = c1 * 0.044715f;
  float t = x * (c1 + c2 * x * x);
  float e = __builtin_amdgcn_exp2f(t);
  return x * __builtin_amdgcn_rcpf(1.0f + e);
}
DEV float dot2(unsigned a, unsigned b, float c) {
  return __builtin_amdgcn_fdot2_f32_bf16(__builtin_bit_cast(bf16x2, a), __builtin_bit_cast(bf16x2, b), c, false);
}
DEV float xhalf_sum(float v) {
  auto r = __builtin_amdgcn_permlane32_swap(__float_as_uint(v), __float_as_uint(v), false, false);
  return __uint_as_float(r[0]) + __uint_as_float(r[1]);
}
#define STAMP(i)
#define RSTAMP(i)
#define STAMP_DECL
#define STAMP_ARG
#define STAMP_PASS
#define STAMP_FLUSH(w)
#define WAIT_V(n) asm volatile("s_waitcnt vmcnt(%0)" ::"n"(n) : "memory")
#define WAIT_L(n) asm volatile("s_waitcnt lgkmcnt(%0)" ::"n"(n) : "memory")
#define SCHED_FENCE() __builtin_amdgcn_sched_barrier(0)
#define RAW_BARRIER() do { WAIT_L(0); __builtin_amdgcn_s_barrier(); } while (0)
#define BARRIER_NOWAIT() do { SCHED_FENCE(); asm volatile("s_barrier" ::: "memory"); SCHED_FENCE(); } while (0)

DEV unsigned fresh_lane() {
  unsigned l;
  asm volatile("v_mbcnt_lo_u32_b32 %0, -1, 0\n\tv_mbcnt_hi_u32_b32 %0, -1, %0" : "=v"(l));
  return l;
}
DEV __amdgpu_buffer_rsrc_t mk_rsrc(const void* p, unsigned bytes) {
  return __builtin_amdgcn_make_buffer_rsrc((void*)p, 0, (int)bytes, 0x00020000);
}
DEV void dma16(__amdgpu_buffer_rsrc_t r, char* lds_dst, unsigned voff, unsigned soff) {
  __builtin_amdgcn_raw_ptr_buffer_load_lds(r, (ldsptr_t)lds_dst, 16, voff, soff, 0, 0);
}
DEV bf16x8 tr_frag(const char* p0, const char* p1) {
  s16x4 a = __builtin_amdgcn_ds_read_tr16_b64_v4i16((__attribute__((address_space(3))) s16x4*)p0);
  s16x4 b = __builtin_amdgcn_ds_read_tr16_b64_v4i16((__attribute__((address_space(3))) s16x4*)p1);
  typedef __attribute__((ext_vector_type(8))) short s16x8;
  s16x8 r = __builtin_shufflevector(a, b, 0, 1, 2, 3, 4, 5, 6, 7);
  return __builtin_bit_cast(bf16x8, r);
}
DEV f32x16 mfma32(bf16x8 a, bf16x8 b, f32x16 c) { return __builtin_amdgcn_mfma_f32_32x32x16_bf16(a, b, c, 0, 0, 0); }
DEV u32x2 pk4(float a, float b, float c, float d) { u32x2 r; r[0] = pk_bf16(a, b); r[1] = pk_bf16(c, d); return r; }
DEV u32x4 widen(u32x2 X, u32x2 Y) {
  u32x4 o;
  auto a = __builtin_amdgcn_permlane32_swap(X[0], Y[0], false, false);
  auto b = __builtin_amdgcn_permlane32_swap(X[1], Y[1], false, false);
  o[0] = a[0]; o[1] = b[0]; o[2] = a[1]; o[3] = b[1];
  return o;
}
DEV u32x4 pack8(const float* v) { return widen(pk4(v[0], v[1], v[2], v[3]), pk4(v[4], v[5], v[6], v[7])); }

constexpr int K1_A = 16384, K1_ST = 32768;
constexpr int K1_X = 4 * K1_ST;
constexpr int K1_P3 = K1_X + 2048 + 1024 + 1536;
constexpr int K1_LDS = K1_P3 + 2048 + 16;

#define K1_STAUX 16
template <int KIND>
DEV void k1_epi_s(const f32x16& a, const float* fin, int sub, int J, int m0, int rl0, int lane, char* ws) {
  const int l31 = lane & 31, h = lane >> 5;
  const int rl = rl0 + l31, s = m0 + rl;
  float mu = fin[rl], rs = fin[128 + rl];
  if (KIND == 0) rs *= QSCALE;
  if (KIND == 2) {
    float r2 = rs * rs;
    rs *= __builtin_amdgcn_rsqf((1.0f - LN_EPS * r2) + LN_EPS);
  }
  float v[16];
#pragma unroll
  for (int j = 0; j < 4; ++j) {
    f32x4 c = *(const f32x4*)(fin + 256 + sub * 32 + 8 * j + 4 * h);
#pragma unroll
    for (int i = 0; i < 4; ++i) v[4 * j + i] = (a[4 * j + i] - mu * c[i]) * rs;
  }
  if (KIND == 2) {
#pragma unroll
    for (int i = 0; i < 16; ++i) v[i] = gelu_tanh(v[i]);
  }
#pragma unroll
  for (int g = 0; g < 2; ++g) {
    u32x4 o = pack8(v + 8 * g);
    unsigned dst;
    if (KIND == 0) dst = OFF_Q + (unsigned)(2 * J + g) * (S_ * 32) + (unsigned)(s * 32 + h * 16);
    else if (KIND == 1) dst = OFF_K + (unsigned)(2 * J + g) * (S_ * 32) + (unsigned)((s >> 5) * 1024 + h * 512 + (s & 31) * 16);
    else dst = OFF_HB + (unsigned)(((4 * J + 2 * g + h) * HB_ROWS + s + 2) * 16);
    __builtin_amdgcn_raw_buffer_store_b128(o, mk_rsrc(ws, 16 * MB_), dst, 0, K1_STAUX);
  }
}
DEV void k1_epi_v(const f32x16& a, const float* fin, int sub, int J, int m0, int rl0, int lane, char* ws) {
  const int l31 = lane & 31, h = lane >> 5;
  const float cs = fin[256 + sub * 32 + l31];
  float v[16];
#pragma unroll
  for (int j = 0; j < 4; ++j) {
    f32x4 mu = *(const f32x4*)(fin + rl0 + 8 * j + 4 * h), rs = *(const f32x4*)(fin + 128 + rl0 + 8 * j + 4 * h);
#pragma unroll
    for (int i = 0; i < 4; ++i) v[4 * j + i] = (a[4 * j + i] - mu[i] * cs) * rs[i];
  }
  const int n = J * 32 + l31, head = n >> 4, d = n & 15, tile = (m0 + rl0) >> 5;
  const unsigned base = OFF_V + (unsigned)head * (S_ * 32) + tile * 1024;
#pragma unroll
  for (int s = 0; s < 2; ++s) {
    u32x4 o;
#pragma unroll
    for (int j = 0; j < 4; ++j) o[j] = pk_bf16(v[8 * s + 2 * j], v[8 * s + 2 * j + 1]);
    __builtin_amdgcn_raw_buffer_store_b128(o, mk_rsrc(ws, 16 * MB_), base + (s * 2 + h) * 256 + d * 16, 0, K1_STAUX);
  }
}

template <int TYPE>
DEV void k1_compute(char* lds, int wm, int wn, int nt, int m0, char* ws, const char* pfp, unsigned pfs STAMP_ARG) {
  const unsigned lane = fresh_lane();
  const unsigned l31 = lane & 31, h = lane >> 5, blk = (lane >> 4) & 1, q = (lane & 15) >> 2, p = lane & 3;
  const unsigned mask = wm ^ wn;
  const unsigned row = wm * 64 + l31;
  unsigned ao[4], wb[2][2];
#pragma unroll
  for (int j = 0; j < 4; ++j) ao[j] = row * 128 + (((2 * (j ^ mask) + h) ^ ((row >> 1) & 7)) << 4);
  const unsigned cpart = (2 * blk + (p >> 1)) * 16 + 8 * (p & 1);
#pragma unroll
  for (int a = 0; a < 2; ++a) {
    const unsigned w = K1_A + (8 * h + q) * 256 + (((wn + 2 * a) ^ q) << 6) + cpart;
    wb[0][a] = w + mask * 4096; wb[1][a] = w - mask * 4096;
  }
  f32x16 acc[2][2] = {};
  float s1[2][2] = {}, s2[2][2] = {}, cs[2][2] = {};
  bf16x8 xf[4][2], wf[4][2];
  auto rd = [&](int t, int j) {
    const char* sb = lds + (t & 3) * K1_ST;
    xf[j][0] = *(const bf16x8*)(sb + ao[j]);
    wf[j][0] = tr_frag(sb + wb[j & 1][0] + j * 4096, sb + wb[j & 1][0] + j * 4096 + 1024);
    wf[j][1] = tr_frag(sb + wb[j & 1][1] + j * 4096, sb + wb[j & 1][1] + j * 4096 + 1024);
    xf[j][1] = *(const bf16x8*)(sb + ao[j] + 4096);
  };
  auto mm = [&](int j) {
#pragma unroll
    for (int r = 0; r < 2; ++r)
#pragma unroll
      for (int a = 0; a < 2; ++a)
        acc[r][a] = TYPE == 2 ? mfma32(xf[j][r], wf[j][a], acc[r][a]) : mfma32(wf[j][a], xf[j][r], acc[r][a]);
    __builtin_amdgcn_sched_group_barrier(0x008, 1, 0);
    __builtin_amdgcn_sched_group_barrier(0x100, 3, 0);
    __builtin_amdgcn_sched_group_barrier(0x008, 1, 0);
    __builtin_amdgcn_sched_group_barrier(0x100, 3, 0);
    __builtin_amdgcn_sched_group_barrier(0x008, 2, 0);
  };
  auto dots = [&](int j) {
#pragma unroll
    for (int i = 0; i < 4; ++i) {
#pragma unroll
      for (int r = 0; r < 2; ++r) {
        unsigned u = __builtin_bit_cast(u32x4, xf[j][r])[i];
        s1[r][i & 1] = dot2(u, 0x3f803f80u, s1[r][i & 1]); s2[r][i & 1] = dot2(u, u, s2[r][i & 1]);
      }
#pragma unroll
      for (int a = 0; a < 2; ++a) cs[a][i & 1] = dot2(__builtin_bit_cast(u32x4, wf[j + 1][a])[i], 0x3f803f80u, cs[a][i & 1]);
    }
    asm volatile("" : "+v"(s1[0][0]), "+v"(s1[0][1]), "+v"(s1[1][0]), "+v"(s1[1][1]), "+v"(s2[0][0]), "+v"(s2[0][1]), "+v"(s2[1][0]), "+v"(s2[1][1]),
                      "+v"(cs[0][0]), "+v"(cs[0][1]), "+v"(cs[1][0]), "+v"(cs[1][1]));
  };
#pragma unroll
  for (int t = 0; t < 8; ++t) {
    WAIT_L(0);
    BARRIER_NOWAIT();
    STAMP(4 + t);
    rd(t, 0);
    if (t > 0) { mm(3); SCHED_FENCE(); dots(2); }
    SCHED_FENCE();
    rd(t, 1);
    mm(0);
    SCHED_FENCE();
    rd(t, 2);
    mm(1);
    SCHED_FENCE();
    dots(0);
    SCHED_FENCE();
    rd(t, 3);
    mm(2);
    SCHED_FENCE();
  }
  mm(3);
  SCHED_FENCE();
  dots(2);
  STAMP(40);
  float* rsb = (float*)(lds + K1_X);
  float* csb = rsb + 512;
  float* fin = csb + 256;
#pragma unroll
  for (int r = 0; r < 2; ++r) {
    float a = xhalf_sum(s1[r][0] + s1[r][1]), b = xhalf_sum(s2[r][0] + s2[r][1]), c = xhalf_sum(cs[r][0] + cs[r][1]);
    if (h == 0) *(f32x2*)(rsb + (wn * 128 + wm * 64 + r * 32 + l31) * 2) = f32x2{a, b};
    else csb[wm * 128 + (wn + 2 * r) * 32 + l31] = c;
  }
  STAMP(43);
  RAW_BARRIER();
  STAMP(44);
  {
    const int i = wm * 64 + lane;
    f32x2 a = *(const f32x2*)(rsb + i * 2), c = *(const f32x2*)(rsb + (128 + i) * 2);
    float mu = (a[0] + c[0]) * (1.0f / D_);
    float var = fmaxf((a[1] + c[1]) * (1.0f / D_) - mu * mu, 0.f);
    float rs = __builtin_amdgcn_rsqf(var + LN_EPS);
    float cc = csb[i] + csb[128 + i];
    fin[wn * 256 + i] = wn ? cc : mu;
    if (wn == 0) {
      fin[128 + i] = rs;
      if (nt == 0) *(f32x2*)(ws + OFF_ST0 + (size_t)(m0 + i) * 8) = f32x2{mu, rs};
    }
  }
  STAMP(45);
  RAW_BARRIER();
  STAMP(46);
  const int c = nt >> 1;
#pragma unroll
  for (int r = 0; r < 2; ++r) {
    const int rl0 = wm * 64 + r * 32;
    if (TYPE == 0) {
      k1_epi_s<0>(acc[r][0], fin, wn, 2 * c + wn, m0, rl0, lane, ws);
      STAMP(47 + 2 * r);
      k1_epi_s<2>(acc[r][1], fin, wn + 2, 2 * c + wn, m0, rl0, lane, ws);
      STAMP(48 + 2 * r);
    } else if (TYPE == 1) {
      k1_epi_s<1>(acc[r][0], fin, wn, 4 * c + wn, m0, rl0, lane, ws);
      k1_epi_s<1>(acc[r][1], fin, wn + 2, 4 * c + wn + 2, m0, rl0, lane, ws);
    } else {
      k1_epi_v(acc[r][0], fin, wn, 4 * (c - 4) + wn, m0, rl0, lane, ws);
      k1_epi_v(acc[r][1], fin, wn + 2, 4 * (c - 4) + wn + 2, m0, rl0, lane, ws);
    }
  }
  STAMP(41); RSTAMP(42); STAMP_FLUSH(4);
}

__global__ __launch_bounds__(512, 2) void k1(const u16* __restrict__ x, const u16* __restrict__ wqkv, const u16* __restrict__ w1,
                                             const u16* __restrict__ w3, const u16* __restrict__ w2, const u16* __restrict__ w4,
                                             const u16* __restrict__ wo, char* __restrict__ ws) {
#if defined(__HIP_DEVICE_COMPILE__)
  extern __shared__ __attribute__((aligned(16))) char lds[];
  STAMP_DECL
  RSTAMP(0); STAMP(1);
  const int b = blockIdx.x, xcd = b & 7, bi = b >> 3;
  const int mt = (xcd & 3) * 4 + (bi & 3), nt = (xcd >> 2) * 8 + (bi >> 2);
  const int m0 = mt * 128;
  const int tid = threadIdx.x, lane = tid & 63;
  const int wid = __builtin_amdgcn_readfirstlane(tid >> 6);
  if (__builtin_expect(wid < 4, 1)) {
    const int lw = wid;
    auto xrs = mk_rsrc(x, S_ * D_ * 2);
    unsigned va[4];
#pragma unroll
    for (int i = 0; i < 4; ++i) {
      unsigned bb = (lw + 4 * i) * 1024 + lane * 16, row = bb >> 7, cp = (bb >> 4) & 7;
      va[i] = (m0 + row) * 1024 + ((cp ^ ((row >> 1) & 7)) << 4);
    }
    const unsigned kl = lane >> 4, seg = ((lane >> 2) & 3) ^ kl;
    const int c = nt >> 1;
    const bool fromw1 = !(nt & 1) && seg >= 2;
    const unsigned wstride = fromw1 ? 1024u : 3072u;
    const char* wbase = fromw1 ? (const char*)w1 + c * 128 + (seg - 2) * 64
                               : (const char*)wqkv + ((nt & 1) ? 1024 + c * 256 : c * 128) + seg * 64;
    wbase += (lane & 3) * 16 + (lw * 4 + kl) * wstride;
#define K1_ROT (nt + 2 * mt)
    const int rot = K1_ROT;
    auto issue = [&](int t) {
      char* sb = lds + (t & 3) * K1_ST;
      const unsigned kt = (t + rot) & 7;
#pragma unroll
      for (int i = 0; i < 4; ++i) {
        dma16(xrs, sb + (lw + 4 * i) * 1024, va[i], kt * 128);
        __builtin_amdgcn_global_load_lds((const unsigned*)(wbase + (size_t)((kt * 64 + i * 16) * wstride)),
                                         (__attribute__((address_space(3))) unsigned*)(sb + K1_A + (lw + 4 * i) * 1024), 16, 0, 0);
      }
    };
    STAMP(2);
    issue(0); STAMP(3); issue(1);
    if (tid == 0) *(unsigned*)(lds + K1_P3 + 2048) = 0;
    STAMP(4);
    WAIT_V(8);  STAMP(6); BARRIER_NOWAIT(); issue(2); STAMP(20); issue(3); STAMP(21);
    WAIT_V(16); STAMP(7); BARRIER_NOWAIT(); issue(4); STAMP(22);
    WAIT_V(16); STAMP(8); BARRIER_NOWAIT(); issue(5);
    WAIT_V(16); STAMP(9); BARRIER_NOWAIT(); issue(6);
    WAIT_V(16); STAMP(10); BARRIER_NOWAIT(); issue(7);
    const void* mp; unsigned mbytes, mv, mstride;
    if (b < 88) {
      int cg = b % 11, rg = b / 11;
      mp = w3; mbytes = D_ * F_ * 2; mstride = 4 * F_ * 2;
      mv = (rg * 64 + lw * 16 + (lane >> 4)) * (F_ * 2) + cg * 256 + (lane & 15) * 16;
    } else {
      const int it = (b - 88) * 4 + lw;
      mstride = 8192;
      if (it < 16) { mp = w2; mbytes = D_ * D_ * 2; mv = it * 32768 + lane * 128; }
      else if (it < 32) { mp = wo; mbytes = D_ * D_ * 2; mv = (it - 16) * 32768 + lane * 128; }
      else { mp = w4; mbytes = D_ * F_ * 2; mv = (it - 32) * 32768 + lane * 128; }
    }
    auto mrs = mk_rsrc(mp, mbytes);
    u32x4 md[4];
#pragma unroll
    for (int i = 0; i < 4; ++i) md[i] = __builtin_amdgcn_raw_buffer_load_b128(mrs, mv, i * mstride, 0);
    WAIT_V(20); STAMP(11); BARRIER_NOWAIT();
    WAIT_V(12); STAMP(12); BARRIER_NOWAIT();
    WAIT_V(4);  STAMP(13); BARRIER_NOWAIT();
    if (b < 88) {
      float cs[8];
#pragma unroll
      for (int j = 0; j < 8; ++j) cs[j] = 0.f;
#pragma unroll
      for (int i = 0; i < 4; ++i)
#pragma unroll
        for (int j = 0; j < 4; ++j) { cs[2 * j] += bf_lo(md[i][j]); cs[2 * j + 1] += bf_hi(md[i][j]); }
#pragma unroll
      for (int j = 0; j < 8; ++j) { cs[j] += __shfl_xor(cs[j], 16); cs[j] = xhalf_sum(cs[j]); }
      float* p3 = (float*)(lds + K1_P3);
      if (lane < 16) {
        *(f32x4*)(p3 + lw * 128 + lane * 8) = f32x4{cs[0], cs[1], cs[2], cs[3]};
        *(f32x4*)(p3 + lw * 128 + lane * 8 + 4) = f32x4{cs[4], cs[5], cs[6], cs[7]};
      }
      WAIT_L(0);
      unsigned old = 0;
      if (lane == 0) old = __hip_atomic_fetch_add((unsigned*)(lds + K1_P3 + 2048), 1u, __ATOMIC_RELAXED, __HIP_MEMORY_SCOPE_WORKGROUP);
      old = __builtin_amdgcn_readfirstlane(old);
      if (old == 3) {
        f32x2 t = {0.f, 0.f};
#pragma unroll
        for (int w = 0; w < 4; ++w) t += *(const f32x2*)(p3 + w * 128 + lane * 2);
        *(f32x2*)((float*)(ws + OFF_CS3) + (b / 11) * CS3_LD + (b % 11) * 128 + lane * 2) = t;
      }
    } else {
#pragma unroll
      for (int i = 0; i < 4; ++i) asm volatile("" ::"v"(md[i]));
      if (b == 200 && lw == 0) ((unsigned*)(ws + OFF_CNT))[lane * 64] = 0;
      if (b == 201) {
        u32x4 z = {0, 0, 0, 0};
        *(u32x4*)(ws + OFF_HB + ((size_t)lane * HB_ROWS + (lw < 2 ? lw : S_ + lw)) * 16) = z;
      }
    }
    STAMP(41); RSTAMP(42); STAMP_FLUSH(0);
    return;
  }
  const int wm = wid & 1, wn = (wid >> 1) & 1;
  const char* pfp; unsigned pfs;
  {
    const int c = nt >> 1, i = lane - 16, kt = wid - 4;
    if (lane < 16) { pfp = (const char*)x + (size_t)(m0 + (bi >> 2) * 16 + lane) * 1024 + kt * 128; pfs = 128; }
    else {
      const unsigned k = kt * 64 + (bi & 3) * 16 + ((i >> 1) & 15), half = i & 1;
      const bool fw1 = !(nt & 1) && half;
      pfs = fw1 ? 65536u : 196608u;
      pfp = fw1 ? (const char*)w1 + k * 1024 + c * 128
                : (const char*)wqkv + k * 3072 + ((nt & 1) ? 1024 + c * 256 + half * 128 : c * 128);
    }
  }
  if (!(nt & 1)) k1_compute<0>(lds, wm, wn, nt, m0, ws, pfp, pfs STAMP_PASS);
  else if (nt < 8) k1_compute<1>(lds, wm, wn, nt, m0, ws, pfp, pfs STAMP_PASS);
  else k1_compute<2>(lds, wm, wn, nt, m0, ws, pfp, pfs STAMP_PASS);
#endif
}
#define ATTN_ASM \
  "v_mov_b32 v36, %[negm]\n\t" \
  "v_mov_b32 v37, %[negm]\n\t" \
  "v_mov_b32 v38, %[negm]\n\t" \
  "v_mov_b32 v39, %[negm]\n\t" \
  "v_mov_b32 v40, %[negm]\n\t" \
  "v_mov_b32 v41, %[negm]\n\t" \
  "v_mov_b32 v42, %[negm]\n\t" \
  "v_mov_b32 v43, %[negm]\n\t" \
  "v_mov_b32 v44, %[negm]\n\t" \
  "v_mov_b32 v45, %[negm]\n\t" \
  "v_mov_b32 v46, %[negm]\n\t" \
  "v_mov_b32 v47, %[negm]\n\t" \
  "v_mov_b32 v48, %[negm]\n\t" \
  "v_mov_b32 v49, %[negm]\n\t" \
  "v_mov_b32 v50, %[negm]\n\t" \
  "v_mov_b32 v51, %[negm]\n\t" \
  "v_mov_b32 v52, 0\n\t" \
  "v_mov_b32 v53, 0\n\t" \
  "v_mov_b32 v54, 0\n\t" \
  "v_mov_b32 v55, 0\n\t" \
  "v_mov_b32 v56, 0\n\t" \
  "v_mov_b32 v57, 0\n\t" \
  "v_mov_b32 v58, 0\n\t" \
  "v_mov_b32 v59, 0\n\t" \
  "v_mov_b32 v60, 0\n\t" \
  "v_mov_b32 v61, 0\n\t" \
  "v_mov_b32 v62, 0\n\t" \
  "v_mov_b32 v63, 0\n\t" \
  "v_mov_b32 v64, 0\n\t" \
  "v_mov_b32 v65, 0\n\t" \
  "v_mov_b32 v66, 0\n\t" \
  "v_mov_b32 v67, 0\n\t" \
  "v_mov_b32 v104, %[ones]\n\t" \
  "v_mov_b32 v108, %[ones]\n\t" \
  "v_mov_b32 v116, 0\n\t" \
  "v_mov_b32 v105, %[ones]\n\t" \
  "v_mov_b32 v109, %[ones]\n\t" \
  "v_mov_b32 v117, 0\n\t" \
  "v_mov_b32 v106, %[ones]\n\t" \
  "v_mov_b32 v110, %[ones]\n\t" \
  "v_mov_b32 v118, 0\n\t" \
  "v_mov_b32 v107, %[ones]\n\t" \
  "v_mov_b32 v111, %[ones]\n\t" \
  "v_mov_b32 v119, 0\n\t" \
  "s_mov_b32 %[scur], 0\n\t" \
  "s_mov_b32 %[snext], 0x4000\n\t" \
  "s_mov_b32 %[sfree], 0x8000\n\t" \
  "s_mov_b32 %[cnt], 8\n\t" \
  "ds_read_b128 v[100:103], %[lk]\n\t" \
  "s_waitcnt lgkmcnt(0)\n\t" \
  "v_mfma_f32_32x32x16_bf16 v[68:83], v[100:103], v[32:35], v[36:51]\n\t" \
  "ds_read_b128 v[100:103], %[lk] offset:1024\n\t" \
  "s_mov_b64 exec, %[vmask]\n\t" \
  "ds_read_b128 v[104:107], %[lv]\n\t" \
  "s_mov_b64 exec, -1\n\t" \
  "s_nop 7\n\t" \
  "1:\n\t" \
  "v_add_u32 v120, %[scur], %[lk]\n\t" \
  "v_add_u32 v121, %[snext], %[lk]\n\t" \
  "v_add_u32 v122, %[scur], %[lv]\n\t" \
  "v_add_u32 v123, %[snext], %[lv]\n\t" \
  "s_waitcnt lgkmcnt(1)\n\t" \
  "v_mfma_f32_32x32x16_bf16 v[84:99], v[100:103], v[32:35], v[36:51]\n\t" \
  "ds_read_b128 v[100:103], v120 offset:2048\n\t" \
  "v_mfma_f32_32x32x16_bf16 v[52:67], v[108:111], v[116:119], v[52:67]\n\t" \
  "s_mov_b64 exec, %[vmask]\n\t" \
  "ds_read_b128 v[108:111], v122 offset:512\n\t" \
  "s_mov_b64 exec, -1\n\t" \
  "v_exp_f32 v112, v69\n\t" \
  "v_exp_f32 v68, v68\n\t" \
  "v_exp_f32 v113, v71\n\t" \
  "v_mov_b32_sdwa v112, v68 dst_sel:WORD_0 dst_unused:UNUSED_PRESERVE src0_sel:WORD_1\n\t" \
  "v_exp_f32 v70, v70\n\t" \
  "v_exp_f32 v114, v73\n\t" \
  "v_mov_b32_sdwa v113, v70 dst_sel:WORD_0 dst_unused:UNUSED_PRESERVE src0_sel:WORD_1\n\t" \
  "v_exp_f32 v72, v72\n\t" \
  "v_exp_f32 v115, v75\n\t" \
  "v_mov_b32_sdwa v114, v72 dst_sel:WORD_0 dst_unused:UNUSED_PRESERVE src0_sel:WORD_1\n\t" \
  "v_exp_f32 v74, v74\n\t" \
  "v_exp_f32 v116, v77\n\t" \
  "v_mov_b32_sdwa v115, v74 dst_sel:WORD_0 dst_unused:UNUSED_PRESERVE src0_sel:WORD_1\n\t" \
  "v_exp_f32 v76, v76\n\t" \
  "v_exp_f32 v117, v79\n\t" \
  "s_waitcnt lgkmcnt(2)\n\t" \
  "v_mfma_f32_32x32x16_bf16 v[52:67], v[104:107], v[112:115], v[52:67]\n\t" \
  "s_mov_b64 exec, %[vmask]\n\t" \
  "ds_read_b128 v[104:107], v122 offset:1024\n\t" \
  "s_mov_b64 exec, -1\n\t" \
  "v_mov_b32_sdwa v116, v76 dst_sel:WORD_0 dst_unused:UNUSED_PRESERVE src0_sel:WORD_1\n\t" \
  "v_exp_f32 v78, v78\n\t" \
  "v_exp_f32 v118, v81\n\t" \
  "v_mov_b32_sdwa v117, v78 dst_sel:WORD_0 dst_unused:UNUSED_PRESERVE src0_sel:WORD_1\n\t" \
  "v_exp_f32 v80, v80\n\t" \
  "v_exp_f32 v119, v83\n\t" \
  "v_mov_b32_sdwa v118, v80 dst_sel:WORD_0 dst_unused:UNUSED_PRESERVE src0_sel:WORD_1\n\t" \
  "v_exp_f32 v82, v82\n\t" \
  "s_nop 0\n\t" \
  "v_mov_b32_sdwa v119, v82 dst_sel:WORD_0 dst_unused:UNUSED_PRESERVE src0_sel:WORD_1\n\t" \
  "s_waitcnt lgkmcnt(1)\n\t" \
  "v_mfma_f32_32x32x16_bf16 v[68:83], v[100:103], v[32:35], v[36:51]\n\t" \
  "ds_read_b128 v[100:103], v120 offset:3072\n\t" \
  "v_mfma_f32_32x32x16_bf16 v[52:67], v[108:111], v[116:119], v[52:67]\n\t" \
  "s_mov_b64 exec, %[vmask]\n\t" \
  "ds_read_b128 v[108:111], v122 offset:1536\n\t" \
  "s_mov_b64 exec, -1\n\t" \
  "v_exp_f32 v112, v85\n\t" \
  "v_exp_f32 v84, v84\n\t" \
  "v_exp_f32 v113, v87\n\t" \
  "v_mov_b32_sdwa v112, v84 dst_sel:WORD_0 dst_unused:UNUSED_PRESERVE src0_sel:WORD_1\n\t" \
  "v_exp_f32 v86, v86\n\t" \
  "v_exp_f32 v114, v89\n\t" \
  "v_mov_b32_sdwa v113, v86 dst_sel:WORD_0 dst_unused:UNUSED_PRESERVE src0_sel:WORD_1\n\t" \
  "v_exp_f32 v88, v88\n\t" \
  "v_exp_f32 v115, v91\n\t" \
  "v_mov_b32_sdwa v114, v88 dst_sel:WORD_0 dst_unused:UNUSED_PRESERVE src0_sel:WORD_1\n\t" \
  "v_exp_f32 v90, v90\n\t" \
  "v_exp_f32 v116, v93\n\t" \
  "v_mov_b32_sdwa v115, v90 dst_sel:WORD_0 dst_unused:UNUSED_PRESERVE src0_sel:WORD_1\n\t" \
  "v_exp_f32 v92, v92\n\t" \
  "v_exp_f32 v117, v95\n\t" \
  "s_waitcnt lgkmcnt(2)\n\t" \
  "v_mfma_f32_32x32x16_bf16 v[52:67], v[104:107], v[112:115], v[52:67]\n\t" \
  "s_mov_b64 exec, %[vmask]\n\t" \
  "ds_read_b128 v[104:107], v122 offset:2048\n\t" \
  "s_mov_b64 exec, -1\n\t" \
  "v_mov_b32_sdwa v116, v92 dst_sel:WORD_0 dst_unused:UNUSED_PRESERVE src0_sel:WORD_1\n\t" \
  "v_exp_f32 v94, v94\n\t" \
  "v_exp_f32 v118, v97\n\t" \
  "v_mov_b32_sdwa v117, v94 dst_sel:WORD_0 dst_unused:UNUSED_PRESERVE src0_sel:WORD_1\n\t" \
  "v_exp_f32 v96, v96\n\t" \
  "v_exp_f32 v119, v99\n\t" \
  "v_mov_b32_sdwa v118, v96 dst_sel:WORD_0 dst_unused:UNUSED_PRESERVE src0_sel:WORD_1\n\t" \
  "v_exp_f32 v98, v98\n\t" \
  "s_nop 0\n\t" \
  "v_mov_b32_sdwa v119, v98 dst_sel:WORD_0 dst_unused:UNUSED_PRESERVE src0_sel:WORD_1\n\t" \
  "s_waitcnt lgkmcnt(1)\n\t" \
  "v_mfma_f32_32x32x16_bf16 v[84:99], v[100:103], v[32:35], v[36:51]\n\t" \
  "ds_read_b128 v[100:103], v120 offset:4096\n\t" \
  "v_mfma_f32_32x32x16_bf16 v[52:67], v[108:111], v[116:119], v[52:67]\n\t" \
  "s_mov_b64 exec, %[vmask]\n\t" \
  "ds_read_b128 v[108:111], v122 offset:2560\n\t" \
  "s_mov_b64 exec, -1\n\t" \
  "v_exp_f32 v112, v69\n\t" \
  "v_exp_f32 v68, v68\n\t" \
  "v_exp_f32 v113, v71\n\t" \
  "v_mov_b32_sdwa v112, v68 dst_sel:WORD_0 dst_unused:UNUSED_PRESERVE src0_sel:WORD_1\n\t" \
  "v_exp_f32 v70, v70\n\t" \
  "v_exp_f32 v114, v73\n\t" \
  "v_mov_b32_sdwa v113, v70 dst_sel:WORD_0 dst_unused:UNUSED_PRESERVE src0_sel:WORD_1\n\t" \
  "v_exp_f32 v72, v72\n\t" \
  "v_exp_f32 v115, v75\n\t" \
  "v_mov_b32_sdwa v114, v72 dst_sel:WORD_0 dst_unused:UNUSED_PRESERVE src0_sel:WORD_1\n\t" \
  "v_exp_f32 v74, v74\n\t" \
  "v_exp_f32 v116, v77\n\t" \
  "v_mov_b32_sdwa v115, v74 dst_sel:WORD_0 dst_unused:UNUSED_PRESERVE src0_sel:WORD_1\n\t" \
  "v_exp_f32 v76, v76\n\t" \
  "v_exp_f32 v117, v79\n\t" \
  "s_waitcnt lgkmcnt(2)\n\t" \
  "v_mfma_f32_32x32x16_bf16 v[52:67], v[104:107], v[112:115], v[52:67]\n\t" \
  "s_mov_b64 exec, %[vmask]\n\t" \
  "ds_read_b128 v[104:107], v122 offset:3072\n\t" \
  "s_mov_b64 exec, -1\n\t" \
  "v_mov_b32_sdwa v116, v76 dst_sel:WORD_0 dst_unused:UNUSED_PRESERVE src0_sel:WORD_1\n\t" \
  "v_exp_f32 v78, v78\n\t" \
  "v_exp_f32 v118, v81\n\t" \
  "v_mov_b32_sdwa v117, v78 dst_sel:WORD_0 dst_unused:UNUSED_PRESERVE src0_sel:WORD_1\n\t" \
  "v_exp_f32 v80, v80\n\t" \
  "v_exp_f32 v119, v83\n\t" \
  "v_mov_b32_sdwa v118, v80 dst_sel:WORD_0 dst_unused:UNUSED_PRESERVE src0_sel:WORD_1\n\t" \
  "v_exp_f32 v82, v82\n\t" \
  "s_nop 0\n\t" \
  "v_mov_b32_sdwa v119, v82 dst_sel:WORD_0 dst_unused:UNUSED_PRESERVE src0_sel:WORD_1\n\t" \
  "s_waitcnt lgkmcnt(1)\n\t" \
  "v_mfma_f32_32x32x16_bf16 v[68:83], v[100:103], v[32:35], v[36:51]\n\t" \
  "ds_read_b128 v[100:103], v120 offset:5120\n\t" \
  "v_mfma_f32_32x32x16_bf16 v[52:67], v[108:111], v[116:119], v[52:67]\n\t" \
  "s_mov_b64 exec, %[vmask]\n\t" \
  "ds_read_b128 v[108:111], v122 offset:3584\n\t" \
  "s_mov_b64 exec, -1\n\t" \
  "v_exp_f32 v112, v85\n\t" \
  "v_exp_f32 v84, v84\n\t" \
  "v_exp_f32 v113, v87\n\t" \
  "v_mov_b32_sdwa v112, v84 dst_sel:WORD_0 dst_unused:UNUSED_PRESERVE src0_sel:WORD_1\n\t" \
  "v_exp_f32 v86, v86\n\t" \
  "v_exp_f32 v114, v89\n\t" \
  "v_mov_b32_sdwa v113, v86 dst_sel:WORD_0 dst_unused:UNUSED_PRESERVE src0_sel:WORD_1\n\t" \
  "v_exp_f32 v88, v88\n\t" \
  "v_exp_f32 v115, v91\n\t" \
  "v_mov_b32_sdwa v114, v88 dst_sel:WORD_0 dst_unused:UNUSED_PRESERVE src0_sel:WORD_1\n\t" \
  "v_exp_f32 v90, v90\n\t" \
  "v_exp_f32 v116, v93\n\t" \
  "v_mov_b32_sdwa v115, v90 dst_sel:WORD_0 dst_unused:UNUSED_PRESERVE src0_sel:WORD_1\n\t" \
  "v_exp_f32 v92, v92\n\t" \
  "v_exp_f32 v117, v95\n\t" \
  "s_waitcnt lgkmcnt(2)\n\t" \
  "v_mfma_f32_32x32x16_bf16 v[52:67], v[104:107], v[112:115], v[52:67]\n\t" \
  "s_mov_b64 exec, %[vmask]\n\t" \
  "ds_read_b128 v[104:107], v122 offset:4096\n\t" \
  "s_mov_b64 exec, -1\n\t" \
  "v_mov_b32_sdwa v116, v92 dst_sel:WORD_0 dst_unused:UNUSED_PRESERVE src0_sel:WORD_1\n\t" \
  "v_exp_f32 v94, v94\n\t" \
  "v_exp_f32 v118, v97\n\t" \
  "v_mov_b32_sdwa v117, v94 dst_sel:WORD_0 dst_unused:UNUSED_PRESERVE src0_sel:WORD_1\n\t" \
  "v_exp_f32 v96, v96\n\t" \
  "v_exp_f32 v119, v99\n\t" \
  "v_mov_b32_sdwa v118, v96 dst_sel:WORD_0 dst_unused:UNUSED_PRESERVE src0_sel:WORD_1\n\t" \
  "v_exp_f32 v98, v98\n\t" \
  "s_nop 0\n\t" \
  "v_mov_b32_sdwa v119, v98 dst_sel:WORD_0 dst_unused:UNUSED_PRESERVE src0_sel:WORD_1\n\t" \
  "s_waitcnt lgkmcnt(1)\n\t" \
  "v_mfma_f32_32x32x16_bf16 v[84:99], v[100:103], v[32:35], v[36:51]\n\t" \
  "ds_read_b128 v[100:103], v120 offset:6144\n\t" \
  "v_mfma_f32_32x32x16_bf16 v[52:67], v[108:111], v[116:119], v[52:67]\n\t" \
  "s_mov_b64 exec, %[vmask]\n\t" \
  "ds_read_b128 v[108:111], v122 offset:4608\n\t" \
  "s_mov_b64 exec, -1\n\t" \
  "v_exp_f32 v112, v69\n\t" \
  "v_exp_f32 v68, v68\n\t" \
  "v_exp_f32 v113, v71\n\t" \
  "v_mov_b32_sdwa v112, v68 dst_sel:WORD_0 dst_unused:UNUSED_PRESERVE src0_sel:WORD_1\n\t" \
  "v_exp_f32 v70, v70\n\t" \
  "v_exp_f32 v114, v73\n\t" \
  "v_mov_b32_sdwa v113, v70 dst_sel:WORD_0 dst_unused:UNUSED_PRESERVE src0_sel:WORD_1\n\t" \
  "v_exp_f32 v72, v72\n\t" \
  "v_exp_f32 v115, v75\n\t" \
  "v_mov_b32_sdwa v114, v72 dst_sel:WORD_0 dst_unused:UNUSED_PRESERVE src0_sel:WORD_1\n\t" \
  "v_exp_f32 v74, v74\n\t" \
  "v_exp_f32 v116, v77\n\t" \
  "v_mov_b32_sdwa v115, v74 dst_sel:WORD_0 dst_unused:UNUSED_PRESERVE src0_sel:WORD_1\n\t" \
  "v_exp_f32 v76, v76\n\t" \
  "v_exp_f32 v117, v79\n\t" \
  "s_waitcnt lgkmcnt(2)\n\t" \
  "v_mfma_f32_32x32x16_bf16 v[52:67], v[104:107], v[112:115], v[52:67]\n\t" \
  "s_mov_b64 exec, %[vmask]\n\t" \
  "ds_read_b128 v[104:107], v122 offset:5120\n\t" \
  "s_mov_b64 exec, -1\n\t" \
  "v_mov_b32_sdwa v116, v76 dst_sel:WORD_0 dst_unused:UNUSED_PRESERVE src0_sel:WORD_1\n\t" \
  "v_exp_f32 v78, v78\n\t" \
  "v_exp_f32 v118, v81\n\t" \
  "v_mov_b32_sdwa v117, v78 dst_sel:WORD_0 dst_unused:UNUSED_PRESERVE src0_sel:WORD_1\n\t" \
  "v_exp_f32 v80, v80\n\t" \
  "v_exp_f32 v119, v83\n\t" \
  "v_mov_b32_sdwa v118, v80 dst_sel:WORD_0 dst_unused:UNUSED_PRESERVE src0_sel:WORD_1\n\t" \
  "v_exp_f32 v82, v82\n\t" \
  "s_waitcnt vmcnt(0)\n\t" \
  "s_barrier\n\t" \
  "s_add_u32 m0, %[sfree], %[m0k]\n\t" \
  "s_nop 0\n\t" \
  "buffer_load_dwordx4 %[goff], %[rk], %[ssrc] offen lds\n\t" \
  "s_add_u32 m0, m0, 0x2000\n\t" \
  "s_nop 0\n\t" \
  "buffer_load_dwordx4 %[goff], %[rv], %[ssrc] offen lds\n\t" \
  "s_add_u32 %[ssrc], %[ssrc], 0x2000\n\t" \
  "v_mov_b32_sdwa v119, v82 dst_sel:WORD_0 dst_unused:UNUSED_PRESERVE src0_sel:WORD_1\n\t" \
  "s_waitcnt lgkmcnt(1)\n\t" \
  "v_mfma_f32_32x32x16_bf16 v[68:83], v[100:103], v[32:35], v[36:51]\n\t" \
  "ds_read_b128 v[100:103], v120 offset:7168\n\t" \
  "v_mfma_f32_32x32x16_bf16 v[52:67], v[108:111], v[116:119], v[52:67]\n\t" \
  "s_mov_b64 exec, %[vmask]\n\t" \
  "ds_read_b128 v[108:111], v122 offset:5632\n\t" \
  "s_mov_b64 exec, -1\n\t" \
  "v_exp_f32 v112, v85\n\t" \
  "v_exp_f32 v84, v84\n\t" \
  "v_exp_f32 v113, v87\n\t" \
  "v_mov_b32_sdwa v112, v84 dst_sel:WORD_0 dst_unused:UNUSED_PRESERVE src0_sel:WORD_1\n\t" \
  "v_exp_f32 v86, v86\n\t" \
  "v_exp_f32 v114, v89\n\t" \
  "v_mov_b32_sdwa v113, v86 dst_sel:WORD_0 dst_unused:UNUSED_PRESERVE src0_sel:WORD_1\n\t" \
  "v_exp_f32 v88, v88\n\t" \
  "v_exp_f32 v115, v91\n\t" \
  "v_mov_b32_sdwa v114, v88 dst_sel:WORD_0 dst_unused:UNUSED_PRESERVE src0_sel:WORD_1\n\t" \
  "v_exp_f32 v90, v90\n\t" \
  "v_exp_f32 v116, v93\n\t" \
  "v_mov_b32_sdwa v115, v90 dst_sel:WORD_0 dst_unused:UNUSED_PRESERVE src0_sel:WORD_1\n\t" \
  "v_exp_f32 v92, v92\n\t" \
  "v_exp_f32 v117, v95\n\t" \
  "s_waitcnt lgkmcnt(2)\n\t" \
  "v_mfma_f32_32x32x16_bf16 v[52:67], v[104:107], v[112:115], v[52:67]\n\t" \
  "s_mov_b64 exec, %[vmask]\n\t" \
  "ds_read_b128 v[104:107], v122 offset:6144\n\t" \
  "s_mov_b64 exec, -1\n\t" \
  "v_mov_b32_sdwa v116, v92 dst_sel:WORD_0 dst_unused:UNUSED_PRESERVE src0_sel:WORD_1\n\t" \
  "v_exp_f32 v94, v94\n\t" \
  "v_exp_f32 v118, v97\n\t" \
  "v_mov_b32_sdwa v117, v94 dst_sel:WORD_0 dst_unused:UNUSED_PRESERVE src0_sel:WORD_1\n\t" \
  "v_exp_f32 v96, v96\n\t" \
  "v_exp_f32 v119, v99\n\t" \
  "v_mov_b32_sdwa v118, v96 dst_sel:WORD_0 dst_unused:UNUSED_PRESERVE src0_sel:WORD_1\n\t" \
  "v_exp_f32 v98, v98\n\t" \
  "s_nop 0\n\t" \
  "v_mov_b32_sdwa v119, v98 dst_sel:WORD_0 dst_unused:UNUSED_PRESERVE src0_sel:WORD_1\n\t" \
  "s_waitcnt lgkmcnt(1)\n\t" \
  "v_mfma_f32_32x32x16_bf16 v[84:99], v[100:103], v[32:35], v[36:51]\n\t" \
  "ds_read_b128 v[100:103], v121 offset:0\n\t" \
  "v_mfma_f32_32x32x16_bf16 v[52:67], v[108:111], v[116:119], v[52:67]\n\t" \
  "s_mov_b64 exec, %[vmask]\n\t" \
  "ds_read_b128 v[108:111], v122 offset:6656\n\t" \
  "s_mov_b64 exec, -1\n\t" \
  "v_exp_f32 v112, v69\n\t" \
  "v_exp_f32 v68, v68\n\t" \
  "v_exp_f32 v113, v71\n\t" \
  "v_mov_b32_sdwa v112, v68 dst_sel:WORD_0 dst_unused:UNUSED_PRESERVE src0_sel:WORD_1\n\t" \
  "v_exp_f32 v70, v70\n\t" \
  "v_exp_f32 v114, v73\n\t" \
  "v_mov_b32_sdwa v113, v70 dst_sel:WORD_0 dst_unused:UNUSED_PRESERVE src0_sel:WORD_1\n\t" \
  "v_exp_f32 v72, v72\n\t" \
  "v_exp_f32 v115, v75\n\t" \
  "v_mov_b32_sdwa v114, v72 dst_sel:WORD_0 dst_unused:UNUSED_PRESERVE src0_sel:WORD_1\n\t" \
  "v_exp_f32 v74, v74\n\t" \
  "v_exp_f32 v116, v77\n\t" \
  "v_mov_b32_sdwa v115, v74 dst_sel:WORD_0 dst_unused:UNUSED_PRESERVE src0_sel:WORD_1\n\t" \
  "v_exp_f32 v76, v76\n\t" \
  "v_exp_f32 v117, v79\n\t" \
  "s_waitcnt lgkmcnt(2)\n\t" \
  "v_mfma_f32_32x32x16_bf16 v[52:67], v[104:107], v[112:115], v[52:67]\n\t" \
  "s_mov_b64 exec, %[vmask]\n\t" \
  "ds_read_b128 v[104:107], v122 offset:7168\n\t" \
  "s_mov_b64 exec, -1\n\t" \
  "v_mov_b32_sdwa v116, v76 dst_sel:WORD_0 dst_unused:UNUSED_PRESERVE src0_sel:WORD_1\n\t" \
  "v_exp_f32 v78, v78\n\t" \
  "v_exp_f32 v118, v81\n\t" \
  "v_mov_b32_sdwa v117, v78 dst_sel:WORD_0 dst_unused:UNUSED_PRESERVE src0_sel:WORD_1\n\t" \
  "v_exp_f32 v80, v80\n\t" \
  "v_exp_f32 v119, v83\n\t" \
  "v_mov_b32_sdwa v118, v80 dst_sel:WORD_0 dst_unused:UNUSED_PRESERVE src0_sel:WORD_1\n\t" \
  "v_exp_f32 v82, v82\n\t" \
  "s_nop 0\n\t" \
  "v_mov_b32_sdwa v119, v82 dst_sel:WORD_0 dst_unused:UNUSED_PRESERVE src0_sel:WORD_1\n\t" \
  "s_waitcnt lgkmcnt(1)\n\t" \
  "v_mfma_f32_32x32x16_bf16 v[68:83], v[100:103], v[32:35], v[36:51]\n\t" \
  "ds_read_b128 v[100:103], v121 offset:1024\n\t" \
  "v_mfma_f32_32x32x16_bf16 v[52:67], v[108:111], v[116:119], v[52:67]\n\t" \
  "s_mov_b64 exec, %[vmask]\n\t" \
  "ds_read_b128 v[108:111], v122 offset:7680\n\t" \
  "s_mov_b64 exec, -1\n\t" \
  "v_exp_f32 v112, v85\n\t" \
  "v_exp_f32 v84, v84\n\t" \
  "v_exp_f32 v113, v87\n\t" \
  "v_mov_b32_sdwa v112, v84 dst_sel:WORD_0 dst_unused:UNUSED_PRESERVE src0_sel:WORD_1\n\t" \
  "v_exp_f32 v86, v86\n\t" \
  "v_exp_f32 v114, v89\n\t" \
  "v_mov_b32_sdwa v113, v86 dst_sel:WORD_0 dst_unused:UNUSED_PRESERVE src0_sel:WORD_1\n\t" \
  "v_exp_f32 v88, v88\n\t" \
  "v_exp_f32 v115, v91\n\t" \
  "v_mov_b32_sdwa v114, v88 dst_sel:WORD_0 dst_unused:UNUSED_PRESERVE src0_sel:WORD_1\n\t" \
  "v_exp_f32 v90, v90\n\t" \
  "v_exp_f32 v116, v93\n\t" \
  "v_mov_b32_sdwa v115, v90 dst_sel:WORD_0 dst_unused:UNUSED_PRESERVE src0_sel:WORD_1\n\t" \
  "v_exp_f32 v92, v92\n\t" \
  "v_exp_f32 v117, v95\n\t" \
  "s_waitcnt lgkmcnt(2)\n\t" \
  "v_mfma_f32_32x32x16_bf16 v[52:67], v[104:107], v[112:115], v[52:67]\n\t" \
  "s_mov_b64 exec, %[vmask]\n\t" \
  "ds_read_b128 v[104:107], v123 offset:0\n\t" \
  "s_mov_b64 exec, -1\n\t" \
  "v_mov_b32_sdwa v116, v92 dst_sel:WORD_0 dst_unused:UNUSED_PRESERVE src0_sel:WORD_1\n\t" \
  "v_exp_f32 v94, v94\n\t" \
  "v_exp_f32 v118, v97\n\t" \
  "v_mov_b32_sdwa v117, v94 dst_sel:WORD_0 dst_unused:UNUSED_PRESERVE src0_sel:WORD_1\n\t" \
  "v_exp_f32 v96, v96\n\t" \
  "v_exp_f32 v119, v99\n\t" \
  "v_mov_b32_sdwa v118, v96 dst_sel:WORD_0 dst_unused:UNUSED_PRESERVE src0_sel:WORD_1\n\t" \
  "v_exp_f32 v98, v98\n\t" \
  "s_nop 0\n\t" \
  "v_mov_b32_sdwa v119, v98 dst_sel:WORD_0 dst_unused:UNUSED_PRESERVE src0_sel:WORD_1\n\t" \
  "s_mov_b32 %[stmp], %[scur]\n\t" \
  "s_mov_b32 %[scur], %[snext]\n\t" \
  "s_mov_b32 %[snext], %[sfree]\n\t" \
  "s_mov_b32 %[sfree], %[stmp]\n\t" \
  "s_sub_u32 %[cnt], %[cnt], 1\n\t" \
  "s_cmp_lg_u32 %[cnt], 0\n\t" \
  "s_cbranch_scc1 1b\n\t" \
  "s_waitcnt lgkmcnt(0)\n\t" \
  "v_mfma_f32_32x32x16_bf16 v[52:67], v[108:111], v[116:119], v[52:67]\n\t" \
  "s_waitcnt vmcnt(0)\n\t" \
  "s_nop 15\n\t"
#define ATTN_CLOBBERS "v36", "v37", "v38", "v39", "v40", "v41", "v42", "v43", "v44", "v45", "v46", "v47", "v48", "v49", "v50", "v51", "v68", "v69", "v70", "v71", "v72", "v73", "v74", "v75", "v76", "v77", "v78", "v79", "v80", "v81", "v82", "v83", "v84", "v85", "v86", "v87", "v88", "v89", "v90", "v91", "v92", "v93", "v94", "v95", "v96", "v97", "v98", "v99", "v100", "v101", "v102", "v103", "v104", "v105", "v106", "v107", "v108", "v109", "v110", "v111", "v112", "v113", "v114", "v115", "v116", "v117", "v118", "v119", "v120", "v121", "v122", "v123", "m0", "scc", "memory"

constexpr int AT_SLOT = 16384;
DEV void attn_role(char* lds, int a, char* ws) {
#if defined(__HIP_DEVICE_COMPILE__)
  STAMP_DECL
  RSTAMP(0); STAMP(1);
  const int head = (a & 7) * 4 + (a >> 6), qb = (a >> 3) & 7;
  const int tid = threadIdx.x, lane = tid & 63, l31 = lane & 31, h = lane >> 5;
  const int wid = __builtin_amdgcn_readfirstlane(tid >> 6);
  auto krs = mk_rsrc(ws + OFF_K + (size_t)head * (S_ * 32), S_ * 32);
  auto vrs = mk_rsrc(ws + OFF_V + (size_t)head * (S_ * 32), S_ * 32);
  const int q = qb * 256 + wid * 32 + l31;
  bf16x8 qf = *(const bf16x8*)(ws + OFF_Q + (size_t)head * (S_ * 32) + q * 32 + h * 16);
  auto issue = [&](int c, int slot) {
    char* sb = lds + slot * AT_SLOT + wid * 1024;
    dma16(krs, sb, lane * 16, c * 8192 + wid * 1024);
    dma16(vrs, sb + 8192, lane * 16, c * 8192 + wid * 1024);
  };
  const unsigned ldsb = (unsigned)(size_t)lds;
  const unsigned lk = ldsb + lane * 16;
  const unsigned lv = ldsb + 8192 + h * 256 + (l31 & 15) * 16;
  const unsigned goff = lane * 16;
  const unsigned m0k = ldsb + wid * 1024;
  int* flag = (int*)(lds + 3 * AT_SLOT);
  float mref = 0.f;
  f32x16 o;
  for (int attempt = 0; attempt < 2; ++attempt) {
    if (attempt == 1) {
      float mx = -__builtin_inff();
      for (int c = 0; c < 8; ++c) {
        issue(c, 0);
        WAIT_V(0); RAW_BARRIER();
        for (int t = 0; t < 8; ++t) {
          bf16x8 kf = *(const bf16x8*)(lds + t * 1024 + lane * 16);
          f32x16 z = {};
          f32x16 s = mfma32(kf, qf, z);
#pragma unroll
          for (int r = 0; r < 16; ++r) mx = fmaxf(mx, s[r]);
        }
        RAW_BARRIER();
      }
      mx = fmaxf(mx, __shfl_xor(mx, 32));
      mref = mx;
    }
    STAMP(2);
    issue(0, 0);
    issue(1, 1);
    STAMP(3);
    WAIT_V(2); RAW_BARRIER();
    STAMP(4);
    if (attempt == 0) {
      bf16x8 kf = *(const bf16x8*)(lds + lane * 16);
      f32x16 z = {};
      f32x16 s = mfma32(kf, qf, z);
      float mx = s[0];
#pragma unroll
      for (int r = 1; r < 16; ++r) mx = fmaxf(mx, s[r]);
      mx = fmaxf(mx, __shfl_xor(mx, 32));
      mref = mx;
    }
    STAMP(5);
    {
      float negm = -mref;
      unsigned scur, snext, sfree, stmp, cnt, ssrc = 2 * 8192 + wid * 1024;
      unsigned long long vmask = 0x0000FFFF0000FFFFull;
      const unsigned ones = (l31 == 16 || l31 == 20) ? 0x3f803f80u : 0u;
      asm volatile(ATTN_ASM
                   : "={v[52:67]}"(o), [scur] "=&s"(scur), [snext] "=&s"(snext), [sfree] "=&s"(sfree), [stmp] "=&s"(stmp),
                     [cnt] "=&s"(cnt), [ssrc] "+s"(ssrc)
                   : "{v[32:35]}"(qf), [negm] "v"(negm), [lk] "v"(lk), [lv] "v"(lv), [goff] "v"(goff), [rk] "s"(krs), [rv] "s"(vrs),
                     [m0k] "s"(m0k), [vmask] "s"(vmask), [ones] "v"(ones)
                   : ATTN_CLOBBERS);
    }
    STAMP(40);
    bool bad = !(o[8] < 1.0e30f);
#pragma unroll
    for (int r = 0; r < 8; ++r) bad |= !(fabsf(o[r]) < 3.0e38f);
    if (tid == 0) *flag = 0;
    RAW_BARRIER();
    if (attempt == 0 && __any(bad)) { if (lane == 0) *flag = 1; }
    RAW_BARRIER();
    int f = *flag;
    RAW_BARRIER();
    if (f == 0) break;
  }
  float inv = 1.0f / o[8];
  u32x4 ov = widen(pk4(o[0] * inv, o[1] * inv, o[2] * inv, o[3] * inv), pk4(o[4] * inv, o[5] * inv, o[6] * inv, o[7] * inv));
  {
    const unsigned l2 = fresh_lane();
    auto ors = mk_rsrc(ws + OFF_O, 64 * S_ * 16);
    __builtin_amdgcn_raw_buffer_store_b128(ov, ors, ((l2 >> 5) * S_ + (l2 & 31)) * 16, (2 * head * S_ + qb * 256 + wid * 32) * 16, 0);
  }
  STAMP(41); RSTAMP(42); STAMP_FLUSH(0);
#endif
}

namespace bsched {
constexpr int NG = 46;
constexpr int pos(int g) {
  if (g < 8) return 8 * g;
  if (g < 24) return 12 * ((g - 8) % 6);
  int u = (g - 24) % 10;
  return u == 0 ? 72 : u == 1 ? 48 : u == 2 ? 56 : u == 3 ? 64 : 8 * (u - 4);
}
constexpr int npw(int g) { return g < 8 ? 2 : g < 24 ? 3 : g == 45 ? 1 : 2; }
constexpr int iss(int g) {
  if (g < 8) return -1;
  if (g < 24) { int t = g - 8; return t == 0 ? 3 : t == 1 ? 4 : t == 2 ? 6 : t == 3 ? 7 : t == 4 ? 8 : t == 5 ? 9 : t + 6; }
  int u = g - 24;
  return u == 0 ? 9 : u == 1 ? 22 : u == 2 ? 23 : u == 3 ? 23 : u == 4 ? 24 : u == 5 ? 25 : u == 6 ? 25 : u <= 9 ? 26 : u + 19;
}
constexpr int pub(int g) { return g < 8 ? g : g < 24 ? g + 2 : g + 3; }
constexpr int pubbed(int bi) { return bi < 8 ? bi : (bi >= 10 && bi <= 25) ? bi - 2 : (bi >= 27 && bi <= 48) ? bi - 3 : -1; }
constexpr bool before(int a, int b) { return iss(a) != iss(b) ? iss(a) < iss(b) : a < b; }
constexpr int waitcnt(int g) {
  int n = 0;
  for (int o = 0; o < NG; ++o) if (o != g && before(g, o) && iss(o) < pub(g)) n += npw(o);
  return n;
}
}

DEV u32x4 ld_sc1(__amdgpu_buffer_rsrc_t r, unsigned voff, unsigned soff) { return __builtin_amdgcn_raw_buffer_load_b128(r, voff, soff, 16); }
DEV void st_sc1(__amdgpu_buffer_rsrc_t r, u32x4 v, unsigned voff, unsigned soff) { __builtin_amdgcn_raw_buffer_store_b128(v, r, voff, soff, 16); }

DEV void b_signal_wait(unsigned* cnt, int lane) {
  if (lane == 0) {
    __hip_atomic_fetch_add(cnt, 1u, __ATOMIC_RELAXED, __HIP_MEMORY_SCOPE_AGENT);
    for (int it = 0; it < (1 << 22); ++it) {
      if (__hip_atomic_load(cnt, __ATOMIC_RELAXED, __HIP_MEMORY_SCOPE_AGENT) >= 8u) break;
      __builtin_amdgcn_s_sleep(2);
    }
  }
}

DEV void b_loader(char* lds, int lw, int lane, int grp, int j, int c0, const u16* w2, const u16* w3, const u16* w4, char* ws) {
#if defined(__HIP_DEVICE_COMPILE__)
  using namespace bsched;
  auto r2 = mk_rsrc(w2, D_ * D_ * 2);
  auto r3 = mk_rsrc(w3, D_ * F_ * 2);
  auto r4 = mk_rsrc(w4, F_ * D_ * 2);
  unsigned va[2], vb[3];
#pragma unroll
  for (int i = 0; i < 2; ++i) {
    unsigned bb = (lw + 4 * i) * 1024 + lane * 16, row = bb >> 7, cp = (bb >> 4) & 7;
    va[i] = row * 1024 + j * 128 + ((cp ^ (((row >> 1) & 1) << 2)) << 4);
  }
#pragma unroll
  for (int i = 0; i < 3; ++i) {
    unsigned bb = (lw + 4 * i) * 1024 + lane * 16, row = bb / 384, cp = (bb % 384) >> 4;
    vb[i] = row * (F_ * 2) + c0 * 2 + ((cp ^ (((row >> 1) & 1) << 2)) << 4);
  }
  auto issue = [&](auto G) {
    constexpr int g = G;
    char* sb = lds + pos(g) * 1024 + lw * 1024;
    if constexpr (g < 8) { dma16(r2, sb, va[0], g * 65536); dma16(r2, sb + 4096, va[1], g * 65536); }
    else if constexpr (g < 24) {
#pragma unroll
      for (int i = 0; i < 3; ++i) dma16(r3, sb + i * 4096, vb[i], (g - 8) * 32 * (F_ * 2));
    } else {
      dma16(r4, sb, va[0], (g - 24) * 65536);
      if constexpr (g != 45) dma16(r4, sb + 4096, va[1], (g - 24) * 65536);
    }
  };
  static_for<NG>([&](auto G) { if constexpr (iss(G) == -1) issue(G); });
  static_for<50>([&](auto BI) {
    constexpr int bi = BI;
    constexpr int pg = pubbed(bi);
    if constexpr (bi == 10 || bi == 27) {
      if (lw == 0) b_signal_wait((unsigned*)(ws + OFF_CNT) + (bi == 10 ? grp : 32 + grp) * 64, lane);
    }
    if constexpr (pg >= 0) WAIT_V(waitcnt(pg));
    BARRIER_NOWAIT();
    static_for<NG>([&](auto G) { if constexpr (iss(G) == bi) issue(G); });
  });
#endif
}

#define K2_PM1 0
#define K2_PM2 0
#define K2_PM3 0
#define K2_PE1 0
#define K2_PE2 2
#define K2_PE3 2
#define BPRIO(n) do { SCHED_FENCE(); __builtin_amdgcn_s_setprio(n); SCHED_FENCE(); } while (0)
DEV void b_compute(char* lds, int wid, int lane, int grp, int j, int c0, int ntile, const u16* x, char* ws) {
#if defined(__HIP_DEVICE_COMPILE__)
  using namespace bsched;
  STAMP_DECL
  RSTAMP(0); STAMP(1);
  const int wm = wid & 1, kp = wid >> 1;
  const int m0 = grp * 64;
  auto hrs = mk_rsrc(ws + OFF_HB, 64 * HB_ROWS * 16);
  auto x1rs = mk_rsrc(ws + OFF_X1, 64 * S_ * 16);
  auto h2rs = mk_rsrc(ws + OFF_H2, 172 * S_ * 16);
  auto xrs = mk_rsrc(x, S_ * D_ * 2);
  const unsigned col0 = j * 64 + kp * 32;
  unsigned l31, h, blk, q, p, row, sw, cpart, wo13[2];
  auto lane_init = [&]() {
    lane = fresh_lane();
    l31 = lane & 31; h = lane >> 5; blk = (lane >> 4) & 1; q = (lane & 15) >> 2; p = lane & 3;
    row = m0 + wm * 32 + l31; sw = (q >> 1) & 1; cpart = 8 * (p & 1);
#pragma unroll
    for (int a = 0; a < 2; ++a) wo13[a] = (16 * kp + 8 * h + q) * 128 + (((4 * (a ^ kp) + 2 * blk + (p >> 1)) ^ (sw << 2)) << 4) + cpart;
  };
  lane_init();
  u32x2 xv[4];
#pragma unroll
  for (int i = 0; i < 4; ++i) xv[i] = __builtin_amdgcn_raw_buffer_load_b64(xrs, (row * D_ + col0 + 4 * h) * 2, i * 16, 0);
  const f32x2 st0 = *(const f32x2*)(ws + OFF_ST0 + (size_t)row * 8);

  auto phase13 = [&](auto G0_, auto NI_, auto QD_, auto loadA, f32x16 (&acc)[2], auto&& first_issue_done) {
    constexpr int G0 = G0_, NI = NI_, QD = QD_;
    u32x4 aq[QD];
    static_for<QD>([&](auto I) { if constexpr (I < NI) aq[I] = loadA(I); });
    first_issue_done();
    bf16x8 wf[2][2];
    auto rd = [&](auto I) {
      constexpr int i = I;
      const char* sb = lds + pos(G0 + (i >> 1)) * 1024 + (i & 1) * 4096;
      wf[i & 1][0] = tr_frag(sb + wo13[0], sb + wo13[0] + 512);
      wf[i & 1][1] = tr_frag(sb + wo13[1], sb + wo13[1] + 512);
    };
    auto mm = [&](auto I) {
      constexpr int i = I;
      bf16x8 a = __builtin_bit_cast(bf16x8, aq[i % QD]);
      acc[0] = mfma32(wf[i & 1][0], a, acc[0]);
      acc[1] = mfma32(wf[i & 1][1], a, acc[1]);
      if constexpr (i + QD < NI) aq[i % QD] = loadA(IC<i + QD>{});
    };
    static_for<NI>([&](auto I) {
      constexpr int i = I;
      if constexpr ((i & 1) == 0) { BARRIER_NOWAIT(); STAMP(2 + (G0 + (i >> 1))); }
      rd(I);
      if constexpr (i > 0) mm(IC<i - 1>{});
      SCHED_FENCE();
    });
    mm(IC<NI - 1>{});
  };
  auto exchange = [&](f32x16 (&acc)[2], int xpos) {
    char* xb = lds + xpos * 1024;
    char* mine = xb + (wm * 2 + (kp ^ 1)) * 4096 + lane * 16;
    const char* theirs = xb + (wm * 2 + kp) * 4096 + lane * 16;
#pragma unroll
    for (int i = 0; i < 4; ++i) *(f32x4*)(mine + i * 1024) = f32x4{acc[1][4 * i], acc[1][4 * i + 1], acc[1][4 * i + 2], acc[1][4 * i + 3]};
    RAW_BARRIER();
#pragma unroll
    for (int i = 0; i < 4; ++i) {
      f32x4 t = *(const f32x4*)(theirs + i * 1024);
#pragma unroll
      for (int c = 0; c < 4; ++c) acc[0][4 * i + c] += t[c];
    }
  };

  {
    f32x16 acc[2] = {};
    const unsigned va = (h * HB_ROWS + row) * 16 + kp * (2 * HB_ROWS * 16);
    auto loadA = [&](auto I) {
      constexpr int i = I;
      constexpr int kk0 = 4 * (i >> 1) + 2 * (i & 1), g = i >> 2;
      constexpr int d = g == 0 ? -1 : g == 1 ? 1 : g == 2 ? -2 : 2;
      return __builtin_amdgcn_raw_buffer_load_b128(hrs, va, kk0 * (2 * HB_ROWS * 16) + (2 + d) * 16, 0);
    };
    BPRIO(K2_PM1);
    phase13(IC<0>{}, IC<16>{}, IC<10>{}, loadA, acc, [] {});
    BPRIO(K2_PE1);
    exchange(acc, 64);
    STAMP(50);
    float v[16], a1 = 0.f, a2 = 0.f;
#pragma unroll
    for (int i = 0; i < 4; ++i) {
      v[4 * i + 0] = (bf_lo(xv[i][0]) - st0[0]) * st0[1] + acc[0][4 * i + 0];
      v[4 * i + 1] = (bf_hi(xv[i][0]) - st0[0]) * st0[1] + acc[0][4 * i + 1];
      v[4 * i + 2] = (bf_lo(xv[i][1]) - st0[0]) * st0[1] + acc[0][4 * i + 2];
      v[4 * i + 3] = (bf_hi(xv[i][1]) - st0[0]) * st0[1] + acc[0][4 * i + 3];
    }
#pragma unroll
    for (int i = 0; i < 16; ++i) { a1 += v[i]; a2 += v[i] * v[i]; }
    a1 = xhalf_sum(a1); a2 = xhalf_sum(a2);
#pragma unroll
    for (int g = 0; g < 2; ++g) st_sc1(x1rs, pack8(v + 8 * g), ((col0 / 8 + 2 * g + h) * S_ + row) * 16, 0);
    if (lane < 32) {
      auto srs = mk_rsrc(ws + OFF_ST1, 16 * S_ * 8);
      u32x2 sv = {__float_as_uint(a1), __float_as_uint(a2)};
      __builtin_amdgcn_raw_buffer_store_b64(sv, srs, ((col0 >> 5) * S_ + row) * 8, 0, 16);
    }
    WAIT_V(0);
    BARRIER_NOWAIT();
    STAMP(51);
  }
  {
    const int wn = kp;
    lane_init();
    float cs[3];
    {
      const float* cp = (const float*)(ws + OFF_CS3) + h * 4 * CS3_LD + c0 + wn * 96 + l31;
#pragma unroll
      for (int a = 0; a < 3; ++a) {
        float t = 0.f;
#pragma unroll
        for (int i = 0; i < 4; ++i) t += cp[i * CS3_LD + a * 32];
        cs[a] = xhalf_sum(t);
      }
    }
    unsigned wo2[3];
#pragma unroll
    for (int a = 0; a < 3; ++a) wo2[a] = (8 * h + q) * 384 + (((4 * (3 * wn + a) + 2 * blk + (p >> 1)) ^ (sw << 2)) << 4) + cpart;
    f32x16 acc[3] = {};
    constexpr int QD = 8;
    u32x4 aq[QD];
    const unsigned va = (h * S_ + row) * 16;
    BARRIER_NOWAIT();
    STAMP(52);
    u32x2 sp[8];
    {
      auto srs = mk_rsrc(ws + OFF_ST1, 16 * S_ * 8);
#pragma unroll
      for (int i = 0; i < 8; ++i) sp[i] = __builtin_amdgcn_raw_buffer_load_b64(srs, ((h * 8 + i) * S_ + row) * 8, 0, 16);
    }
    static_for<QD>([&](auto I) { aq[I] = ld_sc1(x1rs, va, I * 65536); });
    bf16x8 wf[2][3];
    auto rd = [&](auto I) {
      constexpr int i = I;
      const char* sb = lds + pos(8 + (i >> 1)) * 1024 + (i & 1) * (16 * 384);
#pragma unroll
      for (int a = 0; a < 3; ++a) wf[i & 1][a] = tr_frag(sb + wo2[a], sb + wo2[a] + 4 * 384);
    };
    auto mm = [&](auto I) {
      constexpr int i = I;
      bf16x8 a = __builtin_bit_cast(bf16x8, aq[i % QD]);
#pragma unroll
      for (int t = 0; t < 3; ++t) acc[t] = mfma32(wf[i & 1][t], a, acc[t]);
      if constexpr (i + QD < 32) aq[i % QD] = ld_sc1(x1rs, va, (i + QD) * 65536);
    };
    float mu, rs;
    {
      float a1 = 0.f, a2 = 0.f;
#pragma unroll
      for (int t = 0; t < 8; ++t) { a1 += __uint_as_float(sp[t][0]); a2 += __uint_as_float(sp[t][1]); }
      a1 = xhalf_sum(a1); a2 = xhalf_sum(a2);
      mu = a1 * (1.0f / D_);
      rs = __builtin_amdgcn_rsqf(fmaxf(a2 * (1.0f / D_) - mu * mu, 0.f) + LN_EPS);
      SCHED_FENCE();
    }
    BPRIO(K2_PM2);
    static_for<32>([&](auto I) {
      constexpr int i = I;
      if constexpr ((i & 1) == 0 && i > 0) { BARRIER_NOWAIT(); STAMP(2 + 8 + (i >> 1)); }
      rd(I);
      if constexpr (i > 0) mm(IC<i - 1>{});
      SCHED_FENCE();
    });
    mm(IC<31>{});
    BPRIO(K2_PE2);
    {
      unsigned mh = pk_bf16(-mu, -mu);
      float mlo = -mu - bf_lo(mh);
      u32x4 xe = {h ? 0u : mh, h ? 0u : (pk_bf16(mlo, 0.f)), 0u, 0u};
#pragma unroll
      for (int t = 0; t < 3; ++t) {
        unsigned ch = pk_bf16(cs[t], cs[t]);
        float clo = cs[t] - bf_lo(ch);
        u32x4 we = {h ? 0u : ((ch & 0xffffu) | (pk_bf16(clo, clo) << 16)), h ? 0u : (ch & 0xffffu), 0u, 0u};
        acc[t] = mfma32(__builtin_bit_cast(bf16x8, we), __builtin_bit_cast(bf16x8, xe), acc[t]);
      }
    }
    STAMP(53);
#pragma unroll
    for (int t = 0; t < 3; ++t) {
      if (3 * wn + t < ntile) {
        float v[16];
#pragma unroll
        for (int i = 0; i < 16; ++i) v[i] = gelu_tanh(acc[t][i] * rs);
        const unsigned kt8 = (c0 + (3 * wn + t) * 32) >> 3;
#pragma unroll
        for (int g = 0; g < 2; ++g) st_sc1(h2rs, pack8(v + 8 * g), ((kt8 + 2 * g + h) * S_ + row) * 16, 0);
      }
    }
    WAIT_V(0);
    BARRIER_NOWAIT();
    STAMP(54);
  }
  {
    lane_init();
    f32x16 acc[2] = {};
    const unsigned va = (h * S_ + row) * 16 + kp * 65536;
    auto loadA = [&](auto I) {
      constexpr int i = I;
      constexpr int kk0 = 4 * (i >> 1) + 2 * (i & 1);
      return ld_sc1(h2rs, va, kk0 * 65536);
    };
    BARRIER_NOWAIT();
    STAMP(55);
    constexpr int NI = 43, QD = 10;
    u32x4 aq[QD];
    static_for<QD>([&](auto I) { aq[I] = loadA(I); });
    BPRIO(K2_PM3);
    bf16x8 wf[2][2];
    u32x2 xr[4], x1r[4];
    auto rd = [&](auto I) {
      constexpr int i = I;
      const char* sb = lds + pos(24 + (i >> 1)) * 1024 + (i & 1) * 4096;
      wf[i & 1][0] = tr_frag(sb + wo13[0], sb + wo13[0] + 512);
      wf[i & 1][1] = tr_frag(sb + wo13[1], sb + wo13[1] + 512);
    };
    auto mm = [&](auto I) {
      constexpr int i = I;
      bf16x8 a = __builtin_bit_cast(bf16x8, aq[i % QD]);
      acc[0] = mfma32(wf[i & 1][0], a, acc[0]);
      acc[1] = mfma32(wf[i & 1][1], a, acc[1]);
      if constexpr (i + QD < NI) aq[i % QD] = loadA(IC<i + QD>{});
      if constexpr (i + QD == NI) {
#pragma unroll
        for (int t = 0; t < 4; ++t) {
          xr[t] = __builtin_amdgcn_raw_buffer_load_b64(xrs, (row * D_ + col0 + 4 * h) * 2, t * 16, 0);
          x1r[t] = __builtin_amdgcn_raw_buffer_load_b64(x1rs, ((col0 / 8 + t) * S_ + row) * 16 + 8 * h, 0, 16);
        }
      }
    };
    static_for<NI>([&](auto I) {
      constexpr int i = I;
      if constexpr ((i & 1) == 0 && i > 0) { BARRIER_NOWAIT(); STAMP(2 + 24 + (i >> 1)); }
      rd(I);
      if constexpr (i > 0) mm(IC<i - 1>{});
      SCHED_FENCE();
    });
    mm(IC<NI - 1>{});
    BPRIO(K2_PE3);
    exchange(acc, 32);
    STAMP(56);
    char* rb = ws + OFF_R + ((size_t)((m0 >> 5) + wm) * 16 + (col0 >> 5)) * 4096 + fresh_lane() * 16;
#pragma unroll
    for (int i = 0; i < 4; ++i) {
      f32x4 o;
      o[0] = acc[0][4 * i + 0] + bf_lo(xr[i][0]) + bf_lo(x1r[i][0]);
      o[1] = acc[0][4 * i + 1] + bf_hi(xr[i][0]) + bf_hi(x1r[i][0]);
      o[2] = acc[0][4 * i + 2] + bf_lo(xr[i][1]) + bf_lo(x1r[i][1]);
      o[3] = acc[0][4 * i + 3] + bf_hi(xr[i][1]) + bf_hi(x1r[i][1]);
      *(f32x4*)(rb + i * 1024) = o;
    }
  }
  STAMP(60); RSTAMP(61); STAMP_FLUSH(0);
#endif
}

constexpr int K2_LDS = 81920;
__global__ __launch_bounds__(512, 4) void k2(const u16* __restrict__ x, const u16* __restrict__ w2, const u16* __restrict__ w3,
                                             const u16* __restrict__ w4, char* __restrict__ ws) {
#if defined(__HIP_DEVICE_COMPILE__)
  extern __shared__ __attribute__((aligned(16))) char lds[];
#define K2_SWAP 1
#define K2_BPRIO 2
#define K2_APRIO 1
  const int b = blockIdx.x ^ (K2_SWAP * 256);
  if (b >= 256) { __builtin_amdgcn_s_setprio(K2_APRIO); attn_role(lds, b - 256, ws); return; }
  const int lane = threadIdx.x & 63;
  const int wid = __builtin_amdgcn_readfirstlane(threadIdx.x >> 6);
  const int grp = (b & 7) * 4 + (b >> 6), j = (b >> 3) & 7;
  const int c0 = j < 3 ? j * 192 : 576 + (j - 3) * 160;
  const int ntile = j < 3 ? 6 : 5;
#define K2_BDELAY 24
  __builtin_amdgcn_s_sleep(K2_BDELAY);
  __builtin_amdgcn_s_setprio(K2_BPRIO);
  if (wid >= 4) b_loader(lds, wid - 4, lane, grp, j, c0, w2, w3, w4, ws);
  else b_compute(lds, wid, lane, grp, j, c0, ntile, x, ws);
#endif
}

constexpr int K3_LDS = 131072;
__global__ __launch_bounds__(512, 2) void k3(const u16* __restrict__ wo, const char* __restrict__ ws, u16* __restrict__ out) {
#if defined(__HIP_DEVICE_COMPILE__)
  extern __shared__ __attribute__((aligned(16))) char lds[];
  STAMP_DECL
  RSTAMP(0); STAMP(1);
  const int b = blockIdx.x, xcd = b & 7, bi = b >> 3;
  const int mt = xcd * 4 + (bi >> 3), nt = bi & 7;
  const int m0 = mt * 64;
  const int tid = threadIdx.x, lane = tid & 63;
  const int wid = __builtin_amdgcn_readfirstlane(tid >> 6);
  auto ors = mk_rsrc(ws + OFF_O, 64 * S_ * 16);
  auto wrs = mk_rsrc(wo, D_ * D_ * 2);
  const unsigned va = (m0 + lane) * 16;
  unsigned vw;
  { unsigned row = lane >> 3, cp = lane & 7; vw = row * 1024 + nt * 128 + ((cp ^ (((row >> 1) & 1) << 2)) << 4); }
  const int rot = (nt & 3);
#pragma unroll
  for (int c = 0; c < 4; ++c) {
    const int cc = (c + rot) & 3;
#pragma unroll
    for (int i = 0; i < 2; ++i) {
      const int pc = 2 * wid + i;
      dma16(ors, lds + (c * 16 + pc) * 1024, va, (cc * 16 + pc) * (S_ * 16));
      dma16(wrs, lds + 65536 + (c * 16 + pc) * 1024, vw, (cc * 16 + pc) * 8192);
    }
  }
  const unsigned l31 = lane & 31, h = lane >> 5, blk = (lane >> 4) & 1, q = (lane & 15) >> 2, p = lane & 3;
  const int wm = wid & 1, wn = (wid >> 1) & 1;
  f32x4 r[4];
  if (wid < 4) {
    const char* rb = ws + OFF_R + ((size_t)((m0 >> 5) + wm) * 16 + nt * 2 + wn) * 4096 + lane * 16;
#pragma unroll
    for (int i = 0; i < 4; ++i) r[i] = *(const f32x4*)(rb + i * 1024);
  }
  const unsigned ao = h * 1024 + (wm * 32 + l31) * 16;
  const unsigned wo_ = 65536 + (8 * h + q) * 128 + (((4 * wn + 2 * blk + (p >> 1)) ^ (((q >> 1) & 1) << 2)) << 4) + 8 * (p & 1);
  f32x16 acc = {};
  static_for<4>([&](auto C) {
    constexpr int c = C;
    if (wid < 4) WAIT_V((3 - c) * 4 + 4); else WAIT_V((3 - c) * 4);
    BARRIER_NOWAIT();
    STAMP(4 + c);
    if (wid < 4) {
      bf16x8 xf[8], wf[8];
#pragma unroll
      for (int s = 0; s < 8; ++s) {
        xf[s] = *(const bf16x8*)(lds + c * 16384 + s * 2048 + ao);
        wf[s] = tr_frag(lds + c * 16384 + s * 2048 + wo_, lds + c * 16384 + s * 2048 + wo_ + 512);
      }
#pragma unroll
      for (int s = 0; s < 8; ++s) acc = mfma32(wf[s], xf[s], acc);
    }
  });
  STAMP(40);
  if (wid < 4) {
    float v[16];
#pragma unroll
    for (int i = 0; i < 4; ++i)
#pragma unroll
      for (int c = 0; c < 4; ++c) v[4 * i + c] = acc[4 * i + c] + r[i][c];
    u16* dst = out + (size_t)(m0 + wm * 32 + l31) * D_ + nt * 64 + wn * 32 + h * 8;
#pragma unroll
    for (int g = 0; g < 2; ++g) *(u32x4*)(dst + g * 16) = pack8(v + 8 * g);
  }
  STAMP(41); RSTAMP(42); STAMP_FLUSH(0);
#endif
}

#define SEL(i) true
extern "C" void kernel_launch(void* const* d_in, const int* in_sizes, int n_in, void* d_out, int out_size,
                              void* d_ws, size_t ws_size, hipStream_t stream) {
  const u16* x = (const u16*)d_in[0];
  const u16* wqkv = (const u16*)d_in[1];
  const u16* wo = (const u16*)d_in[2];
  const u16* w1 = (const u16*)d_in[4];
  const u16* w2 = (const u16*)d_in[5];
  const u16* w3 = (const u16*)d_in[6];
  const u16* w4 = (const u16*)d_in[7];
  char* ws = (char*)d_ws;
  (void)hipFuncSetAttribute((const void*)k1, hipFuncAttributeMaxDynamicSharedMemorySize, K1_LDS);
  (void)hipFuncSetAttribute((const void*)k2, hipFuncAttributeMaxDynamicSharedMemorySize, K2_LDS);
  (void)hipFuncSetAttribute((const void*)k3, hipFuncAttributeMaxDynamicSharedMemorySize, K3_LDS);
  if (SEL(0)) k1<<<256, 512, K1_LDS, stream>>>(x, wqkv, w1, w3, w2, w4, wo, ws);
  if (SEL(1)) k2<<<512, 512, K2_LDS, stream>>>(x, w2, w3, w4, ws);
  if (SEL(2)) k3<<<256, 512, K3_LDS, stream>>>(wo, ws, (u16*)d_out);
}
```

```cpp
#include <hip/hip_runtime.h>
#include <stdint.h>

typedef __attribute__((ext_vector_type(8))) __bf16 bf16x8;
typedef __attribute__((ext_vector_type(2))) __bf16 bf16x2;
typedef __attribute__((ext_vector_type(4))) short s16x4;
typedef __attribute__((ext_vector_type(16))) float f32x16;
typedef __attribute__((ext_vector_type(4))) float f32x4;
typedef __attribute__((ext_vector_type(2))) float f32x2;
typedef __attribute__((ext_vector_type(4))) unsigned u32x4;
typedef __attribute__((ext_vector_type(2))) unsigned u32x2;
typedef __attribute__((address_space(3))) void* ldsptr_t;
typedef unsigned short u16;

#define DEV __device__ __forceinline__

constexpr int S_ = 2048, D_ = 512, H_ = 32, F_ = 1376;
constexpr float LN_EPS = 1e-5f;
constexpr float QSCALE = 0.25f * 1.4426950408889634f;

constexpr size_t MB_ = 1 << 20, KB_ = 1 << 10;
constexpr size_t OFF_Q = 0, OFF_K = 2 * MB_, OFF_V = 4 * MB_, OFF_O = 6 * MB_, OFF_X1 = 8 * MB_, OFF_HB = 10 * MB_;
constexpr size_t OFF_ST0 = 12 * MB_ + 64 * KB_, OFF_CNT = 12 * MB_ + 96 * KB_, OFF_CS3 = 12 * MB_ + 128 * KB_;
constexpr size_t OFF_ST1 = 12 * MB_ + 256 * KB_, OFF_H2 = 13 * MB_, OFF_R = 19 * MB_;
constexpr int HB_ROWS = S_ + 4;
constexpr int CS3_P = 8, CS3_LD = 1408;

template <int N> struct IC { static constexpr int value = N; constexpr operator int() const { return N; } };
template <int I, int N, class F> DEV void static_for_impl(F&& f) {
  if constexpr (I < N) { f(IC<I>{}); static_for_impl<I + 1, N>(f); }
}
template <int N, class F> DEV void static_for(F&& f) { static_for_impl<0, N>(f); }

DEV unsigned pk_bf16(float lo, float hi) {
  f32x2 v = {lo, hi};
  return __builtin_bit_cast(unsigned, __builtin_convertvector(v, bf16x2));
}
DEV float bf_lo(unsigned u) { return __uint_as_float(u << 16); }
DEV float bf_hi(unsigned u) { return __uint_as_float(u & 0xffff0000u); }
DEV float gelu_tanh(float x) {
  const float c1 = -2.0f * 0.7978845608028654f * 1.4426950408889634f;
  const float c2 = c1 * 0.044715f;
  float t = x * (c1 + c2 * x * x);
  float e = __builtin_amdgcn_exp2f(t);
  return x * __builtin_amdgcn_rcpf(1.0f + e);
}
DEV float dot2(unsigned a, unsigned b, float c) {
  return __builtin_amdgcn_fdot2_f32_bf16(__builtin_bit_cast(bf16x2, a), __builtin_bit_cast(bf16x2, b), c, false);
}
DEV float xhalf_sum(float v) {
  auto r = __builtin_amdgcn_permlane32_swap(__float_as_uint(v), __float_as_uint(v), false, false);
  return __uint_as_float(r[0]) + __uint_as_float(r[1]);
}
#define STAMP(i)
#define RSTAMP(i)
#define STAMP_DECL
#define STAMP_ARG
#define STAMP_PASS
#define STAMP_FLUSH(w)
#define WAIT_V(n) asm volatile("s_waitcnt vmcnt(%0)" ::"n"(n) : "memory")
#define WAIT_L(n) asm volatile("s_waitcnt lgkmcnt(%0)" ::"n"(n) : "memory")
#define SCHED_FENCE() __builtin_amdgcn_sched_barrier(0)
#define RAW_BARRIER() do { WAIT_L(0); __builtin_amdgcn_s_barrier(); } while (0)
#define BARRIER_NOWAIT() do { SCHED_FENCE(); asm volatile("s_barrier" ::: "memory"); SCHED_FENCE(); } while (0)

DEV unsigned fresh_lane() {
  unsigned l;
  asm volatile("v_mbcnt_lo_u32_b32 %0, -1, 0\n\tv_mbcnt_hi_u32_b32 %0, -1, %0" : "=v"(l));
  return l;
}
DEV __amdgpu_buffer_rsrc_t mk_rsrc(const void* p, unsigned bytes) {
  return __builtin_amdgcn_make_buffer_rsrc((void*)p, 0, (int)bytes, 0x00020000);
}
DEV void dma16(__amdgpu_buffer_rsrc_t r, char* lds_dst, unsigned voff, unsigned soff) {
  __builtin_amdgcn_raw_ptr_buffer_load_lds(r, (ldsptr_t)lds_dst, 16, voff, soff, 0, 0);
}
DEV bf16x8 tr_frag(const char* p0, const char* p1) {
  s16x4 a = __builtin_amdgcn_ds_read_tr16_b64_v4i16((__attribute__((address_space(3))) s16x4*)p0);
  s16x4 b = __builtin_amdgcn_ds_read_tr16_b64_v4i16((__attribute__((address_space(3))) s16x4*)p1);
  typedef __attribute__((ext_vector_type(8))) short s16x8;
  s16x8 r = __builtin_shufflevector(a, b, 0, 1, 2, 3, 4, 5, 6, 7);
  return __builtin_bit_cast(bf16x8, r);
}
DEV f32x16 mfma32(bf16x8 a, bf16x8 b, f32x16 c) { return __builtin_amdgcn_mfma_f32_32x32x16_bf16(a, b, c, 0, 0, 0); }
DEV u32x2 pk4(float a, float b, float c, float d) { u32x2 r; r[0] = pk_bf16(a, b); r[1] = pk_bf16(c, d); return r; }
DEV u32x4 widen(u32x2 X, u32x2 Y) {
  u32x4 o;
  auto a = __builtin_amdgcn_permlane32_swap(X[0], Y[0], false, false);
  auto b = __builtin_amdgcn_permlane32_swap(X[1], Y[1], false, false);
  o[0] = a[0]; o[1] = b[0]; o[2] = a[1]; o[3] = b[1];
  return o;
}
DEV u32x4 pack8(const float* v) { return widen(pk4(v[0], v[1], v[2], v[3]), pk4(v[4], v[5], v[6], v[7])); }

constexpr int K1_A = 16384, K1_ST = 32768;
constexpr int K1_X = 4 * K1_ST;
constexpr int K1_P3 = K1_X + 2048 + 1024 + 1536;
constexpr int K1_LDS = K1_P3 + 2048 + 16;

#define K1_STAUX 0
template <int KIND>
DEV void k1_epi_s(const f32x16& a, const float* fin, int sub, int J, int m0, int rl0, int lane, char* ws) {
  const int l31 = lane & 31, h = lane >> 5;
  const int rl = rl0 + l31, s = m0 + rl;
  float mu = fin[rl], rs = fin[128 + rl];
  if (KIND == 0) rs *= QSCALE;
  if (KIND == 2) {
    float r2 = rs * rs;
    rs *= __builtin_amdgcn_rsqf((1.0f - LN_EPS * r2) + LN_EPS);
  }
  float v[16];
#pragma unroll
  for (int j = 0; j < 4; ++j) {
    f32x4 c = *(const f32x4*)(fin + 256 + sub * 32 + 8 * j + 4 * h);
#pragma unroll
    for (int i = 0; i < 4; ++i) v[4 * j + i] = (a[4 * j + i] - mu * c[i]) * rs;
  }
  if (KIND == 2) {
#pragma unroll
    for (int i = 0; i < 16; ++i) v[i] = gelu_tanh(v[i]);
  }
#pragma unroll
  for (int g = 0; g < 2; ++g) {
    u32x4 o = pack8(v + 8 * g);
    unsigned dst;
    if (KIND == 0) dst = OFF_Q + (unsigned)(2 * J + g) * (S_ * 32) + (unsigned)(s * 32 + h * 16);
    else if (KIND == 1) dst = OFF_K + (unsigned)(2 * J + g) * (S_ * 32) + (unsigned)((s >> 5) * 1024 + h * 512 + (s & 31) * 16);
    else dst = OFF_HB + (unsigned)(((4 * J + 2 * g + h) * HB_ROWS + s + 2) * 16);
    __builtin_amdgcn_raw_buffer_store_b128(o, mk_rsrc(ws, 16 * MB_), dst, 0, K1_STAUX);
  }
}
DEV void k1_epi_v(const f32x16& a, const float* fin, int sub, int J, int m0, int rl0, int lane, char* ws) {
  const int l31 = lane & 31, h = lane >> 5;
  const float cs = fin[256 + sub * 32 + l31];
  float v[16];
#pragma unroll
  for (int j = 0; j < 4; ++j) {
    f32x4 mu = *(const f32x4*)(fin + rl0 + 8 * j + 4 * h), rs = *(const f32x4*)(fin + 128 + rl0 + 8 * j + 4 * h);
#pragma unroll
    for (int i = 0; i < 4; ++i) v[4 * j + i] = (a[4 * j + i] - mu[i] * cs) * rs[i];
  }
  const int n = J * 32 + l31, head = n >> 4, d = n & 15, tile = (m0 + rl0) >> 5;
  const unsigned base = OFF_V + (unsigned)head * (S_ * 32) + tile * 1024;
#pragma unroll
  for (int s = 0; s < 2; ++s) {
    u32x4 o;
#pragma unroll
    for (int j = 0; j < 4; ++j) o[j] = pk_bf16(v[8 * s + 2 * j], v[8 * s + 2 * j + 1]);
    __builtin_amdgcn_raw_buffer_store_b128(o, mk_rsrc(ws, 16 * MB_), base + (s * 2 + h) * 256 + d * 16, 0, K1_STAUX);
  }
}

template <int TYPE>
DEV void k1_compute(char* lds, int wm, int wn, int nt, int m0, char* ws, const char* pfp, unsigned pfs STAMP_ARG) {
  const unsigned lane = fresh_lane();
  const unsigned l31 = lane & 31, h = lane >> 5, blk = (lane >> 4) & 1, q = (lane & 15) >> 2, p = lane & 3;
  const unsigned mask = wm ^ wn;
  const unsigned row = wm * 64 + l31;
  unsigned ao[4], wb[2][2];
#pragma unroll
  for (int j = 0; j < 4; ++j) ao[j] = row * 128 + (((2 * (j ^ mask) + h) ^ ((row >> 1) & 7)) << 4);
  const unsigned cpart = (2 * blk + (p >> 1)) * 16 + 8 * (p & 1);
#pragma unroll
  for (int a = 0; a < 2; ++a) {
    const unsigned w = K1_A + (8 * h + q) * 256 + (((wn + 2 * a) ^ q) << 6) + cpart;
    wb[0][a] = w + mask * 4096; wb[1][a] = w - mask * 4096;
  }
  f32x16 acc[2][2] = {};
  float s1[2][2] = {}, s2[2][2] = {}, cs[2][2] = {};
  bf16x8 xf[4][2], wf[4][2];
  auto rd = [&](int t, int j) {
    const char* sb = lds + (t & 3) * K1_ST;
    xf[j][0] = *(const bf16x8*)(sb + ao[j]);
    wf[j][0] = tr_frag(sb + wb[j & 1][0] + j * 4096, sb + wb[j & 1][0] + j * 4096 + 1024);
    wf[j][1] = tr_frag(sb + wb[j & 1][1] + j * 4096, sb + wb[j & 1][1] + j * 4096 + 1024);
    xf[j][1] = *(const bf16x8*)(sb + ao[j] + 4096);
  };
  auto mm = [&](int j) {
#pragma unroll
    for (int r = 0; r < 2; ++r)
#pragma unroll
      for (int a = 0; a < 2; ++a)
        acc[r][a] = TYPE == 2 ? mfma32(xf[j][r], wf[j][a], acc[r][a]) : mfma32(wf[j][a], xf[j][r], acc[r][a]);
    __builtin_amdgcn_sched_group_barrier(0x008, 1, 0);
    __builtin_amdgcn_sched_group_barrier(0x100, 3, 0);
    __builtin_amdgcn_sched_group_barrier(0x008, 1, 0);
    __builtin_amdgcn_sched_group_barrier(0x100, 3, 0);
    __builtin_amdgcn_sched_group_barrier(0x008, 2, 0);
  };
  auto dots = [&](int j) {
#pragma unroll
    for (int i = 0; i < 4; ++i) {
#pragma unroll
      for (int r = 0; r < 2; ++r) {
        unsigned u = __builtin_bit_cast(u32x4, xf[j][r])[i];
        s1[r][i & 1] = dot2(u, 0x3f803f80u, s1[r][i & 1]); s2[r][i & 1] = dot2(u, u, s2[r][i & 1]);
      }
#pragma unroll
      for (int a = 0; a < 2; ++a) cs[a][i & 1] = dot2(__builtin_bit_cast(u32x4, wf[j + 1][a])[i], 0x3f803f80u, cs[a][i & 1]);
    }
    asm volatile("" : "+v"(s1[0][0]), "+v"(s1[0][1]), "+v"(s1[1][0]), "+v"(s1[1][1]), "+v"(s2[0][0]), "+v"(s2[0][1]), "+v"(s2[1][0]), "+v"(s2[1][1]),
                      "+v"(cs[0][0]), "+v"(cs[0][1]), "+v"(cs[1][0]), "+v"(cs[1][1]));
  };
#pragma unroll
  for (int t = 0; t < 8; ++t) {
    WAIT_L(0);
    BARRIER_NOWAIT();
    STAMP(4 + t);
    rd(t, 0);
    if (t > 0) { mm(3); SCHED_FENCE(); dots(2); }
    SCHED_FENCE();
    rd(t, 1);
    mm(0);
    SCHED_FENCE();
    rd(t, 2);
    mm(1);
    SCHED_FENCE();
    dots(0);
    SCHED_FENCE();
    rd(t, 3);
    mm(2);
    SCHED_FENCE();
  }
  mm(3);
  SCHED_FENCE();
  dots(2);
  STAMP(40);
  float* rsb = (float*)(lds + K1_X);
  float* csb = rsb + 512;
  float* fin = csb + 256;
#pragma unroll
  for (int r = 0; r < 2; ++r) {
    float a = xhalf_sum(s1[r][0] + s1[r][1]), b = xhalf_sum(s2[r][0] + s2[r][1]), c = xhalf_sum(cs[r][0] + cs[r][1]);
    if (h == 0) *(f32x2*)(rsb + (wn * 128 + wm * 64 + r * 32 + l31) * 2) = f32x2{a, b};
    else csb[wm * 128 + (wn + 2 * r) * 32 + l31] = c;
  }
  STAMP(43);
  RAW_BARRIER();
  STAMP(44);
  {
    const int i = wm * 64 + lane;
    f32x2 a = *(const f32x2*)(rsb + i * 2), c = *(const f32x2*)(rsb + (128 + i) * 2);
    float mu = (a[0] + c[0]) * (1.0f / D_);
    float var = fmaxf((a[1] + c[1]) * (1.0f / D_) - mu * mu, 0.f);
    float rs = __builtin_amdgcn_rsqf(var + LN_EPS);
    float cc = csb[i] + csb[128 + i];
    fin[wn * 256 + i] = wn ? cc : mu;
    if (wn == 0) {
      fin[128 + i] = rs;
      if (nt == 0) *(f32x2*)(ws + OFF_ST0 + (size_t)(m0 + i) * 8) = f32x2{mu, rs};
    }
  }
  STAMP(45);
  RAW_BARRIER();
  STAMP(46);
  const int c = nt >> 1;
#pragma unroll
  for (int r = 0; r < 2; ++r) {
    const int rl0 = wm * 64 + r * 32;
    if (TYPE == 0) {
      k1_epi_s<0>(acc[r][0], fin, wn, 2 * c + wn, m0, rl0, lane, ws);
      STAMP(47 + 2 * r);
      k1_epi_s<2>(acc[r][1], fin, wn + 2, 2 * c + wn, m0, rl0, lane, ws);
      STAMP(48 + 2 * r);
    } else if (TYPE == 1) {
      k1_epi_s<1>(acc[r][0], fin, wn, 4 * c + wn, m0, rl0, lane, ws);
      k1_epi_s<1>(acc[r][1], fin, wn + 2, 4 * c + wn + 2, m0, rl0, lane, ws);
    } else {
      k1_epi_v(acc[r][0], fin, wn, 4 * (c - 4) + wn, m0, rl0, lane, ws);
      k1_epi_v(acc[r][1], fin, wn + 2, 4 * (c - 4) + wn + 2, m0, rl0, lane, ws);
    }
  }
  STAMP(41); RSTAMP(42); STAMP_FLUSH(4);
}

__global__ __launch_bounds__(512, 2) void k1(const u16* __restrict__ x, const u16* __restrict__ wqkv, const u16* __restrict__ w1,
                                             const u16* __restrict__ w3, const u16* __restrict__ w2, const u16* __restrict__ w4,
                                             const u16* __restrict__ wo, char* __restrict__ ws) {
#if defined(__HIP_DEVICE_COMPILE__)
  extern __shared__ __attribute__((aligned(16))) char lds[];
  STAMP_DECL
  RSTAMP(0); STAMP(1);
  const int b = blockIdx.x, xcd = b & 7, bi = b >> 3;
  const int mt = (xcd & 3) * 4 + (bi & 3), nt = (xcd >> 2) * 8 + (bi >> 2);
  const int m0 = mt * 128;
  const int tid = threadIdx.x, lane = tid & 63;
  const int wid = __builtin_amdgcn_readfirstlane(tid >> 6);
  if (__builtin_expect(wid < 4, 1)) {
    const int lw = wid;
    auto xrs = mk_rsrc(x, S_ * D_ * 2);
    unsigned va[4];
#pragma unroll
    for (int i = 0; i < 4; ++i) {
      unsigned bb = (lw + 4 * i) * 1024 + lane * 16, row = bb >> 7, cp = (bb >> 4) & 7;
      va[i] = (m0 + row) * 1024 + ((cp ^ ((row >> 1) & 7)) << 4);
    }
    const unsigned kl = lane >> 4, seg = ((lane >> 2) & 3) ^ kl;
    const int c = nt >> 1;
    const bool fromw1 = !(nt & 1) && seg >= 2;
    const unsigned wstride = fromw1 ? 1024u : 3072u;
    const char* wbase = fromw1 ? (const char*)w1 + c * 128 + (seg - 2) * 64
                               : (const char*)wqkv + ((nt & 1) ? 1024 + c * 256 : c * 128) + seg * 64;
    wbase += (lane & 3) * 16 + (lw * 4 + kl) * wstride;
#define K1_ROT (nt + 2 * mt)
    const int rot = K1_ROT;
    auto issue = [&](int t) {
      char* sb = lds + (t & 3) * K1_ST;
      const unsigned kt = (t + rot) & 7;
#pragma unroll
      for (int i = 0; i < 4; ++i) {
        dma16(xrs, sb + (lw + 4 * i) * 1024, va[i], kt * 128);
        __builtin_amdgcn_global_load_lds((const unsigned*)(wbase + (size_t)((kt * 64 + i * 16) * wstride)),
                                         (__attribute__((address_space(3))) unsigned*)(sb + K1_A + (lw + 4 * i) * 1024), 16, 0, 0);
      }
    };
    STAMP(2);
    issue(0); STAMP(3); issue(1);
    if (tid == 0) *(unsigned*)(lds + K1_P3 + 2048) = 0;
    STAMP(4);
    WAIT_V(8);  STAMP(6); BARRIER_NOWAIT(); issue(2); STAMP(20); issue(3); STAMP(21);
    WAIT_V(16); STAMP(7); BARRIER_NOWAIT(); issue(4); STAMP(22);
    WAIT_V(16); STAMP(8); BARRIER_NOWAIT(); issue(5);
    WAIT_V(16); STAMP(9); BARRIER_NOWAIT(); issue(6);
    WAIT_V(16); STAMP(10); BARRIER_NOWAIT(); issue(7);
    const void* mp; unsigned mbytes, mv, mstride;
    if (b < 88) {
      int cg = b % 11, rg = b / 11;
      mp = w3; mbytes = D_ * F_ * 2; mstride = 4 * F_ * 2;
      mv = (rg * 64 + lw * 16 + (lane >> 4)) * (F_ * 2) + cg * 256 + (lane & 15) * 16;
    } else {
      const int it = (b - 88) * 4 + lw;
      mstride = 8192;
      if (it < 16) { mp = w2; mbytes = D_ * D_ * 2; mv = it * 32768 + lane * 128; }
      else if (it < 32) { mp = wo; mbytes = D_ * D_ * 2; mv = (it - 16) * 32768 + lane * 128; }
      else { mp = w4; mbytes = D_ * F_ * 2; mv = (it - 32) * 32768 + lane * 128; }
    }
    auto mrs = mk_rsrc(mp, mbytes);
    u32x4 md[4];
#pragma unroll
    for (int i = 0; i < 4; ++i) md[i] = __builtin_amdgcn_raw_buffer_load_b128(mrs, mv, i * mstride, 0);
    WAIT_V(20); STAMP(11); BARRIER_NOWAIT();
    WAIT_V(12); STAMP(12); BARRIER_NOWAIT();
    WAIT_V(4);  STAMP(13); BARRIER_NOWAIT();
    if (b < 88) {
      float cs[8];
#pragma unroll
      for (int j = 0; j < 8; ++j) cs[j] = 0.f;
#pragma unroll
      for (int i = 0; i < 4; ++i)
#pragma unroll
        for (int j = 0; j < 4; ++j) { cs[2 * j] += bf_lo(md[i][j]); cs[2 * j + 1] += bf_hi(md[i][j]); }
#pragma unroll
      for (int j = 0; j < 8; ++j) { cs[j] += __shfl_xor(cs[j], 16); cs[j] = xhalf_sum(cs[j]); }
      float* p3 = (float*)(lds + K1_P3);
      if (lane < 16) {
        *(f32x4*)(p3 + lw * 128 + lane * 8) = f32x4{cs[0], cs[1], cs[2], cs[3]};
        *(f32x4*)(p3 + lw * 128 + lane * 8 + 4) = f32x4{cs[4], cs[5], cs[6], cs[7]};
      }
      WAIT_L(0);
      unsigned old = 0;
      if (lane == 0) old = __hip_atomic_fetch_add((unsigned*)(lds + K1_P3 + 2048), 1u, __ATOMIC_RELAXED, __HIP_MEMORY_SCOPE_WORKGROUP);
      old = __builtin_amdgcn_readfirstlane(old);
      if (old == 3) {
        f32x2 t = {0.f, 0.f};
#pragma unroll
        for (int w = 0; w < 4; ++w) t += *(const f32x2*)(p3 + w * 128 + lane * 2);
        *(f32x2*)((float*)(ws + OFF_CS3) + (b / 11) * CS3_LD + (b % 11) * 128 + lane * 2) = t;
      }
    } else {
#pragma unroll
      for (int i = 0; i < 4; ++i) asm volatile("" ::"v"(md[i]));
      if (b == 200 && lw < 2) ((unsigned*)(ws + OFF_CNT))[(lw * 64 + lane) * 64] = 0;
      if (b == 201) {
        u32x4 z = {0, 0, 0, 0};
        *(u32x4*)(ws + OFF_HB + ((size_t)lane * HB_ROWS + (lw < 2 ? lw : S_ + lw)) * 16) = z;
      }
    }
    STAMP(41); RSTAMP(42); STAMP_FLUSH(0);
    return;
  }
  const int wm = wid & 1, wn = (wid >> 1) & 1;
  const char* pfp; unsigned pfs;
  {
    const int c = nt >> 1, i = lane - 16, kt = wid - 4;
    if (lane < 16) { pfp = (const char*)x + (size_t)(m0 + (bi >> 2) * 16 + lane) * 1024 + kt * 128; pfs = 128; }
    else {
      const unsigned k = kt * 64 + (bi & 3) * 16 + ((i >> 1) & 15), half = i & 1;
      const bool fw1 = !(nt & 1) && half;
      pfs = fw1 ? 65536u : 196608u;
      pfp = fw1 ? (const char*)w1 + k * 1024 + c * 128
                : (const char*)wqkv + k * 3072 + ((nt & 1) ? 1024 + c * 256 + half * 128 : c * 128);
    }
  }
  if (!(nt & 1)) k1_compute<0>(lds, wm, wn, nt, m0, ws, pfp, pfs STAMP_PASS);
  else if (nt < 8) k1_compute<1>(lds, wm, wn, nt, m0, ws, pfp, pfs STAMP_PASS);
  else k1_compute<2>(lds, wm, wn, nt, m0, ws, pfp, pfs STAMP_PASS);
#endif
}
#define ATTN_ASM \
  "v_mov_b32 v36, %[negm]\n\t" \
  "v_mov_b32 v37, %[negm]\n\t" \
  "v_mov_b32 v38, %[negm]\n\t" \
  "v_mov_b32 v39, %[negm]\n\t" \
  "v_mov_b32 v40, %[negm]\n\t" \
  "v_mov_b32 v41, %[negm]\n\t" \
  "v_mov_b32 v42, %[negm]\n\t" \
  "v_mov_b32 v43, %[negm]\n\t" \
  "v_mov_b32 v44, %[negm]\n\t" \
  "v_mov_b32 v45, %[negm]\n\t" \
  "v_mov_b32 v46, %[negm]\n\t" \
  "v_mov_b32 v47, %[negm]\n\t" \
  "v_mov_b32 v48, %[negm]\n\t" \
  "v_mov_b32 v49, %[negm]\n\t" \
  "v_mov_b32 v50, %[negm]\n\t" \
  "v_mov_b32 v51, %[negm]\n\t" \
  "v_mov_b32 v52, 0\n\t" \
  "v_mov_b32 v53, 0\n\t" \
  "v_mov_b32 v54, 0\n\t" \
  "v_mov_b32 v55, 0\n\t" \
  "v_mov_b32 v56, 0\n\t" \
  "v_mov_b32 v57, 0\n\t" \
  "v_mov_b32 v58, 0\n\t" \
  "v_mov_b32 v59, 0\n\t" \
  "v_mov_b32 v60, 0\n\t" \
  "v_mov_b32 v61, 0\n\t" \
  "v_mov_b32 v62, 0\n\t" \
  "v_mov_b32 v63, 0\n\t" \
  "v_mov_b32 v64, 0\n\t" \
  "v_mov_b32 v65, 0\n\t" \
  "v_mov_b32 v66, 0\n\t" \
  "v_mov_b32 v67, 0\n\t" \
  "v_mov_b32 v104, %[ones]\n\t" \
  "v_mov_b32 v108, %[ones]\n\t" \
  "v_mov_b32 v116, 0\n\t" \
  "v_mov_b32 v105, %[ones]\n\t" \
  "v_mov_b32 v109, %[ones]\n\t" \
  "v_mov_b32 v117, 0\n\t" \
  "v_mov_b32 v106, %[ones]\n\t" \
  "v_mov_b32 v110, %[ones]\n\t" \
  "v_mov_b32 v118, 0\n\t" \
  "v_mov_b32 v107, %[ones]\n\t" \
  "v_mov_b32 v111, %[ones]\n\t" \
  "v_mov_b32 v119, 0\n\t" \
  "s_mov_b32 %[scur], 0\n\t" \
  "s_mov_b32 %[snext], 0x4000\n\t" \
  "s_mov_b32 %[sfree], 0x8000\n\t" \
  "s_mov_b32 %[cnt], 8\n\t" \
  "ds_read_b128 v[100:103], %[lk]\n\t" \
  "s_waitcnt lgkmcnt(0)\n\t" \
  "v_mfma_f32_32x32x16_bf16 v[68:83], v[100:103], v[32:35], v[36:51]\n\t" \
  "ds_read_b128 v[100:103], %[lk] offset:1024\n\t" \
  "s_mov_b64 exec, %[vmask]\n\t" \
  "ds_read_b128 v[104:107], %[lv]\n\t" \
  "s_mov_b64 exec, -1\n\t" \
  "s_nop 7\n\t" \
  "1:\n\t" \
  "v_add_u32 v120, %[scur], %[lk]\n\t" \
  "v_add_u32 v121, %[snext], %[lk]\n\t" \
  "v_add_u32 v122, %[scur], %[lv]\n\t" \
  "v_add_u32 v123, %[snext], %[lv]\n\t" \
  "s_waitcnt lgkmcnt(1)\n\t" \
  "v_mfma_f32_32x32x16_bf16 v[84:99], v[100:103], v[32:35], v[36:51]\n\t" \
  "ds_read_b128 v[100:103], v120 offset:2048\n\t" \
  "v_mfma_f32_32x32x16_bf16 v[52:67], v[108:111], v[116:119], v[52:67]\n\t" \
  "s_mov_b64 exec, %[vmask]\n\t" \
  "ds_read_b128 v[108:111], v122 offset:512\n\t" \
  "s_mov_b64 exec, -1\n\t" \
  "v_exp_f32 v112, v69\n\t" \
  "v_exp_f32 v68, v68\n\t" \
  "v_exp_f32 v113, v71\n\t" \
  "v_mov_b32_sdwa v112, v68 dst_sel:WORD_0 dst_unused:UNUSED_PRESERVE src0_sel:WORD_1\n\t" \
  "v_exp_f32 v70, v70\n\t" \
  "v_exp_f32 v114, v73\n\t" \
  "v_mov_b32_sdwa v113, v70 dst_sel:WORD_0 dst_unused:UNUSED_PRESERVE src0_sel:WORD_1\n\t" \
  "v_exp_f32 v72, v72\n\t" \
  "v_exp_f32 v115, v75\n\t" \
  "v_mov_b32_sdwa v114, v72 dst_sel:WORD_0 dst_unused:UNUSED_PRESERVE src0_sel:WORD_1\n\t" \
  "v_exp_f32 v74, v74\n\t" \
  "v_exp_f32 v116, v77\n\t" \
  "v_mov_b32_sdwa v115, v74 dst_sel:WORD_0 dst_unused:UNUSED_PRESERVE src0_sel:WORD_1\n\t" \
  "v_exp_f32 v76, v76\n\t" \
  "v_exp_f32 v117, v79\n\t" \
  "s_waitcnt lgkmcnt(2)\n\t" \
  "v_mfma_f32_32x32x16_bf16 v[52:67], v[104:107], v[112:115], v[52:67]\n\t" \
  "s_mov_b64 exec, %[vmask]\n\t" \
  "ds_read_b128 v[104:107], v122 offset:1024\n\t" \
  "s_mov_b64 exec, -1\n\t" \
  "v_mov_b32_sdwa v116, v76 dst_sel:WORD_0 dst_unused:UNUSED_PRESERVE src0_sel:WORD_1\n\t" \
  "v_exp_f32 v78, v78\n\t" \
  "v_exp_f32 v118, v81\n\t" \
  "v_mov_b32_sdwa v117, v78 dst_sel:WORD_0 dst_unused:UNUSED_PRESERVE src0_sel:WORD_1\n\t" \
  "v_exp_f32 v80, v80\n\t" \
  "v_exp_f32 v119, v83\n\t" \
  "v_mov_b32_sdwa v118, v80 dst_sel:WORD_0 dst_unused:UNUSED_PRESERVE src0_sel:WORD_1\n\t" \
  "v_exp_f32 v82, v82\n\t" \
  "s_nop 0\n\t" \
  "v_mov_b32_sdwa v119, v82 dst_sel:WORD_0 dst_unused:UNUSED_PRESERVE src0_sel:WORD_1\n\t" \
  "s_waitcnt lgkmcnt(1)\n\t" \
  "v_mfma_f32_32x32x16_bf16 v[68:83], v[100:103], v[32:35], v[36:51]\n\t" \
  "ds_read_b128 v[100:103], v120 offset:3072\n\t" \
  "v_mfma_f32_32x32x16_bf16 v[52:67], v[108:111], v[116:119], v[52:67]\n\t" \
  "s_mov_b64 exec, %[vmask]\n\t" \
  "ds_read_b128 v[108:111], v122 offset:1536\n\t" \
  "s_mov_b64 exec, -1\n\t" \
  "v_exp_f32 v112, v85\n\t" \
  "v_exp_f32 v84, v84\n\t" \
  "v_exp_f32 v113, v87\n\t" \
  "v_mov_b32_sdwa v112, v84 dst_sel:WORD_0 dst_unused:UNUSED_PRESERVE src0_sel:WORD_1\n\t" \
  "v_exp_f32 v86, v86\n\t" \
  "v_exp_f32 v114, v89\n\t" \
  "v_mov_b32_sdwa v113, v86 dst_sel:WORD_0 dst_unused:UNUSED_PRESERVE src0_sel:WORD_1\n\t" \
  "v_exp_f32 v88, v88\n\t" \
  "v_exp_f32 v115, v91\n\t" \
  "v_mov_b32_sdwa v114, v88 dst_sel:WORD_0 dst_unused:UNUSED_PRESERVE src0_sel:WORD_1\n\t" \
  "v_exp_f32 v90, v90\n\t" \
  "v_exp_f32 v116, v93\n\t" \
  "v_mov_b32_sdwa v115, v90 dst_sel:WORD_0 dst_unused:UNUSED_PRESERVE src0_sel:WORD_1\n\t" \
  "v_exp_f32 v92, v92\n\t" \
  "v_exp_f32 v117, v95\n\t" \
  "s_waitcnt lgkmcnt(2)\n\t" \
  "v_mfma_f32_32x32x16_bf16 v[52:67], v[104:107], v[112:115], v[52:67]\n\t" \
  "s_mov_b64 exec, %[vmask]\n\t" \
  "ds_read_b128 v[104:107], v122 offset:2048\n\t" \
  "s_mov_b64 exec, -1\n\t" \
  "v_mov_b32_sdwa v116, v92 dst_sel:WORD_0 dst_unused:UNUSED_PRESERVE src0_sel:WORD_1\n\t" \
  "v_exp_f32 v94, v94\n\t" \
  "v_exp_f32 v118, v97\n\t" \
  "v_mov_b32_sdwa v117, v94 dst_sel:WORD_0 dst_unused:UNUSED_PRESERVE src0_sel:WORD_1\n\t" \
  "v_exp_f32 v96, v96\n\t" \
  "v_exp_f32 v119, v99\n\t" \
  "v_mov_b32_sdwa v118, v96 dst_sel:WORD_0 dst_unused:UNUSED_PRESERVE src0_sel:WORD_1\n\t" \
  "v_exp_f32 v98, v98\n\t" \
  "s_nop 0\n\t" \
  "v_mov_b32_sdwa v119, v98 dst_sel:WORD_0 dst_unused:UNUSED_PRESERVE src0_sel:WORD_1\n\t" \
  "s_waitcnt lgkmcnt(1)\n\t" \
  "v_mfma_f32_32x32x16_bf16 v[84:99], v[100:103], v[32:35], v[36:51]\n\t" \
  "ds_read_b128 v[100:103], v120 offset:4096\n\t" \
  "v_mfma_f32_32x32x16_bf16 v[52:67], v[108:111], v[116:119], v[52:67]\n\t" \
  "s_mov_b64 exec, %[vmask]\n\t" \
  "ds_read_b128 v[108:111], v122 offset:2560\n\t" \
  "s_mov_b64 exec, -1\n\t" \
  "v_exp_f32 v112, v69\n\t" \
  "v_exp_f32 v68, v68\n\t" \
  "v_exp_f32 v113, v71\n\t" \
  "v_mov_b32_sdwa v112, v68 dst_sel:WORD_0 dst_unused:UNUSED_PRESERVE src0_sel:WORD_1\n\t" \
  "v_exp_f32 v70, v70\n\t" \
  "v_exp_f32 v114, v73\n\t" \
  "v_mov_b32_sdwa v113, v70 dst_sel:WORD_0 dst_unused:UNUSED_PRESERVE src0_sel:WORD_1\n\t" \
  "v_exp_f32 v72, v72\n\t" \
  "v_exp_f32 v115, v75\n\t" \
  "v_mov_b32_sdwa v114, v72 dst_sel:WORD_0 dst_unused:UNUSED_PRESERVE src0_sel:WORD_1\n\t" \
  "v_exp_f32 v74, v74\n\t" \
  "v_exp_f32 v116, v77\n\t" \
  "v_mov_b32_sdwa v115, v74 dst_sel:WORD_0 dst_unused:UNUSED_PRESERVE src0_sel:WORD_1\n\t" \
  "v_exp_f32 v76, v76\n\t" \
  "v_exp_f32 v117, v79\n\t" \
  "s_waitcnt lgkmcnt(2)\n\t" \
  "v_mfma_f32_32x32x16_bf16 v[52:67], v[104:107], v[112:115], v[52:67]\n\t" \
  "s_mov_b64 exec, %[vmask]\n\t" \
  "ds_read_b128 v[104:107], v122 offset:3072\n\t" \
  "s_mov_b64 exec, -1\n\t" \
  "v_mov_b32_sdwa v116, v76 dst_sel:WORD_0 dst_unused:UNUSED_PRESERVE src0_sel:WORD_1\n\t" \
  "v_exp_f32 v78, v78\n\t" \
  "v_exp_f32 v118, v81\n\t" \
  "v_mov_b32_sdwa v117, v78 dst_sel:WORD_0 dst_unused:UNUSED_PRESERVE src0_sel:WORD_1\n\t" \
  "v_exp_f32 v80, v80\n\t" \
  "v_exp_f32 v119, v83\n\t" \
  "v_mov_b32_sdwa v118, v80 dst_sel:WORD_0 dst_unused:UNUSED_PRESERVE src0_sel:WORD_1\n\t" \
  "v_exp_f32 v82, v82\n\t" \
  "s_nop 0\n\t" \
  "v_mov_b32_sdwa v119, v82 dst_sel:WORD_0 dst_unused:UNUSED_PRESERVE src0_sel:WORD_1\n\t" \
  "s_waitcnt lgkmcnt(1)\n\t" \
  "v_mfma_f32_32x32x16_bf16 v[68:83], v[100:103], v[32:35], v[36:51]\n\t" \
  "ds_read_b128 v[100:103], v120 offset:5120\n\t" \
  "v_mfma_f32_32x32x16_bf16 v[52:67], v[108:111], v[116:119], v[52:67]\n\t" \
  "s_mov_b64 exec, %[vmask]\n\t" \
  "ds_read_b128 v[108:111], v122 offset:3584\n\t" \
  "s_mov_b64 exec, -1\n\t" \
  "v_exp_f32 v112, v85\n\t" \
  "v_exp_f32 v84, v84\n\t" \
  "v_exp_f32 v113, v87\n\t" \
  "v_mov_b32_sdwa v112, v84 dst_sel:WORD_0 dst_unused:UNUSED_PRESERVE src0_sel:WORD_1\n\t" \
  "v_exp_f32 v86, v86\n\t" \
  "v_exp_f32 v114, v89\n\t" \
  "v_mov_b32_sdwa v113, v86 dst_sel:WORD_0 dst_unused:UNUSED_PRESERVE src0_sel:WORD_1\n\t" \
  "v_exp_f32 v88, v88\n\t" \
  "v_exp_f32 v115, v91\n\t" \
  "v_mov_b32_sdwa v114, v88 dst_sel:WORD_0 dst_unused:UNUSED_PRESERVE src0_sel:WORD_1\n\t" \
  "v_exp_f32 v90, v90\n\t" \
  "v_exp_f32 v116, v93\n\t" \
  "v_mov_b32_sdwa v115, v90 dst_sel:WORD_0 dst_unused:UNUSED_PRESERVE src0_sel:WORD_1\n\t" \
  "v_exp_f32 v92, v92\n\t" \
  "v_exp_f32 v117, v95\n\t" \
  "s_waitcnt lgkmcnt(2)\n\t" \
  "v_mfma_f32_32x32x16_bf16 v[52:67], v[104:107], v[112:115], v[52:67]\n\t" \
  "s_mov_b64 exec, %[vmask]\n\t" \
  "ds_read_b128 v[104:107], v122 offset:4096\n\t" \
  "s_mov_b64 exec, -1\n\t" \
  "v_mov_b32_sdwa v116, v92 dst_sel:WORD_0 dst_unused:UNUSED_PRESERVE src0_sel:WORD_1\n\t" \
  "v_exp_f32 v94, v94\n\t" \
  "v_exp_f32 v118, v97\n\t" \
  "v_mov_b32_sdwa v117, v94 dst_sel:WORD_0 dst_unused:UNUSED_PRESERVE src0_sel:WORD_1\n\t" \
  "v_exp_f32 v96, v96\n\t" \
  "v_exp_f32 v119, v99\n\t" \
  "v_mov_b32_sdwa v118, v96 dst_sel:WORD_0 dst_unused:UNUSED_PRESERVE src0_sel:WORD_1\n\t" \
  "v_exp_f32 v98, v98\n\t" \
  "s_nop 0\n\t" \
  "v_mov_b32_sdwa v119, v98 dst_sel:WORD_0 dst_unused:UNUSED_PRESERVE src0_sel:WORD_1\n\t" \
  "s_waitcnt lgkmcnt(1)\n\t" \
  "v_mfma_f32_32x32x16_bf16 v[84:99], v[100:103], v[32:35], v[36:51]\n\t" \
  "ds_read_b128 v[100:103], v120 offset:6144\n\t" \
  "v_mfma_f32_32x32x16_bf16 v[52:67], v[108:111], v[116:119], v[52:67]\n\t" \
  "s_mov_b64 exec, %[vmask]\n\t" \
  "ds_read_b128 v[108:111], v122 offset:4608\n\t" \
  "s_mov_b64 exec, -1\n\t" \
  "v_exp_f32 v112, v69\n\t" \
  "v_exp_f32 v68, v68\n\t" \
  "v_exp_f32 v113, v71\n\t" \
  "v_mov_b32_sdwa v112, v68 dst_sel:WORD_0 dst_unused:UNUSED_PRESERVE src0_sel:WORD_1\n\t" \
  "v_exp_f32 v70, v70\n\t" \
  "v_exp_f32 v114, v73\n\t" \
  "v_mov_b32_sdwa v113, v70 dst_sel:WORD_0 dst_unused:UNUSED_PRESERVE src0_sel:WORD_1\n\t" \
  "v_exp_f32 v72, v72\n\t" \
  "v_exp_f32 v115, v75\n\t" \
  "v_mov_b32_sdwa v114, v72 dst_sel:WORD_0 dst_unused:UNUSED_PRESERVE src0_sel:WORD_1\n\t" \
  "v_exp_f32 v74, v74\n\t" \
  "v_exp_f32 v116, v77\n\t" \
  "v_mov_b32_sdwa v115, v74 dst_sel:WORD_0 dst_unused:UNUSED_PRESERVE src0_sel:WORD_1\n\t" \
  "v_exp_f32 v76, v76\n\t" \
  "v_exp_f32 v117, v79\n\t" \
  "s_waitcnt lgkmcnt(2)\n\t" \
  "v_mfma_f32_32x32x16_bf16 v[52:67], v[104:107], v[112:115], v[52:67]\n\t" \
  "s_mov_b64 exec, %[vmask]\n\t" \
  "ds_read_b128 v[104:107], v122 offset:5120\n\t" \
  "s_mov_b64 exec, -1\n\t" \
  "v_mov_b32_sdwa v116, v76 dst_sel:WORD_0 dst_unused:UNUSED_PRESERVE src0_sel:WORD_1\n\t" \
  "v_exp_f32 v78, v78\n\t" \
  "v_exp_f32 v118, v81\n\t" \
  "v_mov_b32_sdwa v117, v78 dst_sel:WORD_0 dst_unused:UNUSED_PRESERVE src0_sel:WORD_1\n\t" \
  "v_exp_f32 v80, v80\n\t" \
  "v_exp_f32 v119, v83\n\t" \
  "v_mov_b32_sdwa v118, v80 dst_sel:WORD_0 dst_unused:UNUSED_PRESERVE src0_sel:WORD_1\n\t" \
  "v_exp_f32 v82, v82\n\t" \
  "s_waitcnt vmcnt(0)\n\t" \
  "s_barrier\n\t" \
  "s_add_u32 m0, %[sfree], %[m0k]\n\t" \
  "s_nop 0\n\t" \
  "buffer_load_dwordx4 %[goff], %[rk], %[ssrc] offen lds\n\t" \
  "s_add_u32 m0, m0, 0x2000\n\t" \
  "s_nop 0\n\t" \
  "buffer_load_dwordx4 %[goff], %[rv], %[ssrc] offen lds\n\t" \
  "s_add_u32 %[ssrc], %[ssrc], 0x2000\n\t" \
  "v_mov_b32_sdwa v119, v82 dst_sel:WORD_0 dst_unused:UNUSED_PRESERVE src0_sel:WORD_1\n\t" \
  "s_waitcnt lgkmcnt(1)\n\t" \
  "v_mfma_f32_32x32x16_bf16 v[68:83], v[100:103], v[32:35], v[36:51]\n\t" \
  "ds_read_b128 v[100:103], v120 offset:7168\n\t" \
  "v_mfma_f32_32x32x16_bf16 v[52:67], v[108:111], v[116:119], v[52:67]\n\t" \
  "s_mov_b64 exec, %[vmask]\n\t" \
  "ds_read_b128 v[108:111], v122 offset:5632\n\t" \
  "s_mov_b64 exec, -1\n\t" \
  "v_exp_f32 v112, v85\n\t" \
  "v_exp_f32 v84, v84\n\t" \
  "v_exp_f32 v113, v87\n\t" \
  "v_mov_b32_sdwa v112, v84 dst_sel:WORD_0 dst_unused:UNUSED_PRESERVE src0_sel:WORD_1\n\t" \
  "v_exp_f32 v86, v86\n\t" \
  "v_exp_f32 v114, v89\n\t" \
  "v_mov_b32_sdwa v113, v86 dst_sel:WORD_0 dst_unused:UNUSED_PRESERVE src0_sel:WORD_1\n\t" \
  "v_exp_f32 v88, v88\n\t" \
  "v_exp_f32 v115, v91\n\t" \
  "v_mov_b32_sdwa v114, v88 dst_sel:WORD_0 dst_unused:UNUSED_PRESERVE src0_sel:WORD_1\n\t" \
  "v_exp_f32 v90, v90\n\t" \
  "v_exp_f32 v116, v93\n\t" \
  "v_mov_b32_sdwa v115, v90 dst_sel:WORD_0 dst_unused:UNUSED_PRESERVE src0_sel:WORD_1\n\t" \
  "v_exp_f32 v92, v92\n\t" \
  "v_exp_f32 v117, v95\n\t" \
  "s_waitcnt lgkmcnt(2)\n\t" \
  "v_mfma_f32_32x32x16_bf16 v[52:67], v[104:107], v[112:115], v[52:67]\n\t" \
  "s_mov_b64 exec, %[vmask]\n\t" \
  "ds_read_b128 v[104:107], v122 offset:6144\n\t" \
  "s_mov_b64 exec, -1\n\t" \
  "v_mov_b32_sdwa v116, v92 dst_sel:WORD_0 dst_unused:UNUSED_PRESERVE src0_sel:WORD_1\n\t" \
  "v_exp_f32 v94, v94\n\t" \
  "v_exp_f32 v118, v97\n\t" \
  "v_mov_b32_sdwa v117, v94 dst_sel:WORD_0 dst_unused:UNUSED_PRESERVE src0_sel:WORD_1\n\t" \
  "v_exp_f32 v96, v96\n\t" \
  "v_exp_f32 v119, v99\n\t" \
  "v_mov_b32_sdwa v118, v96 dst_sel:WORD_0 dst_unused:UNUSED_PRESERVE src0_sel:WORD_1\n\t" \
  "v_exp_f32 v98, v98\n\t" \
  "s_nop 0\n\t" \
  "v_mov_b32_sdwa v119, v98 dst_sel:WORD_0 dst_unused:UNUSED_PRESERVE src0_sel:WORD_1\n\t" \
  "s_waitcnt lgkmcnt(1)\n\t" \
  "v_mfma_f32_32x32x16_bf16 v[84:99], v[100:103], v[32:35], v[36:51]\n\t" \
  "ds_read_b128 v[100:103], v121 offset:0\n\t" \
  "v_mfma_f32_32x32x16_bf16 v[52:67], v[108:111], v[116:119], v[52:67]\n\t" \
  "s_mov_b64 exec, %[vmask]\n\t" \
  "ds_read_b128 v[108:111], v122 offset:6656\n\t" \
  "s_mov_b64 exec, -1\n\t" \
  "v_exp_f32 v112, v69\n\t" \
  "v_exp_f32 v68, v68\n\t" \
  "v_exp_f32 v113, v71\n\t" \
  "v_mov_b32_sdwa v112, v68 dst_sel:WORD_0 dst_unused:UNUSED_PRESERVE src0_sel:WORD_1\n\t" \
  "v_exp_f32 v70, v70\n\t" \
  "v_exp_f32 v114, v73\n\t" \
  "v_mov_b32_sdwa v113, v70 dst_sel:WORD_0 dst_unused:UNUSED_PRESERVE src0_sel:WORD_1\n\t" \
  "v_exp_f32 v72, v72\n\t" \
  "v_exp_f32 v115, v75\n\t" \
  "v_mov_b32_sdwa v114, v72 dst_sel:WORD_0 dst_unused:UNUSED_PRESERVE src0_sel:WORD_1\n\t" \
  "v_exp_f32 v74, v74\n\t" \
  "v_exp_f32 v116, v77\n\t" \
  "v_mov_b32_sdwa v115, v74 dst_sel:WORD_0 dst_unused:UNUSED_PRESERVE src0_sel:WORD_1\n\t" \
  "v_exp_f32 v76, v76\n\t" \
  "v_exp_f32 v117, v79\n\t" \
  "s_waitcnt lgkmcnt(2)\n\t" \
  "v_mfma_f32_32x32x16_bf16 v[52:67], v[104:107], v[112:115], v[52:67]\n\t" \
  "s_mov_b64 exec, %[vmask]\n\t" \
  "ds_read_b128 v[104:107], v122 offset:7168\n\t" \
  "s_mov_b64 exec, -1\n\t" \
  "v_mov_b32_sdwa v116, v76 dst_sel:WORD_0 dst_unused:UNUSED_PRESERVE src0_sel:WORD_1\n\t" \
  "v_exp_f32 v78, v78\n\t" \
  "v_exp_f32 v118, v81\n\t" \
  "v_mov_b32_sdwa v117, v78 dst_sel:WORD_0 dst_unused:UNUSED_PRESERVE src0_sel:WORD_1\n\t" \
  "v_exp_f32 v80, v80\n\t" \
  "v_exp_f32 v119, v83\n\t" \
  "v_mov_b32_sdwa v118, v80 dst_sel:WORD_0 dst_unused:UNUSED_PRESERVE src0_sel:WORD_1\n\t" \
  "v_exp_f32 v82, v82\n\t" \
  "s_nop 0\n\t" \
  "v_mov_b32_sdwa v119, v82 dst_sel:WORD_0 dst_unused:UNUSED_PRESERVE src0_sel:WORD_1\n\t" \
  "s_waitcnt lgkmcnt(1)\n\t" \
  "v_mfma_f32_32x32x16_bf16 v[68:83], v[100:103], v[32:35], v[36:51]\n\t" \
  "ds_read_b128 v[100:103], v121 offset:1024\n\t" \
  "v_mfma_f32_32x32x16_bf16 v[52:67], v[108:111], v[116:119], v[52:67]\n\t" \
  "s_mov_b64 exec, %[vmask]\n\t" \
  "ds_read_b128 v[108:111], v122 offset:7680\n\t" \
  "s_mov_b64 exec, -1\n\t" \
  "v_exp_f32 v112, v85\n\t" \
  "v_exp_f32 v84, v84\n\t" \
  "v_exp_f32 v113, v87\n\t" \
  "v_mov_b32_sdwa v112, v84 dst_sel:WORD_0 dst_unused:UNUSED_PRESERVE src0_sel:WORD_1\n\t" \
  "v_exp_f32 v86, v86\n\t" \
  "v_exp_f32 v114, v89\n\t" \
  "v_mov_b32_sdwa v113, v86 dst_sel:WORD_0 dst_unused:UNUSED_PRESERVE src0_sel:WORD_1\n\t" \
  "v_exp_f32 v88, v88\n\t" \
  "v_exp_f32 v115, v91\n\t" \
  "v_mov_b32_sdwa v114, v88 dst_sel:WORD_0 dst_unused:UNUSED_PRESERVE src0_sel:WORD_1\n\t" \
  "v_exp_f32 v90, v90\n\t" \
  "v_exp_f32 v116, v93\n\t" \
  "v_mov_b32_sdwa v115, v90 dst_sel:WORD_0 dst_unused:UNUSED_PRESERVE src0_sel:WORD_1\n\t" \
  "v_exp_f32 v92, v92\n\t" \
  "v_exp_f32 v117, v95\n\t" \
  "s_waitcnt lgkmcnt(2)\n\t" \
  "v_mfma_f32_32x32x16_bf16 v[52:67], v[104:107], v[112:115], v[52:67]\n\t" \
  "s_mov_b64 exec, %[vmask]\n\t" \
  "ds_read_b128 v[104:107], v123 offset:0\n\t" \
  "s_mov_b64 exec, -1\n\t" \
  "v_mov_b32_sdwa v116, v92 dst_sel:WORD_0 dst_unused:UNUSED_PRESERVE src0_sel:WORD_1\n\t" \
  "v_exp_f32 v94, v94\n\t" \
  "v_exp_f32 v118, v97\n\t" \
  "v_mov_b32_sdwa v117, v94 dst_sel:WORD_0 dst_unused:UNUSED_PRESERVE src0_sel:WORD_1\n\t" \
  "v_exp_f32 v96, v96\n\t" \
  "v_exp_f32 v119, v99\n\t" \
  "v_mov_b32_sdwa v118, v96 dst_sel:WORD_0 dst_unused:UNUSED_PRESERVE src0_sel:WORD_1\n\t" \
  "v_exp_f32 v98, v98\n\t" \
  "s_nop 0\n\t" \
  "v_mov_b32_sdwa v119, v98 dst_sel:WORD_0 dst_unused:UNUSED_PRESERVE src0_sel:WORD_1\n\t" \
  "s_mov_b32 %[stmp], %[scur]\n\t" \
  "s_mov_b32 %[scur], %[snext]\n\t" \
  "s_mov_b32 %[snext], %[sfree]\n\t" \
  "s_mov_b32 %[sfree], %[stmp]\n\t" \
  "s_sub_u32 %[cnt], %[cnt], 1\n\t" \
  "s_cmp_lg_u32 %[cnt], 0\n\t" \
  "s_cbranch_scc1 1b\n\t" \
  "s_waitcnt lgkmcnt(0)\n\t" \
  "v_mfma_f32_32x32x16_bf16 v[52:67], v[108:111], v[116:119], v[52:67]\n\t" \
  "s_waitcnt vmcnt(0)\n\t" \
  "s_nop 15\n\t"
#define ATTN_CLOBBERS "v36", "v37", "v38", "v39", "v40", "v41", "v42", "v43", "v44", "v45", "v46", "v47", "v48", "v49", "v50", "v51", "v68", "v69", "v70", "v71", "v72", "v73", "v74", "v75", "v76", "v77", "v78", "v79", "v80", "v81", "v82", "v83", "v84", "v85", "v86", "v87", "v88", "v89", "v90", "v91", "v92", "v93", "v94", "v95", "v96", "v97", "v98", "v99", "v100", "v101", "v102", "v103", "v104", "v105", "v106", "v107", "v108", "v109", "v110", "v111", "v112", "v113", "v114", "v115", "v116", "v117", "v118", "v119", "v120", "v121", "v122", "v123", "m0", "scc", "memory"

constexpr int AT_SLOT = 16384;
DEV void attn_role(char* lds, int a, char* ws) {
#if defined(__HIP_DEVICE_COMPILE__)
  STAMP_DECL
  RSTAMP(0); STAMP(1);
  const int head = (a & 7) * 4 + (a >> 6), qb = (a >> 3) & 7;
  const int tid = threadIdx.x, lane = tid & 63, l31 = lane & 31, h = lane >> 5;
  const int wid = __builtin_amdgcn_readfirstlane(tid >> 6);
  auto krs = mk_rsrc(ws + OFF_K + (size_t)head * (S_ * 32), S_ * 32);
  auto vrs = mk_rsrc(ws + OFF_V + (size_t)head * (S_ * 32), S_ * 32);
  const int q = qb * 256 + wid * 32 + l31;
  bf16x8 qf = *(const bf16x8*)(ws + OFF_Q + (size_t)head * (S_ * 32) + q * 32 + h * 16);
  auto issue = [&](int c, int slot) {
    char* sb = lds + slot * AT_SLOT + wid * 1024;
    dma16(krs, sb, lane * 16, c * 8192 + wid * 1024);
    dma16(vrs, sb + 8192, lane * 16, c * 8192 + wid * 1024);
  };
  const unsigned ldsb = (unsigned)(size_t)lds;
  const unsigned lk = ldsb + lane * 16;
  const unsigned lv = ldsb + 8192 + h * 256 + (l31 & 15) * 16;
  const unsigned goff = lane * 16;
  const unsigned m0k = ldsb + wid * 1024;
  int* flag = (int*)(lds + 3 * AT_SLOT);
  float mref = 0.f;
  f32x16 o;
  for (int attempt = 0; attempt < 2; ++attempt) {
    if (attempt == 1) {
      float mx = -__builtin_inff();
      for (int c = 0; c < 8; ++c) {
        issue(c, 0);
        WAIT_V(0); RAW_BARRIER();
        for (int t = 0; t < 8; ++t) {
          bf16x8 kf = *(const bf16x8*)(lds + t * 1024 + lane * 16);
          f32x16 z = {};
          f32x16 s = mfma32(kf, qf, z);
#pragma unroll
          for (int r = 0; r < 16; ++r) mx = fmaxf(mx, s[r]);
        }
        RAW_BARRIER();
      }
      mx = fmaxf(mx, __shfl_xor(mx, 32));
      mref = mx;
    }
    STAMP(2);
    issue(0, 0);
    issue(1, 1);
    STAMP(3);
    WAIT_V(2); RAW_BARRIER();
    STAMP(4);
    if (attempt == 0) {
      bf16x8 kf = *(const bf16x8*)(lds + lane * 16);
      f32x16 z = {};
      f32x16 s = mfma32(kf, qf, z);
      float mx = s[0];
#pragma unroll
      for (int r = 1; r < 16; ++r) mx = fmaxf(mx, s[r]);
      mx = fmaxf(mx, __shfl_xor(mx, 32));
      mref = mx;
    }
    STAMP(5);
    {
      float negm = -mref;
      unsigned scur, snext, sfree, stmp, cnt, ssrc = 2 * 8192 + wid * 1024;
      unsigned long long vmask = 0x0000FFFF0000FFFFull;
      const unsigned ones = (l31 == 16 || l31 == 20) ? 0x3f803f80u : 0u;
      asm volatile(ATTN_ASM
                   : "={v[52:67]}"(o), [scur] "=&s"(scur), [snext] "=&s"(snext), [sfree] "=&s"(sfree), [stmp] "=&s"(stmp),
                     [cnt] "=&s"(cnt), [ssrc] "+s"(ssrc)
                   : "{v[32:35]}"(qf), [negm] "v"(negm), [lk] "v"(lk), [lv] "v"(lv), [goff] "v"(goff), [rk] "s"(krs), [rv] "s"(vrs),
                     [m0k] "s"(m0k), [vmask] "s"(vmask), [ones] "v"(ones)
                   : ATTN_CLOBBERS);
    }
    STAMP(40);
    bool bad = !(o[8] < 1.0e30f);
#pragma unroll
    for (int r = 0; r < 8; ++r) bad |= !(fabsf(o[r]) < 3.0e38f);
    if (tid == 0) *flag = 0;
    RAW_BARRIER();
    if (attempt == 0 && __any(bad)) { if (lane == 0) *flag = 1; }
    RAW_BARRIER();
    int f = *flag;
    RAW_BARRIER();
    if (f == 0) break;
  }
  float inv = 1.0f / o[8];
  u32x4 ov = widen(pk4(o[0] * inv, o[1] * inv, o[2] * inv, o[3] * inv), pk4(o[4] * inv, o[5] * inv, o[6] * inv, o[7] * inv));
  {
    const unsigned l2 = fresh_lane();
    auto ors = mk_rsrc(ws + OFF_O, 64 * S_ * 16);
    __builtin_amdgcn_raw_buffer_store_b128(ov, ors, ((l2 >> 5) * S_ + (l2 & 31)) * 16, (2 * head * S_ + qb * 256 + wid * 32) * 16, 16);
    WAIT_V(0);
    BARRIER_NOWAIT();
    if (tid == 0) __hip_atomic_fetch_add((unsigned*)(ws + OFF_CNT) + (64 + qb) * 64, 1u, __ATOMIC_RELAXED, __HIP_MEMORY_SCOPE_AGENT);
  }
  STAMP(41); RSTAMP(42); STAMP_FLUSH(0);
#endif
}

#define K2_XBAR 43
namespace bsched {
constexpr int NG = 54;
constexpr int pos(int g) {
  if (g < 8) return 8 * g;
  if (g < 24) return 12 * ((g - 8) % 6);
  int u = (g - 24) % 10;
  return u == 0 ? 72 : u == 1 ? 48 : u == 2 ? 56 : u == 3 ? 64 : 8 * (u - 4);
}
constexpr int npw(int g) { return g < 8 ? 2 : g < 24 ? 3 : g == 45 ? 1 : 2; }
constexpr int iss(int g) {
  if (g < 8) return -1;
  if (g < 24) { int t = g - 8; return t == 0 ? 3 : t == 1 ? 4 : t == 2 ? 6 : t == 3 ? 7 : t == 4 ? 8 : t == 5 ? 9 : t + 6; }
  int u = g - 24;
  return u == 0 ? 9 : u == 1 ? 22 : u == 2 ? 23 : u == 3 ? 23 : u == 4 ? 24 : u == 5 ? 25 : u == 6 ? 25 : u <= 9 ? 26 : u + 19;
}
constexpr int pub(int g) { return g < 8 ? g : g < 24 ? g + 2 : g + 3; }
constexpr int pubbed(int bi) { return bi < 8 ? bi : (bi >= 10 && bi <= 25) ? bi - 2 : (bi >= 27 && bi <= 56) ? bi - 3 : -1; }
constexpr bool before(int a, int b) { return iss(a) != iss(b) ? iss(a) < iss(b) : a < b; }
constexpr int waitcnt(int g) {
  int n = 0;
  for (int o = 0; o < NG; ++o) if (o != g && before(g, o) && iss(o) < pub(g)) n += npw(o);
  return n;
}
}

DEV u32x4 ld_sc1(__amdgpu_buffer_rsrc_t r, unsigned voff, unsigned soff) { return __builtin_amdgcn_raw_buffer_load_b128(r, voff, soff, 16); }
DEV void st_sc1(__amdgpu_buffer_rsrc_t r, u32x4 v, unsigned voff, unsigned soff) { __builtin_amdgcn_raw_buffer_store_b128(v, r, voff, soff, 16); }

DEV void b_signal_wait(unsigned* cnt, int lane) {
  if (lane == 0) {
    __hip_atomic_fetch_add(cnt, 1u, __ATOMIC_RELAXED, __HIP_MEMORY_SCOPE_AGENT);
    for (int it = 0; it < (1 << 22); ++it) {
      if (__hip_atomic_load(cnt, __ATOMIC_RELAXED, __HIP_MEMORY_SCOPE_AGENT) >= 8u) break;
      __builtin_amdgcn_s_sleep(2);
    }
  }
}

DEV void b_wait(unsigned* cnt, unsigned target, int lane) {
  if (lane == 0) {
    for (int it = 0; it < (1 << 22); ++it) {
      if (__hip_atomic_load(cnt, __ATOMIC_RELAXED, __HIP_MEMORY_SCOPE_AGENT) >= target) break;
      __builtin_amdgcn_s_sleep(2);
    }
  }
}

DEV void b_loader(char* lds, int lw, int lane, int grp, int j, int c0, const u16* w2, const u16* w3, const u16* w4, const u16* wo, char* ws) {
#if defined(__HIP_DEVICE_COMPILE__)
  using namespace bsched;
  auto r2 = mk_rsrc(w2, D_ * D_ * 2);
  auto r3 = mk_rsrc(w3, D_ * F_ * 2);
  auto r4 = mk_rsrc(w4, F_ * D_ * 2);
  auto r5 = mk_rsrc(wo, D_ * D_ * 2);
  unsigned va[2], vb[3];
#pragma unroll
  for (int i = 0; i < 2; ++i) {
    unsigned bb = (lw + 4 * i) * 1024 + lane * 16, row = bb >> 7, cp = (bb >> 4) & 7;
    va[i] = row * 1024 + j * 128 + ((cp ^ (((row >> 1) & 1) << 2)) << 4);
  }
#pragma unroll
  for (int i = 0; i < 3; ++i) {
    unsigned bb = (lw + 4 * i) * 1024 + lane * 16, row = bb / 384, cp = (bb % 384) >> 4;
    vb[i] = row * (F_ * 2) + c0 * 2 + ((cp ^ (((row >> 1) & 1) << 2)) << 4);
  }
  auto issue = [&](auto G) {
    constexpr int g = G;
    char* sb = lds + pos(g) * 1024 + lw * 1024;
    if constexpr (g < 8) { dma16(r2, sb, va[0], g * 65536); dma16(r2, sb + 4096, va[1], g * 65536); }
    else if constexpr (g < 24) {
#pragma unroll
      for (int i = 0; i < 3; ++i) dma16(r3, sb + i * 4096, vb[i], (g - 8) * 32 * (F_ * 2));
    } else if constexpr (g < 46) {
      dma16(r4, sb, va[0], (g - 24) * 65536);
      if constexpr (g != 45) dma16(r4, sb + 4096, va[1], (g - 24) * 65536);
    } else {
      dma16(r5, sb, va[0], (g - 46) * 65536);
      dma16(r5, sb + 4096, va[1], (g - 46) * 65536);
    }
  };
  static_for<NG>([&](auto G) { if constexpr (iss(G) == -1) issue(G); });
  static_for<58>([&](auto BI) {
    constexpr int bi = BI;
    constexpr int pg = pubbed(bi);
    if constexpr (bi == 10 || bi == 27) {
      if (lw == 0) b_signal_wait((unsigned*)(ws + OFF_CNT) + (bi == 10 ? grp : 32 + grp) * 64, lane);
    }
    if constexpr (bi == K2_XBAR) {
      if (lw == 0) b_wait((unsigned*)(ws + OFF_CNT) + (64 + (grp >> 2)) * 64, 32u, lane);
    }
    if constexpr (pg >= 0) WAIT_V(waitcnt(pg));
    BARRIER_NOWAIT();
    static_for<NG>([&](auto G) { if constexpr (iss(G) == bi) issue(G); });
  });
#endif
}

#define K2_PM1 0
#define K2_PM2 0
#define K2_PM3 0
#define K2_PE1 0
#define K2_PE2 2
#define K2_PE3 2
#define BPRIO(n) do { SCHED_FENCE(); __builtin_amdgcn_s_setprio(n); SCHED_FENCE(); } while (0)
DEV void b_compute(char* lds, int wid, int lane, int grp, int j, int c0, int ntile, const u16* x, char* ws, u16* out) {
#if defined(__HIP_DEVICE_COMPILE__)
  using namespace bsched;
  STAMP_DECL
  RSTAMP(0); STAMP(1);
  const int wm = wid & 1, kp = wid >> 1;
  const int m0 = grp * 64;
  auto hrs = mk_rsrc(ws + OFF_HB, 64 * HB_ROWS * 16);
  auto x1rs = mk_rsrc(ws + OFF_X1, 64 * S_ * 16);
  auto h2rs = mk_rsrc(ws + OFF_H2, 172 * S_ * 16);
  auto xrs = mk_rsrc(x, S_ * D_ * 2);
  const unsigned col0 = j * 64 + kp * 32;
  unsigned l31, h, blk, q, p, row, sw, cpart, wo13[2];
  auto lane_init = [&]() {
    lane = fresh_lane();
    l31 = lane & 31; h = lane >> 5; blk = (lane >> 4) & 1; q = (lane & 15) >> 2; p = lane & 3;
    row = m0 + wm * 32 + l31; sw = (q >> 1) & 1; cpart = 8 * (p & 1);
#pragma unroll
    for (int a = 0; a < 2; ++a) wo13[a] = (16 * kp + 8 * h + q) * 128 + (((4 * (a ^ kp) + 2 * blk + (p >> 1)) ^ (sw << 2)) << 4) + cpart;
  };
  lane_init();
  u32x2 xv[4];
#pragma unroll
  for (int i = 0; i < 4; ++i) xv[i] = __builtin_amdgcn_raw_buffer_load_b64(xrs, (row * D_ + col0 + 4 * h) * 2, i * 16, 0);
  const f32x2 st0 = *(const f32x2*)(ws + OFF_ST0 + (size_t)row * 8);

  auto phase13 = [&](auto G0_, auto NI_, auto QD_, auto loadA, f32x16 (&acc)[2], auto&& first_issue_done) {
    constexpr int G0 = G0_, NI = NI_, QD = QD_;
    u32x4 aq[QD];
    static_for<QD>([&](auto I) { if constexpr (I < NI) aq[I] = loadA(I); });
    first_issue_done();
    bf16x8 wf[2][2];
    auto rd = [&](auto I) {
      constexpr int i = I;
      const char* sb = lds + pos(G0 + (i >> 1)) * 1024 + (i & 1) * 4096;
      wf[i & 1][0] = tr_frag(sb + wo13[0], sb + wo13[0] + 512);
      wf[i & 1][1] = tr_frag(sb + wo13[1], sb + wo13[1] + 512);
    };
    auto mm = [&](auto I) {
      constexpr int i = I;
      bf16x8 a = __builtin_bit_cast(bf16x8, aq[i % QD]);
      acc[0] = mfma32(wf[i & 1][0], a, acc[0]);
      acc[1] = mfma32(wf[i & 1][1], a, acc[1]);
      if constexpr (i + QD < NI) aq[i % QD] = loadA(IC<i + QD>{});
    };
    static_for<NI>([&](auto I) {
      constexpr int i = I;
      if constexpr ((i & 1) == 0) { BARRIER_NOWAIT(); STAMP(2 + (G0 + (i >> 1))); }
      rd(I);
      if constexpr (i > 0) mm(IC<i - 1>{});
      SCHED_FENCE();
    });
    mm(IC<NI - 1>{});
  };
  auto exchange = [&](f32x16 (&acc)[2], int xpos) {
    char* xb = lds + xpos * 1024;
    char* mine = xb + (wm * 2 + (kp ^ 1)) * 4096 + lane * 16;
    const char* theirs = xb + (wm * 2 + kp) * 4096 + lane * 16;
#pragma unroll
    for (int i = 0; i < 4; ++i) *(f32x4*)(mine + i * 1024) = f32x4{acc[1][4 * i], acc[1][4 * i + 1], acc[1][4 * i + 2], acc[1][4 * i + 3]};
    RAW_BARRIER();
#pragma unroll
    for (int i = 0; i < 4; ++i) {
      f32x4 t = *(const f32x4*)(theirs + i * 1024);
#pragma unroll
      for (int c = 0; c < 4; ++c) acc[0][4 * i + c] += t[c];
    }
  };

  {
    f32x16 acc[2] = {};
    const unsigned va = (h * HB_ROWS + row) * 16 + kp * (2 * HB_ROWS * 16);
    auto loadA = [&](auto I) {
      constexpr int i = I;
      constexpr int kk0 = 4 * (i >> 1) + 2 * (i & 1), g = i >> 2;
      constexpr int d = g == 0 ? -1 : g == 1 ? 1 : g == 2 ? -2 : 2;
      return __builtin_amdgcn_raw_buffer_load_b128(hrs, va, kk0 * (2 * HB_ROWS * 16) + (2 + d) * 16, 0);
    };
    BPRIO(K2_PM1);
    phase13(IC<0>{}, IC<16>{}, IC<10>{}, loadA, acc, [] {});
    BPRIO(K2_PE1);
    exchange(acc, 64);
    STAMP(50);
    float v[16], a1 = 0.f, a2 = 0.f;
#pragma unroll
    for (int i = 0; i < 4; ++i) {
      v[4 * i + 0] = (bf_lo(xv[i][0]) - st0[0]) * st0[1] + acc[0][4 * i + 0];
      v[4 * i + 1] = (bf_hi(xv[i][0]) - st0[0]) * st0[1] + acc[0][4 * i + 1];
      v[4 * i + 2] = (bf_lo(xv[i][1]) - st0[0]) * st0[1] + acc[0][4 * i + 2];
      v[4 * i + 3] = (bf_hi(xv[i][1]) - st0[0]) * st0[1] + acc[0][4 * i + 3];
    }
#pragma unroll
    for (int i = 0; i < 16; ++i) { a1 += v[i]; a2 += v[i] * v[i]; }
    a1 = xhalf_sum(a1); a2 = xhalf_sum(a2);
#pragma unroll
    for (int g = 0; g < 2; ++g) st_sc1(x1rs, pack8(v + 8 * g), ((col0 / 8 + 2 * g + h) * S_ + row) * 16, 0);
    if (lane < 32) {
      auto srs = mk_rsrc(ws + OFF_ST1, 16 * S_ * 8);
      u32x2 sv = {__float_as_uint(a1), __float_as_uint(a2)};
      __builtin_amdgcn_raw_buffer_store_b64(sv, srs, ((col0 >> 5) * S_ + row) * 8, 0, 16);
    }
    WAIT_V(0);
    BARRIER_NOWAIT();
    STAMP(51);
  }
  {
    const int wn = kp;
    lane_init();
    float cs[3];
    {
      const float* cp = (const float*)(ws + OFF_CS3) + h * 4 * CS3_LD + c0 + wn * 96 + l31;
#pragma unroll
      for (int a = 0; a < 3; ++a) {
        float t = 0.f;
#pragma unroll
        for (int i = 0; i < 4; ++i) t += cp[i * CS3_LD + a * 32];
        cs[a] = xhalf_sum(t);
      }
    }
    unsigned wo2[3];
#pragma unroll
    for (int a = 0; a < 3; ++a) wo2[a] = (8 * h + q) * 384 + (((4 * (3 * wn + a) + 2 * blk + (p >> 1)) ^ (sw << 2)) << 4) + cpart;
    f32x16 acc[3] = {};
    constexpr int QD = 8;
    u32x4 aq[QD];
    const unsigned va = (h * S_ + row) * 16;
    BARRIER_NOWAIT();
    STAMP(52);
    u32x2 sp[8];
    {
      auto srs = mk_rsrc(ws + OFF_ST1, 16 * S_ * 8);
#pragma unroll
      for (int i = 0; i < 8; ++i) sp[i] = __builtin_amdgcn_raw_buffer_load_b64(srs, ((h * 8 + i) * S_ + row) * 8, 0, 16);
    }
    static_for<QD>([&](auto I) { aq[I] = ld_sc1(x1rs, va, I * 65536); });
    bf16x8 wf[2][3];
    auto rd = [&](auto I) {
      constexpr int i = I;
      const char* sb = lds + pos(8 + (i >> 1)) * 1024 + (i & 1) * (16 * 384);
#pragma unroll
      for (int a = 0; a < 3; ++a) wf[i & 1][a] = tr_frag(sb + wo2[a], sb + wo2[a] + 4 * 384);
    };
    auto mm = [&](auto I) {
      constexpr int i = I;
      bf16x8 a = __builtin_bit_cast(bf16x8, aq[i % QD]);
#pragma unroll
      for (int t = 0; t < 3; ++t) acc[t] = mfma32(wf[i & 1][t], a, acc[t]);
      if constexpr (i + QD < 32) aq[i % QD] = ld_sc1(x1rs, va, (i + QD) * 65536);
    };
    float mu, rs;
    {
      float a1 = 0.f, a2 = 0.f;
#pragma unroll
      for (int t = 0; t < 8; ++t) { a1 += __uint_as_float(sp[t][0]); a2 += __uint_as_float(sp[t][1]); }
      a1 = xhalf_sum(a1); a2 = xhalf_sum(a2);
      mu = a1 * (1.0f / D_);
      rs = __builtin_amdgcn_rsqf(fmaxf(a2 * (1.0f / D_) - mu * mu, 0.f) + LN_EPS);
      SCHED_FENCE();
    }
    BPRIO(K2_PM2);
    static_for<32>([&](auto I) {
      constexpr int i = I;
      if constexpr ((i & 1) == 0 && i > 0) { BARRIER_NOWAIT(); STAMP(2 + 8 + (i >> 1)); }
      rd(I);
      if constexpr (i > 0) mm(IC<i - 1>{});
      SCHED_FENCE();
    });
    mm(IC<31>{});
    BPRIO(K2_PE2);
    {
      unsigned mh = pk_bf16(-mu, -mu);
      float mlo = -mu - bf_lo(mh);
      u32x4 xe = {h ? 0u : mh, h ? 0u : (pk_bf16(mlo, 0.f)), 0u, 0u};
#pragma unroll
      for (int t = 0; t < 3; ++t) {
        unsigned ch = pk_bf16(cs[t], cs[t]);
        float clo = cs[t] - bf_lo(ch);
        u32x4 we = {h ? 0u : ((ch & 0xffffu) | (pk_bf16(clo, clo) << 16)), h ? 0u : (ch & 0xffffu), 0u, 0u};
        acc[t] = mfma32(__builtin_bit_cast(bf16x8, we), __builtin_bit_cast(bf16x8, xe), acc[t]);
      }
    }
    STAMP(53);
#pragma unroll
    for (int t = 0; t < 3; ++t) {
      if (3 * wn + t < ntile) {
        float v[16];
#pragma unroll
        for (int i = 0; i < 16; ++i) v[i] = gelu_tanh(acc[t][i] * rs);
        const unsigned kt8 = (c0 + (3 * wn + t) * 32) >> 3;
#pragma unroll
        for (int g = 0; g < 2; ++g) st_sc1(h2rs, pack8(v + 8 * g), ((kt8 + 2 * g + h) * S_ + row) * 16, 0);
      }
    }
    WAIT_V(0);
    BARRIER_NOWAIT();
    STAMP(54);
  }
  {
    lane_init();
    auto ors = mk_rsrc(ws + OFF_O, 64 * S_ * 16);
    const unsigned va = (h * S_ + row) * 16 + kp * 65536;
    u32x2 xr[4], x1r[4];
#pragma unroll
    for (int t = 0; t < 4; ++t) {
      xr[t] = __builtin_amdgcn_raw_buffer_load_b64(xrs, (row * D_ + col0 + 4 * h) * 2, t * 16, 0);
      x1r[t] = __builtin_amdgcn_raw_buffer_load_b64(x1rs, ((col0 / 8 + t) * S_ + row) * 16 + 8 * h, 0, 16);
    }
    BARRIER_NOWAIT();
    STAMP(55);
    constexpr int NW = 43, NI = NW + 16, QD = 10, NS = 16;
    constexpr int XS = 2 * (K2_XBAR - 27);
    u32x4 aq[NS];
    auto loadH = [&](auto I) { constexpr int i = I; return ld_sc1(h2rs, va, (4 * (i >> 1) + 2 * (i & 1)) * 65536); };
    auto loadO = [&](auto N) { constexpr int n = N; return ld_sc1(ors, va, (4 * (n >> 1) + 2 * (n & 1)) * 65536); };
    static_for<QD>([&](auto I) { aq[I] = loadH(I); });
    f32x16 acc[2] = {};
#pragma unroll
    for (int i = 0; i < 4; ++i) {
      acc[0][4 * i + 0] = bf_lo(xr[i][0]) + bf_lo(x1r[i][0]);
      acc[0][4 * i + 1] = bf_hi(xr[i][0]) + bf_hi(x1r[i][0]);
      acc[0][4 * i + 2] = bf_lo(xr[i][1]) + bf_lo(x1r[i][1]);
      acc[0][4 * i + 3] = bf_hi(xr[i][1]) + bf_hi(x1r[i][1]);
    }
    BPRIO(K2_PM3);
    bf16x8 wf[2][2];
    auto rd = [&](auto I) {
      constexpr int i = I;
      constexpr int g = i < NW ? 24 + (i >> 1) : 46 + ((i - NW) >> 1), hf = i < NW ? (i & 1) : ((i - NW) & 1);
      const char* sb = lds + pos(g) * 1024 + hf * 4096;
      wf[i & 1][0] = tr_frag(sb + wo13[0], sb + wo13[0] + 512);
      wf[i & 1][1] = tr_frag(sb + wo13[1], sb + wo13[1] + 512);
    };
    auto mm = [&](auto I) {
      constexpr int i = I;
      bf16x8 a = __builtin_bit_cast(bf16x8, aq[i % NS]);
      acc[0] = mfma32(wf[i & 1][0], a, acc[0]);
      acc[1] = mfma32(wf[i & 1][1], a, acc[1]);
      if constexpr (i + QD < NW) aq[(i + QD) % NS] = loadH(IC<i + QD>{});
      constexpr int n = i + NS - NW;
      if constexpr (n >= 0 && n < 16 && i >= XS - 1) aq[i % NS] = loadO(IC<(n >= 0 && n < 16) ? n : 0>{});
    };
    static_for<NI>([&](auto I) {
      constexpr int i = I;
      constexpr bool bar = i < NW ? ((i & 1) == 0 && i > 0) : (((i - NW) & 1) == 0);
      if constexpr (bar) { BARRIER_NOWAIT(); if constexpr (i < NW) STAMP(2 + 24 + (i >> 1)); }
      if constexpr (i == XS) {
        static_for<16>([&](auto N) { constexpr int n = N; if constexpr (n + NW - NS < XS - 1) aq[(NW + n) % NS] = loadO(N); });
      }
      rd(I);
      if constexpr (i > 0) mm(IC<i - 1>{});
      SCHED_FENCE();
    });
    mm(IC<NI - 1>{});
    STAMP(57);
    BPRIO(K2_PE3);
    exchange(acc, 48);
    STAMP(56);
    float v[16];
#pragma unroll
    for (int i = 0; i < 16; ++i) v[i] = acc[0][i];
    auto outrs = mk_rsrc(out, S_ * D_ * 2);
    const unsigned l2 = fresh_lane();
    const unsigned vo = ((m0 + wm * 32 + (l2 & 31)) * D_ + col0 + (l2 >> 5) * 8) * 2;
#pragma unroll
    for (int g = 0; g < 2; ++g) __builtin_amdgcn_raw_buffer_store_b128(pack8(v + 8 * g), outrs, vo, g * 32, 0);
  }
  STAMP(60); RSTAMP(61); STAMP_FLUSH(0);
#endif
}

constexpr int K2_LDS = 81920;
__global__ __launch_bounds__(512, 4) void k2(const u16* __restrict__ x, const u16* __restrict__ w2, const u16* __restrict__ w3,
                                             const u16* __restrict__ w4, const u16* __restrict__ wo, char* __restrict__ ws,
                                             u16* __restrict__ out) {
#if defined(__HIP_DEVICE_COMPILE__)
  extern __shared__ __attribute__((aligned(16))) char lds[];
#define K2_SWAP 1
#define K2_BPRIO 2
#define K2_APRIO 1
  const int b = blockIdx.x ^ (K2_SWAP * 256);
  if (b >= 256) { __builtin_amdgcn_s_setprio(K2_APRIO); attn_role(lds, b - 256, ws); return; }
  const int lane = threadIdx.x & 63;
  const int wid = __builtin_amdgcn_readfirstlane(threadIdx.x >> 6);
  const int grp = (b & 7) * 4 + (b >> 6), j = (b >> 3) & 7;
  const int c0 = j < 3 ? j * 192 : 576 + (j - 3) * 160;
  const int ntile = j < 3 ? 6 : 5;
#define K2_BDELAY 24
  __builtin_amdgcn_s_sleep(K2_BDELAY);
  __builtin_amdgcn_s_setprio(K2_BPRIO);
  if (wid >= 4) b_loader(lds, wid - 4, lane, grp, j, c0, w2, w3, w4, wo, ws);
  else b_compute(lds, wid, lane, grp, j, c0, ntile, x, ws, out);
#endif
}

#define SEL(i) true
extern "C" void kernel_launch(void* const* d_in, const int* in_sizes, int n_in, void* d_out, int out_size,
                              void* d_ws, size_t ws_size, hipStream_t stream) {
  const u16* x = (const u16*)d_in[0];
  const u16* wqkv = (const u16*)d_in[1];
  const u16* wo = (const u16*)d_in[2];
  const u16* w1 = (const u16*)d_in[4];
  const u16* w2 = (const u16*)d_in[5];
  const u16* w3 = (const u16*)d_in[6];
  const u16* w4 = (const u16*)d_in[7];
  char* ws = (char*)d_ws;
  (void)hipFuncSetAttribute((const void*)k1, hipFuncAttributeMaxDynamicSharedMemorySize, K1_LDS);
  (void)hipFuncSetAttribute((const void*)k2, hipFuncAttributeMaxDynamicSharedMemorySize, K2_LDS);
  if (SEL(0)) k1<<<256, 512, K1_LDS, stream>>>(x, wqkv, w1, w3, w2, w4, wo, ws);
  if (SEL(1)) k2<<<512, 512, K2_LDS, stream>>>(x, w2, w3, w4, wo, ws, (u16*)d_out);
}
```

```cpp
#define A_HINTCNT 1
#define X_TLB 1
#define X_KVI 1
#include <hip/hip_runtime.h>
#include <stdint.h>

typedef __attribute__((ext_vector_type(8))) __bf16 bf16x8;
typedef __attribute__((ext_vector_type(2))) __bf16 bf16x2;
typedef __attribute__((ext_vector_type(4))) short s16x4;
typedef __attribute__((ext_vector_type(16))) float f32x16;
typedef __attribute__((ext_vector_type(4))) float f32x4;
typedef __attribute__((ext_vector_type(2))) float f32x2;
typedef __attribute__((ext_vector_type(4))) unsigned u32x4;
typedef __attribute__((ext_vector_type(2))) unsigned u32x2;
typedef __attribute__((address_space(3))) void* ldsptr_t;
typedef unsigned short u16;

#define DEV __device__ __forceinline__

constexpr int S_ = 2048, D_ = 512, H_ = 32, F_ = 1376;
constexpr float LN_EPS = 1e-5f;
constexpr float QSCALE = 0.25f * 1.4426950408889634f;

constexpr size_t MB_ = 1 << 20, KB_ = 1 << 10;
constexpr size_t OFF_Q = 0, OFF_K = 2 * MB_, OFF_V = X_KVI ? 2 * MB_ + S_ * 32 : 4 * MB_, OFF_O = 6 * MB_, OFF_X1 = 8 * MB_, OFF_HB = 10 * MB_;
constexpr unsigned KV_STR = (X_KVI ? 2 : 1) * S_ * 32;
constexpr size_t OFF_ST0 = 12 * MB_ + 64 * KB_, OFF_CS3 = 12 * MB_ + 160 * KB_;
constexpr size_t OFF_ST1 = 12 * MB_ + 256 * KB_, OFF_H2 = 13 * MB_;
constexpr size_t OFF_FL = 20 * MB_;
#define HB_OFF 8
constexpr int HB_ROWS = S_ + 2 * HB_OFF;
constexpr int CS3_P = 8, CS3_LD = 1408;

template <int N> struct IC { static constexpr int value = N; constexpr operator int() const { return N; } };
template <int I, int N, class F> DEV void static_for_impl(F&& f) {
  if constexpr (I < N) { f(IC<I>{}); static_for_impl<I + 1, N>(f); }
}
template <int N, class F> DEV void static_for(F&& f) { static_for_impl<0, N>(f); }

DEV unsigned pk_bf16(float lo, float hi) {
  f32x2 v = {lo, hi};
  return __builtin_bit_cast(unsigned, __builtin_convertvector(v, bf16x2));
}
DEV float bf_lo(unsigned u) { return __uint_as_float(u << 16); }
DEV float bf_hi(unsigned u) { return __uint_as_float(u & 0xffff0000u); }
constexpr float GELU_C1 = -2.0f * 0.7978845608028654f * 1.4426950408889634f, GELU_C2 = GELU_C1 * 0.044715f;
DEV float gelu_tanh(float x) {
  const float c1 = -2.0f * 0.7978845608028654f * 1.4426950408889634f;
  const float c2 = c1 * 0.044715f;
  float t = x * (c1 + c2 * x * x);
  float e = __builtin_amdgcn_exp2f(t);
  return x * __builtin_amdgcn_rcpf(1.0f + e);
}
DEV float dot2(unsigned a, unsigned b, float c) {
  return __builtin_amdgcn_fdot2_f32_bf16(__builtin_bit_cast(bf16x2, a), __builtin_bit_cast(bf16x2, b), c, false);
}
DEV float xhalf_sum(float v) {
  auto r = __builtin_amdgcn_permlane32_swap(__float_as_uint(v), __float_as_uint(v), false, false);
  return __uint_as_float(r[0]) + __uint_as_float(r[1]);
}
#define STAMP(i)
#define RSTAMP(i)
#define STAMP_DECL
#define HWSTAMP()
#define STAMP_ARG
#define STAMP_PASS
#define STAMP_FLUSH(w)
#define PROBE -1
#define G_PFMID 6
#define PROBE_T0()
#define PROBE_AT(k)
#define PROBE_END()
#define WAIT_V(n) asm volatile("s_waitcnt vmcnt(%0)" ::"n"(n) : "memory")
#define WAIT_L(n) asm volatile("s_waitcnt lgkmcnt(%0)" ::"n"(n) : "memory")
#define SCHED_FENCE() __builtin_amdgcn_sched_barrier(0)
#define RAW_BARRIER() do { WAIT_L(0); __builtin_amdgcn_s_barrier(); } while (0)
#define BARRIER_NOWAIT() do { SCHED_FENCE(); asm volatile("s_barrier" ::: "memory"); SCHED_FENCE(); } while (0)

DEV unsigned fresh_lane() {
  unsigned l;
  asm volatile("v_mbcnt_lo_u32_b32 %0, -1, 0\n\tv_mbcnt_hi_u32_b32 %0, -1, %0" : "=v"(l));
  return l;
}
DEV __amdgpu_buffer_rsrc_t mk_rsrc(const void* p, unsigned bytes) {
  return __builtin_amdgcn_make_buffer_rsrc((void*)p, 0, (int)bytes, 0x00020000);
}
DEV __amdgpu_buffer_rsrc_t mk_rsrc_u(const void* p, unsigned bytes) {
  const unsigned long long a = (unsigned long long)p;
  const unsigned lo = __builtin_amdgcn_readfirstlane((unsigned)a), hi = __builtin_amdgcn_readfirstlane((unsigned)(a >> 32));
  return mk_rsrc((const void*)(((unsigned long long)hi << 32) | lo), bytes);
}
DEV void dma16(__amdgpu_buffer_rsrc_t r, char* lds_dst, unsigned voff, unsigned soff) {
  __builtin_amdgcn_raw_ptr_buffer_load_lds(r, (ldsptr_t)lds_dst, 16, voff, soff, 0, 0);
}
DEV void dma16c(__amdgpu_buffer_rsrc_t r, char* lds_dst, unsigned voff, unsigned soff) {
  __builtin_amdgcn_raw_ptr_buffer_load_lds(r, (ldsptr_t)lds_dst, 16, voff, soff, 0, 16);
}
DEV u32x4 ld_sc1(__amdgpu_buffer_rsrc_t r, unsigned voff, unsigned soff) { return __builtin_amdgcn_raw_buffer_load_b128(r, voff, soff, 16); }
DEV void st_sc1(__amdgpu_buffer_rsrc_t r, u32x4 v, unsigned voff, unsigned soff) { __builtin_amdgcn_raw_buffer_store_b128(v, r, voff, soff, 16); }
DEV unsigned ldw_sc1(__amdgpu_buffer_rsrc_t r, unsigned voff) { return __builtin_amdgcn_raw_buffer_load_b32(r, voff, 0, 16); }
DEV void stw_sc1(__amdgpu_buffer_rsrc_t r, unsigned v, unsigned voff) { __builtin_amdgcn_raw_buffer_store_b32(v, r, voff, 0, 16); }
#define WAIT_V0() __builtin_amdgcn_s_waitcnt(0x0f70)
template <int N> DEV void sleep_n() {
  if constexpr (N > 127) { __builtin_amdgcn_s_sleep(127); sleep_n<N - 127>(); }
  else if constexpr (N > 0) __builtin_amdgcn_s_sleep(N);
}
constexpr int POLL_MAX = 1 << 20;
#define POLL_SLEEP 4
extern "C" __device__ unsigned long long dispatch_index() __asm("llvm.amdgcn.dispatch.id");
DEV u32x2 launch_token() {
  const unsigned long long id = dispatch_index(), qp = (unsigned long long)__builtin_amdgcn_queue_ptr();
  return u32x2{(unsigned)id ^ 0x5bd1e995u, (unsigned)(qp >> 8) + (unsigned)(id >> 32) * 0x9e3779b1u};
}
DEV __amdgpu_buffer_rsrc_t flag_rsrc(char* ws) { return mk_rsrc(ws + OFF_FL, 512 * KB_); }
DEV void flag_post(char* ws, unsigned blk, unsigned word, bool on) {
  __builtin_amdgcn_raw_buffer_store_b64(launch_token(), flag_rsrc(ws), on ? blk * 1024 + word * 8 : 0x7f000000u, 0, 16);
}
DEV u32x2 flag_load(char* ws, unsigned blk, unsigned word) { return __builtin_amdgcn_raw_buffer_load_b64(flag_rsrc(ws), blk * 1024 + word * 8, 0, 16); }
DEV bool flag_ok(u32x2 v) { const u32x2 t = launch_token(); return __all(v[0] == t[0] && v[1] == t[1]); }
DEV void flag_wait(char* ws, unsigned blk, unsigned word) {
#pragma unroll 1
  for (int it = 0; it < POLL_MAX; ++it) {
    if (flag_ok(flag_load(ws, blk, word))) break;
    __builtin_amdgcn_s_sleep(POLL_SLEEP);
  }
}
DEV unsigned attn_blk(unsigned head, unsigned qb) { return (head & 3) * 64 + qb * 8 + (head >> 2); }
DEV unsigned mix_blk(unsigned grp, unsigned j) { return 256 + (grp >> 2) + j * 8 + (grp & 3) * 64; }
DEV bf16x8 tr_frag(const char* p0, const char* p1) {
  s16x4 a = __builtin_amdgcn_ds_read_tr16_b64_v4i16((__attribute__((address_space(3))) s16x4*)p0);
  s16x4 b = __builtin_amdgcn_ds_read_tr16_b64_v4i16((__attribute__((address_space(3))) s16x4*)p1);
  typedef __attribute__((ext_vector_type(8))) short s16x8;
  s16x8 r = __builtin_shufflevector(a, b, 0, 1, 2, 3, 4, 5, 6, 7);
  return __builtin_bit_cast(bf16x8, r);
}
DEV f32x16 mfma32(bf16x8 a, bf16x8 b, f32x16 c) { return __builtin_amdgcn_mfma_f32_32x32x16_bf16(a, b, c, 0, 0, 0); }
DEV u32x2 pk4(float a, float b, float c, float d) { u32x2 r; r[0] = pk_bf16(a, b); r[1] = pk_bf16(c, d); return r; }
DEV u32x4 widen(u32x2 X, u32x2 Y) {
  u32x4 o;
  auto a = __builtin_amdgcn_permlane32_swap(X[0], Y[0], false, false);
  auto b = __builtin_amdgcn_permlane32_swap(X[1], Y[1], false, false);
  o[0] = a[0]; o[1] = b[0]; o[2] = a[1]; o[3] = b[1];
  return o;
}
DEV u32x4 pack8(const float* v) { return widen(pk4(v[0], v[1], v[2], v[3]), pk4(v[4], v[5], v[6], v[7])); }

constexpr int G_SL = 8192, G_W0 = 5 * G_SL, G_NS = 16;
constexpr int G_SCR = 2 * G_SL;
#define G_STAUX 16
DEV unsigned g_swz(unsigned row) { return ((row >> 1) & 2) | ((row >> 3) & 1); }
DEV f32x2 fma2(f32x2 a, f32x2 b, f32x2 c) { return __builtin_elementwise_fma(a, b, c); }
DEV f32x2 splat2(float v) { return f32x2{v, v}; }
DEV void k1_colsums(f32x2 (&c)[8], const float* cs0, int h) {
#pragma unroll
  for (int j = 0; j < 4; ++j) {
    f32x4 t = *(const f32x4*)(cs0 + 8 * j + 4 * h) + *(const f32x4*)(cs0 + 128 + 8 * j + 4 * h);
    c[2 * j] = f32x2{t[0], t[1]}; c[2 * j + 1] = f32x2{t[2], t[3]};
  }
}
template <int KIND>
DEV void k1_epi_s(const f32x16& a, float mu, float rs, const f32x2 (&c)[8], int J, int s, int h, char* ws) {
  f32x2 v[8];
  const f32x2 nmu = splat2(-mu);
#pragma unroll
  for (int i = 0; i < 8; ++i) v[i] = fma2(c[i], nmu, f32x2{a[2 * i], a[2 * i + 1]});
  if (KIND == 2) {
    const float r2 = rs * rs;
    rs *= __builtin_amdgcn_rsqf((1.0f - LN_EPS * r2) + LN_EPS);
    const float irs = __builtin_amdgcn_rcpf(rs);
    const f32x2 k1 = splat2(GELU_C1 * rs), k2 = splat2(GELU_C2 * rs * rs * rs), ir = splat2(irs);
#pragma unroll
    for (int i = 0; i < 8; ++i) {
      f32x2 t = v[i] * fma2(v[i] * v[i], k2, k1);
      f32x2 e = {__builtin_amdgcn_exp2f(t[0]), __builtin_amdgcn_exp2f(t[1])};
      f32x2 d = fma2(e, ir, ir);
      v[i] *= f32x2{__builtin_amdgcn_rcpf(d[0]), __builtin_amdgcn_rcpf(d[1])};
    }
  } else {
    const f32x2 r = splat2(KIND == 0 ? rs * QSCALE : rs);
#pragma unroll
    for (int i = 0; i < 8; ++i) v[i] *= r;
  }
#pragma unroll
  for (int g = 0; g < 2; ++g) {
    u32x4 o = widen(u32x2{pk_bf16(v[4 * g][0], v[4 * g][1]), pk_bf16(v[4 * g + 1][0], v[4 * g + 1][1])},
                    u32x2{pk_bf16(v[4 * g + 2][0], v[4 * g + 2][1]), pk_bf16(v[4 * g + 3][0], v[4 * g + 3][1])});
    unsigned dst;
    if (KIND == 0) dst = OFF_Q + (unsigned)(2 * J + g) * (S_ * 32) + (unsigned)(s * 32 + h * 16);
    else if (KIND == 1) dst = OFF_K + (unsigned)(2 * J + g) * KV_STR + (unsigned)((s >> 5) * 1024 + h * 512 + (s & 31) * 16);
    else dst = OFF_HB + (unsigned)(((4 * J + 2 * g + h) * HB_ROWS + s + HB_OFF) * 16);
    __builtin_amdgcn_raw_buffer_store_b128(o, mk_rsrc(ws, 16 * MB_), dst, 0, G_STAUX);
  }
}
DEV void k1_epi_v(const f32x16& a, const float* st, float cs, int J, int tile, int l31, int h, char* ws) {
  f32x2 v[8];
  const f32x2 ncs = splat2(-cs);
#pragma unroll
  for (int j = 0; j < 4; ++j) {
    f32x4 mu = *(const f32x4*)(st + 8 * j + 4 * h), rs = *(const f32x4*)(st + 64 + 8 * j + 4 * h);
    v[2 * j] = fma2(f32x2{mu[0], mu[1]}, ncs, f32x2{a[4 * j], a[4 * j + 1]}) * f32x2{rs[0], rs[1]};
    v[2 * j + 1] = fma2(f32x2{mu[2], mu[3]}, ncs, f32x2{a[4 * j + 2], a[4 * j + 3]}) * f32x2{rs[2], rs[3]};
  }
  const int n = J * 32 + l31, head = n >> 4, d = n & 15;
  const unsigned base = OFF_V + (unsigned)head * KV_STR + tile * 1024;
#pragma unroll
  for (int s = 0; s < 2; ++s) {
    u32x4 o;
#pragma unroll
    for (int j = 0; j < 4; ++j) o[j] = pk_bf16(v[4 * s + j][0], v[4 * s + j][1]);
    __builtin_amdgcn_raw_buffer_store_b128(o, mk_rsrc(ws, 16 * MB_), base + (s * 2 + h) * 256 + d * 16, 0, G_STAUX);
  }
}
DEV f32x2 row_stats(const float* rsb, int i) {
  f32x2 a = *(const f32x2*)(rsb + i * 2), c = *(const f32x2*)(rsb + (128 + i) * 2);
  float mu = (a[0] + c[0]) * (1.0f / D_);
  float var = fmaxf((a[1] + c[1]) * (1.0f / D_) - mu * mu, 0.f);
  return f32x2{mu, __builtin_amdgcn_rsqf(var + LN_EPS)};
}

DEV bf16x8 tr_frag3(__attribute__((address_space(3))) char* p) {
  s16x4 a = __builtin_amdgcn_ds_read_tr16_b64_v4i16((__attribute__((address_space(3))) s16x4*)p);
  s16x4 b = __builtin_amdgcn_ds_read_tr16_b64_v4i16((__attribute__((address_space(3))) s16x4*)(p + 1024));
  typedef __attribute__((ext_vector_type(8))) short s16x8;
  s16x8 r = __builtin_shufflevector(a, b, 0, 1, 2, 3, 4, 5, 6, 7);
  return __builtin_bit_cast(bf16x8, r);
}
DEV void o_prefill(char* ws, int a, int wid) {
  const int head = (a & 7) * 4 + (a >> 6), qb = (a >> 3) & 7;
  const unsigned l2 = fresh_lane();
  st_sc1(mk_rsrc(ws + OFF_O, 64 * S_ * 16), u32x4{~0u, ~0u, ~0u, ~0u}, ((l2 >> 5) * S_ + (l2 & 31) + 2 * head * S_ + qb * 256 + wid * 32) * 16, 0);
}

template <int TYPE>
DEV void g_compute(char* lds, int wm, int wn, int mt, int nt, char* ws STAMP_ARG) {
  constexpr bool VT = TYPE == 2;
  const int m0 = mt * 128;
  unsigned ao[2], wb[2][2];
  {
    const unsigned lane = fresh_lane();
    const unsigned l31 = lane & 31, h = lane >> 5, blk = (lane >> 4) & 1, q = (lane & 15) >> 2, p = lane & 3;
    const unsigned mask = wm ^ wn;
    const unsigned row = wm * 64 + l31;
    const unsigned cpart = (2 * blk + (p >> 1)) * 16 + 8 * (p & 1);
#pragma unroll
    for (int j = 0; j < 2; ++j) {
      ao[j] = (unsigned)(size_t)lds + row * 64 + (((2 * (j ^ mask) + h) ^ g_swz(row)) << 4);
      asm volatile("" : "+v"(ao[j]));
#pragma unroll
      for (int a = 0; a < 2; ++a) {
        wb[j][a] = (unsigned)(size_t)lds + G_W0 + (8 * h + q) * 256 + (((wn + 2 * a) ^ q) << 6) + cpart + (j ^ mask) * 4096;
        asm volatile("" : "+v"(wb[j][a]));
      }
    }
  }
  typedef __attribute__((address_space(3))) char* lp_t;
  f32x16 acc[2][2] = {};
  float s1[2][2] = {}, s2[2][2] = {}, cs[2][2] = {};
  bf16x8 xf[2][2], wf[2][2];
  auto rd = [&](int t, int j) {
    const int so = (t % 5) * G_SL;
    xf[j][0] = *(const __attribute__((address_space(3))) bf16x8*)((lp_t)(size_t)ao[j] + so);
    wf[j][0] = tr_frag3((lp_t)(size_t)wb[j][0] + so);
    wf[j][1] = tr_frag3((lp_t)(size_t)wb[j][1] + so);
    xf[j][1] = *(const __attribute__((address_space(3))) bf16x8*)((lp_t)(size_t)ao[j] + so + 2048);
  };
  auto mm = [&](int j) {
#pragma unroll
    for (int r = 0; r < 2; ++r)
#pragma unroll
      for (int a = 0; a < 2; ++a)
        acc[r][a] = VT ? mfma32(xf[j][r], wf[j][a], acc[r][a]) : mfma32(wf[j][a], xf[j][r], acc[r][a]);
    __builtin_amdgcn_sched_group_barrier(0x008, 1, 0);
    __builtin_amdgcn_sched_group_barrier(0x100, 3, 0);
    __builtin_amdgcn_sched_group_barrier(0x008, 1, 0);
    __builtin_amdgcn_sched_group_barrier(0x100, 3, 0);
    __builtin_amdgcn_sched_group_barrier(0x008, 2, 0);
  };
  auto dots = [&]() {
#pragma unroll
    for (int i = 0; i < 4; ++i) {
#pragma unroll
      for (int r = 0; r < 2; ++r) {
        unsigned u = __builtin_bit_cast(u32x4, xf[0][r])[i];
        s1[r][i & 1] = dot2(u, 0x3f803f80u, s1[r][i & 1]); s2[r][i & 1] = dot2(u, u, s2[r][i & 1]);
      }
#pragma unroll
      for (int a = 0; a < 2; ++a) cs[a][i & 1] = dot2(__builtin_bit_cast(u32x4, wf[1][a])[i], 0x3f803f80u, cs[a][i & 1]);
    }
    asm volatile("" : "+v"(s1[0][0]), "+v"(s1[0][1]), "+v"(s1[1][0]), "+v"(s1[1][1]), "+v"(s2[0][0]), "+v"(s2[0][1]), "+v"(s2[1][0]), "+v"(s2[1][1]),
                      "+v"(cs[0][0]), "+v"(cs[0][1]), "+v"(cs[1][0]), "+v"(cs[1][1]));
  };
#pragma unroll
  for (int t = 0; t < G_NS; ++t) {
    WAIT_L(0);
    BARRIER_NOWAIT();
    STAMP(4 + t);
    if (t == 0) PROBE_AT(6);
    if (t == G_PFMID) {
      const int b = blockIdx.x;
      o_prefill(ws, b, wm + 2 * wn); o_prefill(ws, b, wm + 2 * wn + 4);
    }
    rd(t, 0);
    if (t > 0) mm(1);
    SCHED_FENCE();
    rd(t, 1);
    mm(0);
    SCHED_FENCE();
    dots();
    SCHED_FENCE();
  }
  mm(1);
  PROBE_AT(7);
  STAMP(40);
  const unsigned lane = fresh_lane();
  const unsigned l31 = lane & 31, h = lane >> 5;
  float* rsb = (float*)(lds + G_SCR);
  float* csb = rsb + 512;
  float* fin = csb + 256;
#pragma unroll
  for (int r = 0; r < 2; ++r) {
    float a = xhalf_sum(s1[r][0] + s1[r][1]), b = xhalf_sum(s2[r][0] + s2[r][1]), c = xhalf_sum(cs[r][0] + cs[r][1]);
    if (h == 0) *(f32x2*)(rsb + (wn * 128 + wm * 64 + r * 32 + l31) * 2) = f32x2{a, b};
    else csb[wm * 128 + (wn + 2 * r) * 32 + l31] = c;
  }
  STAMP(43);
  WAIT_V0();
  RAW_BARRIER();
  STAMP(44);
  const int c = nt >> 1;
  if (VT) {
    float* sw = fin + (wn * 2 + wm) * 128;
    f32x2 st = row_stats(rsb, wm * 64 + lane);
    sw[lane] = st[0]; sw[64 + lane] = st[1];
    float cs[2];
#pragma unroll
    for (int a = 0; a < 2; ++a) cs[a] = csb[(wn + 2 * a) * 32 + l31] + csb[128 + (wn + 2 * a) * 32 + l31];
    WAIT_L(0);
#pragma unroll
    for (int r = 0; r < 2; ++r)
#pragma unroll
      for (int a = 0; a < 2; ++a) k1_epi_v(acc[r][a], sw + r * 32, cs[a], 4 * (c - 4) + wn + 2 * a, (m0 + wm * 64 + r * 32) >> 5, l31, h, ws);
  } else {
    f32x2 st[2];
#pragma unroll
    for (int r = 0; r < 2; ++r) st[r] = row_stats(rsb, wm * 64 + r * 32 + l31);
    STAMP(45);
    const int s0 = m0 + wm * 64 + l31;
    if (TYPE == 0 && nt == 0 && wn == 0 && h == 0) {
#pragma unroll
      for (int r = 0; r < 2; ++r)
        __builtin_amdgcn_raw_buffer_store_b64(__builtin_bit_cast(u32x2, st[r]), mk_rsrc(ws + OFF_ST0, S_ * 8), (s0 + r * 32) * 8, 0, 16);
    }
    f32x2 cc[8];
    k1_colsums(cc, csb + (wn + 2) * 32, h);
#pragma unroll
    for (int r = 0; r < 2; ++r) {
      k1_epi_s<TYPE == 0 ? 2 : 1>(acc[r][1], st[r][0], st[r][1], cc, TYPE == 0 ? 2 * c + wn : 4 * c + wn + 2, s0 + r * 32, h, ws);
      STAMP(48 + r);
    }
    if (TYPE == 0 && (mt == 0 || mt == 15) && wm == (mt ? 1 : 0) && l31 < 2) {
#pragma unroll
      for (int g = 0; g < 2; ++g)
        st_sc1(mk_rsrc(ws, 16 * MB_), u32x4{0, 0, 0, 0}, OFF_HB + (unsigned)(((4 * (2 * c + wn) + 2 * g + h) * HB_ROWS + (mt ? S_ + HB_OFF : HB_OFF - 2) + l31) * 16), 0);
    }
    k1_colsums(cc, csb + wn * 32, h);
#pragma unroll
    for (int r = 0; r < 2; ++r) k1_epi_s<TYPE == 0 ? 0 : 1>(acc[r][0], st[r][0], st[r][1], cc, (TYPE == 0 ? 2 : 4) * c + wn, s0 + r * 32, h, ws);
    STAMP(47);
  }
  STAMP(41);
  WAIT_V0();
  BARRIER_NOWAIT();
  PROBE_AT(1);
  STAMP(46); RSTAMP(42); STAMP_FLUSH(4);
}

#define G_PRO 3
constexpr int g_last(int t) {
  int l = t + 3;
  if (G_PRO == 1) l = t == 0 ? 1 : t == 1 ? 2 : t == 2 ? 4 : t + 3;
  if (G_PRO == 2) l = t == 0 ? 2 : t == 1 ? 3 : t == 2 ? 5 : t + 3;
  if (G_PRO == 3) l = t == 0 ? 1 : t == 1 ? 2 : t == 2 ? 3 : t == 3 ? 5 : t + 3;
  if (G_PRO == 4) l = t == 0 ? 0 : t == 1 ? 2 : t == 2 ? 4 : t + 3;
  return l < G_NS ? l : G_NS - 1;
}
DEV void g_phase(char* lds, int b, int wid, const u16* x, const u16* wqkv, const u16* w1, char* ws, unsigned cpf) {
#if defined(__HIP_DEVICE_COMPILE__)
  STAMP_DECL
  RSTAMP(0); STAMP(1);
  const int xcd = b & 7, bi = b >> 3;
  const int mt = (xcd & 3) * 4 + (bi & 3), nt = (xcd >> 2) * 8 + (bi >> 2);
  if (__builtin_expect(wid < 4, 1)) {
    const int lw = wid, lane = threadIdx.x & 63;
    auto xrs = mk_rsrc(x, S_ * D_ * 2);
    unsigned va[2];
#pragma unroll
    for (int i = 0; i < 2; ++i) {
      const unsigned row = (lw + 4 * i) * 16 + (lane >> 2), cp = lane & 3;
      va[i] = (mt * 128 + row) * 1024 + ((cp ^ g_swz(row)) << 4);
    }
    const unsigned kl = lane >> 4, seg = ((lane >> 2) & 3) ^ kl;
    const int c = nt >> 1;
    const bool fromw1 = !(nt & 1) && seg >= 2;
    const unsigned wstride = fromw1 ? 1024u : 3072u;
    const char* wbase = fromw1 ? (const char*)w1 + c * 128 + (seg - 2) * 64
                               : (const char*)wqkv + ((nt & 1) ? 1024 + c * 256 : c * 128) + seg * 64;
    wbase += (lane & 3) * 16 + (lw * 4 + kl) * wstride;
#define G_ROT (2 * (nt + 2 * mt))
    const int rot = G_ROT;
    auto issue = [&](int t) {
      char* sb = lds + (t % 5) * G_SL;
      const unsigned kt = (t + rot) & 15;
#pragma unroll
      for (int i = 0; i < 2; ++i) {
        dma16(xrs, sb + (lw + 4 * i) * 1024, va[i], kt * 64);
        __builtin_amdgcn_global_load_lds((const unsigned*)(wbase + (size_t)((kt * 32 + i * 16) * wstride)),
                                         (__attribute__((address_space(3))) unsigned*)(sb + G_W0 + (lw + 4 * i) * 1024), 16, 0, 0);
      }
    };
    STAMP(2);
    static_for<g_last(0) + 1>([&](auto T) { issue(T); });
    STAMP(3);
    static_for<G_NS>([&](auto T) {
      constexpr int t = T;
      WAIT_V(4 * (g_last(t) - t));
      if constexpr (t == 0) PROBE_AT(6);
      if constexpr (t < 12) STAMP(6 + t);
      BARRIER_NOWAIT();
      static_for<g_last(t + 1) - g_last(t)>([&](auto U) { issue(g_last(t) + 1 + U); });
    });
    asm volatile("" ::"v"(cpf));
    BARRIER_NOWAIT();
    BARRIER_NOWAIT();
    if (lw == 1) {
      const unsigned l = fresh_lane();
      const unsigned c = nt >> 1;
      if (nt & 1) flag_post(ws, attn_blk(8 * (c & 3) + (l >> 3), l & 7), (c >> 2) * 16 + mt, true);
      else {
        const unsigned grp = 2 * mt - 1 + (l >> 3);
        flag_post(ws, l < 32 ? mix_blk(grp, l & 7) : attn_blk(4 * c + (l & 3), mt >> 1), l < 32 ? (l < 16 ? 8 : 0) + c : 32 + (mt & 1),
                  l < 32 ? grp < 32u : l < 36);
      }
      flag_wait(ws, b, l < 34 ? l : 0);
    }
    STAMP(41); RSTAMP(42); STAMP_FLUSH(0);
  } else {
    const int wm = wid & 1, wn = (wid >> 1) & 1;
    if (wid == 4) {
      const unsigned l = fresh_lane(), c = nt >> 1;
      const unsigned a0 = !(nt & 1) ? OFF_Q + 4 * c * (S_ * 32) + mt * 128 * 32 : (c < 4 ? OFF_K : OFF_V) + 8 * (c & 3) * KV_STR + mt * 4096;
      const unsigned a1 = !(nt & 1) ? OFF_HB + (8 * c * HB_ROWS + mt * 128 + HB_OFF) * 16 : 0x7f000000u;
      __builtin_amdgcn_raw_buffer_store_b32(0u, mk_rsrc(ws, 16 * MB_), l == 0 ? a0 : l == 1 ? a1 : 0x7f000000u, 0, 16);
    }
    asm volatile("" ::"v"(cpf));
    if (!(nt & 1)) g_compute<0>(lds, wm, wn, mt, nt, ws STAMP_PASS);
    else if (nt < 8) g_compute<1>(lds, wm, wn, mt, nt, ws STAMP_PASS);
    else g_compute<2>(lds, wm, wn, mt, nt, ws STAMP_PASS);
  }
#endif
}
#define ATTN_ASM \
  "v_mov_b32 v36, %[negm]\n\t" \
  "v_mov_b32 v37, %[negm]\n\t" \
  "v_mov_b32 v38, %[negm]\n\t" \
  "v_mov_b32 v39, %[negm]\n\t" \
  "v_mov_b32 v40, %[negm]\n\t" \
  "v_mov_b32 v41, %[negm]\n\t" \
  "v_mov_b32 v42, %[negm]\n\t" \
  "v_mov_b32 v43, %[negm]\n\t" \
  "v_mov_b32 v44, %[negm]\n\t" \
  "v_mov_b32 v45, %[negm]\n\t" \
  "v_mov_b32 v46, %[negm]\n\t" \
  "v_mov_b32 v47, %[negm]\n\t" \
  "v_mov_b32 v48, %[negm]\n\t" \
  "v_mov_b32 v49, %[negm]\n\t" \
  "v_mov_b32 v50, %[negm]\n\t" \
  "v_mov_b32 v51, %[negm]\n\t" \
  "v_mov_b32 v52, 0\n\t" \
  "v_mov_b32 v53, 0\n\t" \
  "v_mov_b32 v54, 0\n\t" \
  "v_mov_b32 v55, 0\n\t" \
  "v_mov_b32 v56, 0\n\t" \
  "v_mov_b32 v57, 0\n\t" \
  "v_mov_b32 v58, 0\n\t" \
  "v_mov_b32 v59, 0\n\t" \
  "v_mov_b32 v60, 0\n\t" \
  "v_mov_b32 v61, 0\n\t" \
  "v_mov_b32 v62, 0\n\t" \
  "v_mov_b32 v63, 0\n\t" \
  "v_mov_b32 v64, 0\n\t" \
  "v_mov_b32 v65, 0\n\t" \
  "v_mov_b32 v66, 0\n\t" \
  "v_mov_b32 v67, 0\n\t" \
  "v_mov_b32 v104, %[ones]\n\t" \
  "v_mov_b32 v108, %[ones]\n\t" \
  "v_mov_b32 v116, 0\n\t" \
  "v_mov_b32 v105, %[ones]\n\t" \
  "v_mov_b32 v109, %[ones]\n\t" \
  "v_mov_b32 v117, 0\n\t" \
  "v_mov_b32 v106, %[ones]\n\t" \
  "v_mov_b32 v110, %[ones]\n\t" \
  "v_mov_b32 v118, 0\n\t" \
  "v_mov_b32 v107, %[ones]\n\t" \
  "v_mov_b32 v111, %[ones]\n\t" \
  "v_mov_b32 v119, 0\n\t" \
  "s_mov_b32 %[scur], 0\n\t" \
  "s_mov_b32 %[snext], 0x4000\n\t" \
  "s_mov_b32 %[sfree], 0x8000\n\t" \
  "s_mov_b32 %[cnt], 8\n\t" \
  "ds_read_b128 v[100:103], %[lk]\n\t" \
  "s_waitcnt lgkmcnt(0)\n\t" \
  "v_mfma_f32_32x32x16_bf16 v[68:83], v[100:103], v[32:35], v[36:51]\n\t" \
  "ds_read_b128 v[100:103], %[lk] offset:1024\n\t" \
  "s_mov_b64 exec, %[vmask]\n\t" \
  "ds_read_b128 v[104:107], %[lv]\n\t" \
  "s_mov_b64 exec, -1\n\t" \
  "s_nop 7\n\t" \
  "1:\n\t" \
  "v_add_u32 v120, %[scur], %[lk]\n\t" \
  "v_add_u32 v121, %[snext], %[lk]\n\t" \
  "v_add_u32 v122, %[scur], %[lv]\n\t" \
  "v_add_u32 v123, %[snext], %[lv]\n\t" \
  "s_cmp_eq_u32 %[cnt], %[hc]\n\t" \
  "s_cbranch_scc0 2f\n\t" \
  "buffer_store_dwordx2 %[tok], %[fvo], %[rf], 0 offen sc1\n\t" \
  "2:\n\t" \
  "s_waitcnt lgkmcnt(1)\n\t" \
  "v_mfma_f32_32x32x16_bf16 v[84:99], v[100:103], v[32:35], v[36:51]\n\t" \
  "ds_read_b128 v[100:103], v120 offset:2048\n\t" \
  "v_mfma_f32_32x32x16_bf16 v[52:67], v[108:111], v[116:119], v[52:67]\n\t" \
  "s_mov_b64 exec, %[vmask]\n\t" \
  "ds_read_b128 v[108:111], v122 offset:512\n\t" \
  "s_mov_b64 exec, -1\n\t" \
  "v_exp_f32 v112, v69\n\t" \
  "v_exp_f32 v68, v68\n\t" \
  "v_exp_f32 v113, v71\n\t" \
  "v_mov_b32_sdwa v112, v68 dst_sel:WORD_0 dst_unused:UNUSED_PRESERVE src0_sel:WORD_1\n\t" \
  "v_exp_f32 v70, v70\n\t" \
  "v_exp_f32 v114, v73\n\t" \
  "v_mov_b32_sdwa v113, v70 dst_sel:WORD_0 dst_unused:UNUSED_PRESERVE src0_sel:WORD_1\n\t" \
  "v_exp_f32 v72, v72\n\t" \
  "v_exp_f32 v115, v75\n\t" \
  "v_mov_b32_sdwa v114, v72 dst_sel:WORD_0 dst_unused:UNUSED_PRESERVE src0_sel:WORD_1\n\t" \
  "v_exp_f32 v74, v74\n\t" \
  "v_exp_f32 v116, v77\n\t" \
  "v_mov_b32_sdwa v115, v74 dst_sel:WORD_0 dst_unused:UNUSED_PRESERVE src0_sel:WORD_1\n\t" \
  "v_exp_f32 v76, v76\n\t" \
  "v_exp_f32 v117, v79\n\t" \
  "s_waitcnt lgkmcnt(2)\n\t" \
  "v_mfma_f32_32x32x16_bf16 v[52:67], v[104:107], v[112:115], v[52:67]\n\t" \
  "s_mov_b64 exec, %[vmask]\n\t" \
  "ds_read_b128 v[104:107], v122 offset:1024\n\t" \
  "s_mov_b64 exec, -1\n\t" \
  "v_mov_b32_sdwa v116, v76 dst_sel:WORD_0 dst_unused:UNUSED_PRESERVE src0_sel:WORD_1\n\t" \
  "v_exp_f32 v78, v78\n\t" \
  "v_exp_f32 v118, v81\n\t" \
  "v_mov_b32_sdwa v117, v78 dst_sel:WORD_0 dst_unused:UNUSED_PRESERVE src0_sel:WORD_1\n\t" \
  "v_exp_f32 v80, v80\n\t" \
  "v_exp_f32 v119, v83\n\t" \
  "v_mov_b32_sdwa v118, v80 dst_sel:WORD_0 dst_unused:UNUSED_PRESERVE src0_sel:WORD_1\n\t" \
  "v_exp_f32 v82, v82\n\t" \
  "s_nop 0\n\t" \
  "v_mov_b32_sdwa v119, v82 dst_sel:WORD_0 dst_unused:UNUSED_PRESERVE src0_sel:WORD_1\n\t" \
  "s_waitcnt lgkmcnt(1)\n\t" \
  "v_mfma_f32_32x32x16_bf16 v[68:83], v[100:103], v[32:35], v[36:51]\n\t" \
  "ds_read_b128 v[100:103], v120 offset:3072\n\t" \
  "v_mfma_f32_32x32x16_bf16 v[52:67], v[108:111], v[116:119], v[52:67]\n\t" \
  "s_mov_b64 exec, %[vmask]\n\t" \
  "ds_read_b128 v[108:111], v122 offset:1536\n\t" \
  "s_mov_b64 exec, -1\n\t" \
  "v_exp_f32 v112, v85\n\t" \
  "v_exp_f32 v84, v84\n\t" \
  "v_exp_f32 v113, v87\n\t" \
  "v_mov_b32_sdwa v112, v84 dst_sel:WORD_0 dst_unused:UNUSED_PRESERVE src0_sel:WORD_1\n\t" \
  "v_exp_f32 v86, v86\n\t" \
  "v_exp_f32 v114, v89\n\t" \
  "v_mov_b32_sdwa v113, v86 dst_sel:WORD_0 dst_unused:UNUSED_PRESERVE src0_sel:WORD_1\n\t" \
  "v_exp_f32 v88, v88\n\t" \
  "v_exp_f32 v115, v91\n\t" \
  "v_mov_b32_sdwa v114, v88 dst_sel:WORD_0 dst_unused:UNUSED_PRESERVE src0_sel:WORD_1\n\t" \
  "v_exp_f32 v90, v90\n\t" \
  "v_exp_f32 v116, v93\n\t" \
  "v_mov_b32_sdwa v115, v90 dst_sel:WORD_0 dst_unused:UNUSED_PRESERVE src0_sel:WORD_1\n\t" \
  "v_exp_f32 v92, v92\n\t" \
  "v_exp_f32 v117, v95\n\t" \
  "s_waitcnt lgkmcnt(2)\n\t" \
  "v_mfma_f32_32x32x16_bf16 v[52:67], v[104:107], v[112:115], v[52:67]\n\t" \
  "s_mov_b64 exec, %[vmask]\n\t" \
  "ds_read_b128 v[104:107], v122 offset:2048\n\t" \
  "s_mov_b64 exec, -1\n\t" \
  "v_mov_b32_sdwa v116, v92 dst_sel:WORD_0 dst_unused:UNUSED_PRESERVE src0_sel:WORD_1\n\t" \
  "v_exp_f32 v94, v94\n\t" \
  "v_exp_f32 v118, v97\n\t" \
  "v_mov_b32_sdwa v117, v94 dst_sel:WORD_0 dst_unused:UNUSED_PRESERVE src0_sel:WORD_1\n\t" \
  "v_exp_f32 v96, v96\n\t" \
  "v_exp_f32 v119, v99\n\t" \
  "v_mov_b32_sdwa v118, v96 dst_sel:WORD_0 dst_unused:UNUSED_PRESERVE src0_sel:WORD_1\n\t" \
  "v_exp_f32 v98, v98\n\t" \
  "s_nop 0\n\t" \
  "v_mov_b32_sdwa v119, v98 dst_sel:WORD_0 dst_unused:UNUSED_PRESERVE src0_sel:WORD_1\n\t" \
  "s_waitcnt lgkmcnt(1)\n\t" \
  "v_mfma_f32_32x32x16_bf16 v[84:99], v[100:103], v[32:35], v[36:51]\n\t" \
  "ds_read_b128 v[100:103], v120 offset:4096\n\t" \
  "v_mfma_f32_32x32x16_bf16 v[52:67], v[108:111], v[116:119], v[52:67]\n\t" \
  "s_mov_b64 exec, %[vmask]\n\t" \
  "ds_read_b128 v[108:111], v122 offset:2560\n\t" \
  "s_mov_b64 exec, -1\n\t" \
  "v_exp_f32 v112, v69\n\t" \
  "v_exp_f32 v68, v68\n\t" \
  "v_exp_f32 v113, v71\n\t" \
  "v_mov_b32_sdwa v112, v68 dst_sel:WORD_0 dst_unused:UNUSED_PRESERVE src0_sel:WORD_1\n\t" \
  "v_exp_f32 v70, v70\n\t" \
  "v_exp_f32 v114, v73\n\t" \
  "v_mov_b32_sdwa v113, v70 dst_sel:WORD_0 dst_unused:UNUSED_PRESERVE src0_sel:WORD_1\n\t" \
  "v_exp_f32 v72, v72\n\t" \
  "v_exp_f32 v115, v75\n\t" \
  "v_mov_b32_sdwa v114, v72 dst_sel:WORD_0 dst_unused:UNUSED_PRESERVE src0_sel:WORD_1\n\t" \
  "v_exp_f32 v74, v74\n\t" \
  "v_exp_f32 v116, v77\n\t" \
  "v_mov_b32_sdwa v115, v74 dst_sel:WORD_0 dst_unused:UNUSED_PRESERVE src0_sel:WORD_1\n\t" \
  "v_exp_f32 v76, v76\n\t" \
  "v_exp_f32 v117, v79\n\t" \
  "s_waitcnt lgkmcnt(2)\n\t" \
  "v_mfma_f32_32x32x16_bf16 v[52:67], v[104:107], v[112:115], v[52:67]\n\t" \
  "s_mov_b64 exec, %[vmask]\n\t" \
  "ds_read_b128 v[104:107], v122 offset:3072\n\t" \
  "s_mov_b64 exec, -1\n\t" \
  "v_mov_b32_sdwa v116, v76 dst_sel:WORD_0 dst_unused:UNUSED_PRESERVE src0_sel:WORD_1\n\t" \
  "v_exp_f32 v78, v78\n\t" \
  "v_exp_f32 v118, v81\n\t" \
  "v_mov_b32_sdwa v117, v78 dst_sel:WORD_0 dst_unused:UNUSED_PRESERVE src0_sel:WORD_1\n\t" \
  "v_exp_f32 v80, v80\n\t" \
  "v_exp_f32 v119, v83\n\t" \
  "v_mov_b32_sdwa v118, v80 dst_sel:WORD_0 dst_unused:UNUSED_PRESERVE src0_sel:WORD_1\n\t" \
  "v_exp_f32 v82, v82\n\t" \
  "s_nop 0\n\t" \
  "v_mov_b32_sdwa v119, v82 dst_sel:WORD_0 dst_unused:UNUSED_PRESERVE src0_sel:WORD_1\n\t" \
  "s_waitcnt lgkmcnt(1)\n\t" \
  "v_mfma_f32_32x32x16_bf16 v[68:83], v[100:103], v[32:35], v[36:51]\n\t" \
  "ds_read_b128 v[100:103], v120 offset:5120\n\t" \
  "v_mfma_f32_32x32x16_bf16 v[52:67], v[108:111], v[116:119], v[52:67]\n\t" \
  "s_mov_b64 exec, %[vmask]\n\t" \
  "ds_read_b128 v[108:111], v122 offset:3584\n\t" \
  "s_mov_b64 exec, -1\n\t" \
  "v_exp_f32 v112, v85\n\t" \
  "v_exp_f32 v84, v84\n\t" \
  "v_exp_f32 v113, v87\n\t" \
  "v_mov_b32_sdwa v112, v84 dst_sel:WORD_0 dst_unused:UNUSED_PRESERVE src0_sel:WORD_1\n\t" \
  "v_exp_f32 v86, v86\n\t" \
  "v_exp_f32 v114, v89\n\t" \
  "v_mov_b32_sdwa v113, v86 dst_sel:WORD_0 dst_unused:UNUSED_PRESERVE src0_sel:WORD_1\n\t" \
  "v_exp_f32 v88, v88\n\t" \
  "v_exp_f32 v115, v91\n\t" \
  "v_mov_b32_sdwa v114, v88 dst_sel:WORD_0 dst_unused:UNUSED_PRESERVE src0_sel:WORD_1\n\t" \
  "v_exp_f32 v90, v90\n\t" \
  "v_exp_f32 v116, v93\n\t" \
  "v_mov_b32_sdwa v115, v90 dst_sel:WORD_0 dst_unused:UNUSED_PRESERVE src0_sel:WORD_1\n\t" \
  "v_exp_f32 v92, v92\n\t" \
  "v_exp_f32 v117, v95\n\t" \
  "s_waitcnt lgkmcnt(2)\n\t" \
  "v_mfma_f32_32x32x16_bf16 v[52:67], v[104:107], v[112:115], v[52:67]\n\t" \
  "s_mov_b64 exec, %[vmask]\n\t" \
  "ds_read_b128 v[104:107], v122 offset:4096\n\t" \
  "s_mov_b64 exec, -1\n\t" \
  "v_mov_b32_sdwa v116, v92 dst_sel:WORD_0 dst_unused:UNUSED_PRESERVE src0_sel:WORD_1\n\t" \
  "v_exp_f32 v94, v94\n\t" \
  "v_exp_f32 v118, v97\n\t" \
  "v_mov_b32_sdwa v117, v94 dst_sel:WORD_0 dst_unused:UNUSED_PRESERVE src0_sel:WORD_1\n\t" \
  "v_exp_f32 v96, v96\n\t" \
  "v_exp_f32 v119, v99\n\t" \
  "v_mov_b32_sdwa v118, v96 dst_sel:WORD_0 dst_unused:UNUSED_PRESERVE src0_sel:WORD_1\n\t" \
  "v_exp_f32 v98, v98\n\t" \
  "s_nop 0\n\t" \
  "v_mov_b32_sdwa v119, v98 dst_sel:WORD_0 dst_unused:UNUSED_PRESERVE src0_sel:WORD_1\n\t" \
  "s_waitcnt lgkmcnt(1)\n\t" \
  "v_mfma_f32_32x32x16_bf16 v[84:99], v[100:103], v[32:35], v[36:51]\n\t" \
  "ds_read_b128 v[100:103], v120 offset:6144\n\t" \
  "v_mfma_f32_32x32x16_bf16 v[52:67], v[108:111], v[116:119], v[52:67]\n\t" \
  "s_mov_b64 exec, %[vmask]\n\t" \
  "ds_read_b128 v[108:111], v122 offset:4608\n\t" \
  "s_mov_b64 exec, -1\n\t" \
  "v_exp_f32 v112, v69\n\t" \
  "v_exp_f32 v68, v68\n\t" \
  "v_exp_f32 v113, v71\n\t" \
  "v_mov_b32_sdwa v112, v68 dst_sel:WORD_0 dst_unused:UNUSED_PRESERVE src0_sel:WORD_1\n\t" \
  "v_exp_f32 v70, v70\n\t" \
  "v_exp_f32 v114, v73\n\t" \
  "v_mov_b32_sdwa v113, v70 dst_sel:WORD_0 dst_unused:UNUSED_PRESERVE src0_sel:WORD_1\n\t" \
  "v_exp_f32 v72, v72\n\t" \
  "v_exp_f32 v115, v75\n\t" \
  "v_mov_b32_sdwa v114, v72 dst_sel:WORD_0 dst_unused:UNUSED_PRESERVE src0_sel:WORD_1\n\t" \
  "v_exp_f32 v74, v74\n\t" \
  "v_exp_f32 v116, v77\n\t" \
  "v_mov_b32_sdwa v115, v74 dst_sel:WORD_0 dst_unused:UNUSED_PRESERVE src0_sel:WORD_1\n\t" \
  "v_exp_f32 v76, v76\n\t" \
  "v_exp_f32 v117, v79\n\t" \
  "s_waitcnt lgkmcnt(2)\n\t" \
  "v_mfma_f32_32x32x16_bf16 v[52:67], v[104:107], v[112:115], v[52:67]\n\t" \
  "s_mov_b64 exec, %[vmask]\n\t" \
  "ds_read_b128 v[104:107], v122 offset:5120\n\t" \
  "s_mov_b64 exec, -1\n\t" \
  "v_mov_b32_sdwa v116, v76 dst_sel:WORD_0 dst_unused:UNUSED_PRESERVE src0_sel:WORD_1\n\t" \
  "v_exp_f32 v78, v78\n\t" \
  "v_exp_f32 v118, v81\n\t" \
  "v_mov_b32_sdwa v117, v78 dst_sel:WORD_0 dst_unused:UNUSED_PRESERVE src0_sel:WORD_1\n\t" \
  "v_exp_f32 v80, v80\n\t" \
  "v_exp_f32 v119, v83\n\t" \
  "v_mov_b32_sdwa v118, v80 dst_sel:WORD_0 dst_unused:UNUSED_PRESERVE src0_sel:WORD_1\n\t" \
  "v_exp_f32 v82, v82\n\t" \
  "s_cmp_eq_u32 %[cnt], 1\n\t" \
  "s_cbranch_scc1 3f\n\t" \
  "s_waitcnt vmcnt(0)\n\t" \
  "s_barrier\n\t" \
  "s_cmp_eq_u32 %[cnt], 2\n\t" \
  "s_cbranch_scc1 3f\n\t" \
  "s_add_u32 m0, %[sfree], %[m0k]\n\t" \
  "s_nop 0\n\t" \
  "buffer_load_dwordx4 %[goff], %[rk], %[ssrc] offen sc1 lds\n\t" \
  "s_add_u32 m0, m0, 0x2000\n\t" \
  "s_nop 0\n\t" \
  "buffer_load_dwordx4 %[goff], %[rv], %[ssrc] offen sc1 lds\n\t" \
  "s_add_u32 %[ssrc], %[ssrc], 0x2000\n\t" \
  "3:\n\t" \
  "v_mov_b32_sdwa v119, v82 dst_sel:WORD_0 dst_unused:UNUSED_PRESERVE src0_sel:WORD_1\n\t" \
  "s_waitcnt lgkmcnt(1)\n\t" \
  "v_mfma_f32_32x32x16_bf16 v[68:83], v[100:103], v[32:35], v[36:51]\n\t" \
  "ds_read_b128 v[100:103], v120 offset:7168\n\t" \
  "v_mfma_f32_32x32x16_bf16 v[52:67], v[108:111], v[116:119], v[52:67]\n\t" \
  "s_mov_b64 exec, %[vmask]\n\t" \
  "ds_read_b128 v[108:111], v122 offset:5632\n\t" \
  "s_mov_b64 exec, -1\n\t" \
  "v_exp_f32 v112, v85\n\t" \
  "v_exp_f32 v84, v84\n\t" \
  "v_exp_f32 v113, v87\n\t" \
  "v_mov_b32_sdwa v112, v84 dst_sel:WORD_0 dst_unused:UNUSED_PRESERVE src0_sel:WORD_1\n\t" \
  "v_exp_f32 v86, v86\n\t" \
  "v_exp_f32 v114, v89\n\t" \
  "v_mov_b32_sdwa v113, v86 dst_sel:WORD_0 dst_unused:UNUSED_PRESERVE src0_sel:WORD_1\n\t" \
  "v_exp_f32 v88, v88\n\t" \
  "v_exp_f32 v115, v91\n\t" \
  "v_mov_b32_sdwa v114, v88 dst_sel:WORD_0 dst_unused:UNUSED_PRESERVE src0_sel:WORD_1\n\t" \
  "v_exp_f32 v90, v90\n\t" \
  "v_exp_f32 v116, v93\n\t" \
  "v_mov_b32_sdwa v115, v90 dst_sel:WORD_0 dst_unused:UNUSED_PRESERVE src0_sel:WORD_1\n\t" \
  "v_exp_f32 v92, v92\n\t" \
  "v_exp_f32 v117, v95\n\t" \
  "s_waitcnt lgkmcnt(2)\n\t" \
  "v_mfma_f32_32x32x16_bf16 v[52:67], v[104:107], v[112:115], v[52:67]\n\t" \
  "s_mov_b64 exec, %[vmask]\n\t" \
  "ds_read_b128 v[104:107], v122 offset:6144\n\t" \
  "s_mov_b64 exec, -1\n\t" \
  "v_mov_b32_sdwa v116, v92 dst_sel:WORD_0 dst_unused:UNUSED_PRESERVE src0_sel:WORD_1\n\t" \
  "v_exp_f32 v94, v94\n\t" \
  "v_exp_f32 v118, v97\n\t" \
  "v_mov_b32_sdwa v117, v94 dst_sel:WORD_0 dst_unused:UNUSED_PRESERVE src0_sel:WORD_1\n\t" \
  "v_exp_f32 v96, v96\n\t" \
  "v_exp_f32 v119, v99\n\t" \
  "v_mov_b32_sdwa v118, v96 dst_sel:WORD_0 dst_unused:UNUSED_PRESERVE src0_sel:WORD_1\n\t" \
  "v_exp_f32 v98, v98\n\t" \
  "s_nop 0\n\t" \
  "v_mov_b32_sdwa v119, v98 dst_sel:WORD_0 dst_unused:UNUSED_PRESERVE src0_sel:WORD_1\n\t" \
  "s_waitcnt lgkmcnt(1)\n\t" \
  "v_mfma_f32_32x32x16_bf16 v[84:99], v[100:103], v[32:35], v[36:51]\n\t" \
  "ds_read_b128 v[100:103], v121 offset:0\n\t" \
  "v_mfma_f32_32x32x16_bf16 v[52:67], v[108:111], v[116:119], v[52:67]\n\t" \
  "s_mov_b64 exec, %[vmask]\n\t" \
  "ds_read_b128 v[108:111], v122 offset:6656\n\t" \
  "s_mov_b64 exec, -1\n\t" \
  "v_exp_f32 v112, v69\n\t" \
  "v_exp_f32 v68, v68\n\t" \
  "v_exp_f32 v113, v71\n\t" \
  "v_mov_b32_sdwa v112, v68 dst_sel:WORD_0 dst_unused:UNUSED_PRESERVE src0_sel:WORD_1\n\t" \
  "v_exp_f32 v70, v70\n\t" \
  "v_exp_f32 v114, v73\n\t" \
  "v_mov_b32_sdwa v113, v70 dst_sel:WORD_0 dst_unused:UNUSED_PRESERVE src0_sel:WORD_1\n\t" \
  "v_exp_f32 v72, v72\n\t" \
  "v_exp_f32 v115, v75\n\t" \
  "v_mov_b32_sdwa v114, v72 dst_sel:WORD_0 dst_unused:UNUSED_PRESERVE src0_sel:WORD_1\n\t" \
  "v_exp_f32 v74, v74\n\t" \
  "v_exp_f32 v116, v77\n\t" \
  "v_mov_b32_sdwa v115, v74 dst_sel:WORD_0 dst_unused:UNUSED_PRESERVE src0_sel:WORD_1\n\t" \
  "v_exp_f32 v76, v76\n\t" \
  "v_exp_f32 v117, v79\n\t" \
  "s_waitcnt lgkmcnt(2)\n\t" \
  "v_mfma_f32_32x32x16_bf16 v[52:67], v[104:107], v[112:115], v[52:67]\n\t" \
  "s_mov_b64 exec, %[vmask]\n\t" \
  "ds_read_b128 v[104:107], v122 offset:7168\n\t" \
  "s_mov_b64 exec, -1\n\t" \
  "v_mov_b32_sdwa v116, v76 dst_sel:WORD_0 dst_unused:UNUSED_PRESERVE src0_sel:WORD_1\n\t" \
  "v_exp_f32 v78, v78\n\t" \
  "v_exp_f32 v118, v81\n\t" \
  "v_mov_b32_sdwa v117, v78 dst_sel:WORD_0 dst_unused:UNUSED_PRESERVE src0_sel:WORD_1\n\t" \
  "v_exp_f32 v80, v80\n\t" \
  "v_exp_f32 v119, v83\n\t" \
  "v_mov_b32_sdwa v118, v80 dst_sel:WORD_0 dst_unused:UNUSED_PRESERVE src0_sel:WORD_1\n\t" \
  "v_exp_f32 v82, v82\n\t" \
  "s_nop 0\n\t" \
  "v_mov_b32_sdwa v119, v82 dst_sel:WORD_0 dst_unused:UNUSED_PRESERVE src0_sel:WORD_1\n\t" \
  "s_waitcnt lgkmcnt(1)\n\t" \
  "v_mfma_f32_32x32x16_bf16 v[68:83], v[100:103], v[32:35], v[36:51]\n\t" \
  "ds_read_b128 v[100:103], v121 offset:1024\n\t" \
  "v_mfma_f32_32x32x16_bf16 v[52:67], v[108:111], v[116:119], v[52:67]\n\t" \
  "s_mov_b64 exec, %[vmask]\n\t" \
  "ds_read_b128 v[108:111], v122 offset:7680\n\t" \
  "s_mov_b64 exec, -1\n\t" \
  "v_exp_f32 v112, v85\n\t" \
  "v_exp_f32 v84, v84\n\t" \
  "v_exp_f32 v113, v87\n\t" \
  "v_mov_b32_sdwa v112, v84 dst_sel:WORD_0 dst_unused:UNUSED_PRESERVE src0_sel:WORD_1\n\t" \
  "v_exp_f32 v86, v86\n\t" \
  "v_exp_f32 v114, v89\n\t" \
  "v_mov_b32_sdwa v113, v86 dst_sel:WORD_0 dst_unused:UNUSED_PRESERVE src0_sel:WORD_1\n\t" \
  "v_exp_f32 v88, v88\n\t" \
  "v_exp_f32 v115, v91\n\t" \
  "v_mov_b32_sdwa v114, v88 dst_sel:WORD_0 dst_unused:UNUSED_PRESERVE src0_sel:WORD_1\n\t" \
  "v_exp_f32 v90, v90\n\t" \
  "v_exp_f32 v116, v93\n\t" \
  "v_mov_b32_sdwa v115, v90 dst_sel:WORD_0 dst_unused:UNUSED_PRESERVE src0_sel:WORD_1\n\t" \
  "v_exp_f32 v92, v92\n\t" \
  "v_exp_f32 v117, v95\n\t" \
  "s_waitcnt lgkmcnt(2)\n\t" \
  "v_mfma_f32_32x32x16_bf16 v[52:67], v[104:107], v[112:115], v[52:67]\n\t" \
  "s_mov_b64 exec, %[vmask]\n\t" \
  "ds_read_b128 v[104:107], v123 offset:0\n\t" \
  "s_mov_b64 exec, -1\n\t" \
  "v_mov_b32_sdwa v116, v92 dst_sel:WORD_0 dst_unused:UNUSED_PRESERVE src0_sel:WORD_1\n\t" \
  "v_exp_f32 v94, v94\n\t" \
  "v_exp_f32 v118, v97\n\t" \
  "v_mov_b32_sdwa v117, v94 dst_sel:WORD_0 dst_unused:UNUSED_PRESERVE src0_sel:WORD_1\n\t" \
  "v_exp_f32 v96, v96\n\t" \
  "v_exp_f32 v119, v99\n\t" \
  "v_mov_b32_sdwa v118, v96 dst_sel:WORD_0 dst_unused:UNUSED_PRESERVE src0_sel:WORD_1\n\t" \
  "v_exp_f32 v98, v98\n\t" \
  "s_nop 0\n\t" \
  "v_mov_b32_sdwa v119, v98 dst_sel:WORD_0 dst_unused:UNUSED_PRESERVE src0_sel:WORD_1\n\t" \
  "s_mov_b32 %[stmp], %[scur]\n\t" \
  "s_mov_b32 %[scur], %[snext]\n\t" \
  "s_mov_b32 %[snext], %[sfree]\n\t" \
  "s_mov_b32 %[sfree], %[stmp]\n\t" \
  "s_sub_u32 %[cnt], %[cnt], 1\n\t" \
  "s_cmp_lg_u32 %[cnt], 0\n\t" \
  "s_cbranch_scc1 1b\n\t" \
  "s_waitcnt lgkmcnt(0)\n\t" \
  "v_mfma_f32_32x32x16_bf16 v[52:67], v[108:111], v[116:119], v[52:67]\n\t" \
  "s_nop 15\n\t"
#define ATTN_CLOBBERS "v36", "v37", "v38", "v39", "v40", "v41", "v42", "v43", "v44", "v45", "v46", "v47", "v48", "v49", "v50", "v51", "v68", "v69", "v70", "v71", "v72", "v73", "v74", "v75", "v76", "v77", "v78", "v79", "v80", "v81", "v82", "v83", "v84", "v85", "v86", "v87", "v88", "v89", "v90", "v91", "v92", "v93", "v94", "v95", "v96", "v97", "v98", "v99", "v100", "v101", "v102", "v103", "v104", "v105", "v106", "v107", "v108", "v109", "v110", "v111", "v112", "v113", "v114", "v115", "v116", "v117", "v118", "v119", "v120", "v121", "v122", "v123", "m0", "scc", "memory"

constexpr int AT_SLOT = 16384;
DEV char* mk_ptr_u(char* p) {
  const unsigned long long a = (unsigned long long)p;
  const unsigned lo = __builtin_amdgcn_readfirstlane((unsigned)a), hi = __builtin_amdgcn_readfirstlane((unsigned)(a >> 32));
  return (char*)(__attribute__((address_space(1))) char*)(((unsigned long long)hi << 32) | lo);
}
#define K2_APRIO 1
#define K2_APRIO0 3
DEV void attn_role(char* lds, int a, char* ws) {
#if defined(__HIP_DEVICE_COMPILE__)
  STAMP_DECL
  RSTAMP(0); STAMP(1);
  const int head = (a & 7) * 4 + (a >> 6), qb = (a >> 3) & 7;
  const int tid = threadIdx.x, lane = fresh_lane(), l31 = lane & 31, h = lane >> 5;
  const int wid = __builtin_amdgcn_readfirstlane(tid >> 6);
  auto krs = mk_rsrc_u(ws + OFF_K + (size_t)head * KV_STR, S_ * 32);
  auto vrs = mk_rsrc_u(ws + OFF_V + (size_t)head * KV_STR, S_ * 32);
  const int q = qb * 256 + wid * 32 + l31;
  bf16x8 qf;
  {
    const char* qp = ws + OFF_Q + (size_t)head * (S_ * 32) + q * 32 + h * 16;
    asm volatile("global_load_dwordx4 %0, %1, off sc1" : "={v[32:35]}"(qf) : "v"(qp) : "memory");
  }
  auto issue = [&](int c, int slot) {
    char* sb = lds + slot * AT_SLOT + wid * 1024;
    dma16c(krs, sb, lane * 16, c * 8192 + wid * 1024);
    dma16c(vrs, sb + 8192, lane * 16, c * 8192 + wid * 1024);
  };
  const unsigned ldsb = (unsigned)(size_t)lds;
  const unsigned lk = ldsb + lane * 16;
  const unsigned lv = ldsb + 8192 + h * 256 + (l31 & 15) * 16;
  const unsigned goff = lane * 16;
  const unsigned m0k = ldsb + wid * 1024;
  int* flag = (int*)(lds + 3 * AT_SLOT);
  float mref = 0.f;
  bool stored = false;
  f32x16 o;
  if (tid == 0) *flag = 0;
  for (int attempt = 0; attempt < 2; ++attempt) {
    if (attempt == 1) {
      float mx = -__builtin_inff();
      for (int c = 0; c < 8; ++c) {
        issue(c, 0);
        WAIT_V(0); RAW_BARRIER();
        for (int t = 0; t < 8; ++t) {
          bf16x8 kf = *(const bf16x8*)(lds + t * 1024 + lane * 16);
          f32x16 z = {};
          f32x16 s = mfma32(kf, qf, z);
#pragma unroll
          for (int r = 0; r < 16; ++r) mx = fmaxf(mx, s[r]);
        }
        RAW_BARRIER();
      }
      mx = fmaxf(mx, __shfl_xor(mx, 32));
      mref = mx;
    }
    STAMP(2);
    issue(0, 0);
    issue(1, 1);
    STAMP(3);
    asm volatile("s_waitcnt vmcnt(2)" : "+{v[32:35]}"(qf) : : "memory");
    RAW_BARRIER();
    STAMP(4);
    if (attempt == 0) {
      bf16x8 kf = *(const bf16x8*)(lds + lane * 16);
      f32x16 z = {};
      f32x16 s = mfma32(kf, qf, z);
      float mx = s[0];
#pragma unroll
      for (int r = 1; r < 16; ++r) mx = fmaxf(mx, s[r]);
      mx = fmaxf(mx, __shfl_xor(mx, 32));
      mref = mx;
    }
    STAMP(5);
    if (attempt == 0) PROBE_AT(2);
    __builtin_amdgcn_s_setprio(K2_APRIO);
    {
      float negm = -mref;
      unsigned scur, snext, sfree, stmp, cnt, ssrc = 2 * 8192 + wid * 1024;
      unsigned long long vmask = 0x0000FFFF0000FFFFull;
      const unsigned ones = (l31 == 16 || l31 == 20) ? 0x3f803f80u : 0u;
      const u32x2 tok = launch_token();
      const unsigned fvo = lane < 32 ? mix_blk(4 * qb + ((lane >> 3) & 3), lane & 7) * 1024 + (64 + head) * 8 : 0x7f000000u;
      const unsigned hc = wid == 0 ? A_HINTCNT : 99;
      auto frs = flag_rsrc(mk_ptr_u(ws));
      asm volatile(ATTN_ASM
                   : "={v[52:67]}"(o), [scur] "=&s"(scur), [snext] "=&s"(snext), [sfree] "=&s"(sfree), [stmp] "=&s"(stmp),
                     [cnt] "=&s"(cnt), [ssrc] "+s"(ssrc)
                   : "{v[32:35]}"(qf), [negm] "v"(negm), [lk] "v"(lk), [lv] "v"(lv), [goff] "v"(goff), [rk] "s"(krs), [rv] "s"(vrs),
                     [m0k] "s"(m0k), [vmask] "s"(vmask), [ones] "v"(ones), [tok] "v"(tok), [fvo] "v"(fvo), [hc] "s"(hc), [rf] "s"(frs)
                   : ATTN_CLOBBERS);
    }
    STAMP(40);
    bool bad = !(o[8] < 1.0e30f);
#pragma unroll
    for (int r = 0; r < 8; ++r) bad |= !(fabsf(o[r]) < 3.0e38f);
    const bool wbad = attempt == 0 && __any(bad);
    if (wbad && lane == 0) *flag = 1;
    if (!wbad && !stored) {
      stored = true;
      float inv = 1.0f / o[8];
      u32x4 ov = widen(pk4(o[0] * inv, o[1] * inv, o[2] * inv, o[3] * inv), pk4(o[4] * inv, o[5] * inv, o[6] * inv, o[7] * inv));
#pragma unroll
      for (int r = 0; r < 4; ++r) ov[r] = min(ov[r], 0xfffffffeu);
      const unsigned l2 = fresh_lane();
      auto ors = mk_rsrc(ws + OFF_O, 64 * S_ * 16);
      __builtin_amdgcn_raw_buffer_store_b128(ov, ors, ((l2 >> 5) * S_ + (l2 & 31) + 2 * head * S_ + qb * 256 + wid * 32) * 16, 0, 16);
    }
    RAW_BARRIER();
    if (attempt == 1 || *flag == 0) break;
  }
  PROBE_AT(3);
  STAMP(41); RSTAMP(42); HWSTAMP(); STAMP_FLUSH(0); STAMP_FLUSH(7);
#endif
}

#define K2_XBAR 48
#define K2_EARLY 3
namespace bsched {
constexpr int NG = 54;
constexpr int pos(int g) {
  if (g < 8) return 8 * g;
  if (g < 24) return 12 * ((g - 8) % 6);
  int u = (g - 24) % 10;
  return u == 0 ? 72 : u == 1 ? 48 : u == 2 ? 56 : u == 3 ? 64 : 8 * (u - 4);
}
constexpr int npw(int g) { return g < 8 ? 2 : g < 24 ? 3 : g == 45 ? 1 : 2; }
constexpr int iss(int g) {
  if (g < 8) return -1;
  if (g < 24) { int t = g - 8; return t == 0 ? 3 : t == 1 ? 4 : t == 2 ? 6 : t == 3 ? 7 : t == 4 ? 8 : t == 5 ? 9 : t + 6; }
  int u = g - 24;
  return u == 0 ? 9 : u == 1 ? 22 : u == 2 ? 23 : u == 3 ? 23 : u == 4 ? 24 : u == 5 ? 25 : u == 6 ? 25 : u <= 9 ? 26 : u + 19;
}
constexpr int pub(int g) { return g < 8 ? g : g < 24 ? g + 2 : g < 46 ? g + 3 : g < 52 ? 49 : 50; }
constexpr int pubbed(int bi) { return bi < 8 ? bi : (bi >= 10 && bi <= 25) ? bi - 2 : (bi >= 27 && bi <= 48) ? bi - 3 : bi == 49 ? 51 : bi == 50 ? 53 : -1; }
constexpr bool before(int a, int b) { return iss(a) != iss(b) ? iss(a) < iss(b) : a < b; }
constexpr int issued(int b0, int b1) {
  int n = 0;
  for (int o = 0; o < NG; ++o) if (iss(o) >= b0 && iss(o) < b1) n += npw(o);
  return n;
}
constexpr int waitcnt(int g) {
  int n = 0;
  for (int o = 0; o < NG; ++o) if (o != g && before(g, o) && iss(o) < pub(g)) n += npw(o);
  return n;
}
}

#define B_LDELAY 200
DEV void b_loader(char* lds, int lw, int lane, int grp, int j, int c0, const u16* w2, const u16* w3, const u16* w4, const u16* wo, char* ws) {
#if defined(__HIP_DEVICE_COMPILE__)
  using namespace bsched;
  auto r2 = mk_rsrc(w2, D_ * D_ * 2);
  auto r3 = mk_rsrc(w3, D_ * F_ * 2);
  auto r4 = mk_rsrc(w4, F_ * D_ * 2);
  auto r5 = mk_rsrc(wo, D_ * D_ * 2);
  unsigned va[2], vb[3];
#pragma unroll
  for (int i = 0; i < 2; ++i) {
    unsigned bb = (lw + 4 * i) * 1024 + lane * 16, row = bb >> 7, cp = (bb >> 4) & 7;
    va[i] = row * 1024 + j * 128 + ((cp ^ (((row >> 1) & 1) << 2)) << 4);
  }
#pragma unroll
  for (int i = 0; i < 3; ++i) {
    unsigned bb = (lw + 4 * i) * 1024 + lane * 16, row = bb / 384, cp = (bb % 384) >> 4;
    vb[i] = row * (F_ * 2) + c0 * 2 + ((cp ^ (((row >> 1) & 1) << 2)) << 4);
  }
  auto issue = [&](auto G) {
    constexpr int g = G;
    char* sb = lds + pos(g) * 1024 + lw * 1024;
    if constexpr (g < 8) { dma16(r2, sb, va[0], g * 65536); dma16(r2, sb + 4096, va[1], g * 65536); }
    else if constexpr (g < 24) {
#pragma unroll
      for (int i = 0; i < 3; ++i) dma16(r3, sb + i * 4096, vb[i], (g - 8) * 32 * (F_ * 2));
    } else if constexpr (g < 46) {
      dma16(r4, sb, va[0], (g - 24) * 65536);
      if constexpr (g != 45) dma16(r4, sb + 4096, va[1], (g - 24) * 65536);
    } else {
      dma16(r5, sb, va[0], (g - 46) * 65536);
      dma16(r5, sb + 4096, va[1], (g - 46) * 65536);
    }
  };
  sleep_n<B_LDELAY>();
  static_for<NG>([&](auto G) { if constexpr (iss(G) == -1) issue(G); });
  BARRIER_NOWAIT();
  BARRIER_NOWAIT();
  u32x2 early = {0, 0};
  const int b = 256 + (grp >> 2) + j * 8 + (grp & 3) * 64;
  static_for<52>([&](auto BI) {
    constexpr int bi = BI;
    constexpr int pg = pubbed(bi);
    if constexpr (bi == 10 || bi == 27) {
      if (lw == 0) {
        flag_post(ws, mix_blk(grp, lane & 7), (bi == 10 ? 32 : 40) + j, lane < 8);
        flag_wait(ws, b, (bi == 10 ? 32 : 40) + (lane & 7));
      }
    }
    if constexpr (bi == K2_XBAR) {
      if (lw == 0) {
        asm volatile("s_waitcnt vmcnt(%1)" : "+v"(early) : "n"(issued(K2_XBAR - K2_EARLY + 1, K2_XBAR)) : "memory");
        if (!flag_ok(early)) flag_wait(ws, b, 64 + (lane & 31));
      }
    }
    if constexpr (pg >= 0) WAIT_V(waitcnt(pg));
    BARRIER_NOWAIT();
    static_for<NG>([&](auto G) { if constexpr (iss(G) == bi) issue(G); });
    if constexpr (bi == K2_XBAR - K2_EARLY) {
      if (lw == 0) asm volatile("global_load_dwordx2 %0, %1, off sc1" : "=v"(early) : "v"(ws + OFF_FL + b * 1024 + (64 + (lane & 31)) * 8) : "memory");
    }
  });
#endif
}

#define B_SDELAY 60
#define B_PDELAY 100
#define X_XWARM 0
#define X_XDELAY 1
#define CODEPF0 8
#define CODEPF_BYTES 52480
DEV void b_prologue(char* lds, int wid, int b, int grp, const u16* w2, const u16* w3, const u16* w4, const u16* wo, char* ws, u16* out, int j, int c0, int ntile) {
#if defined(__HIP_DEVICE_COMPILE__)
  const int b2 = b - 256, u = b2 - 1;
  const unsigned lane = fresh_lane();
  sleep_n<B_SDELAY>();
  const void* mp; unsigned mbytes, mv, mstride;
  const bool sums = u >= 0 && u < 88;
  if (sums) {
    const int cg = u % 11, rg = u / 11;
    mp = w3; mbytes = D_ * F_ * 2; mstride = 4 * F_ * 2;
    mv = (rg * 64 + wid * 16 + (lane >> 4)) * (F_ * 2) + cg * 256 + (lane & 15) * 16;
  } else {
    const int it = u - 88;
    mstride = 0;
    mv = wid * 8192 + lane * 128;
    if (it < 16) { mp = w2; mbytes = D_ * D_ * 2; mv += it * 32768; }
    else if (it < 32) { mp = wo; mbytes = D_ * D_ * 2; mv += (it - 16) * 32768; }
    else { mp = w4; mbytes = D_ * F_ * 2; mv += (it - 32) * 32768; }
  }
  auto mrs = mk_rsrc(mp, mbytes);
  u32x4 md[4];
  md[0] = __builtin_amdgcn_raw_buffer_load_b128(mrs, mv, 0, 0);
  if (!sums) mv = 0x7f000000u;
#pragma unroll
  for (int i = 1; i < 4; ++i) md[i] = __builtin_amdgcn_raw_buffer_load_b128(mrs, mv, i * mstride, 0);
  {
    const unsigned m0 = grp * 64, row = m0 + (wid & 1) * 32, col0 = j * 64 + (wid >> 1) * 32;
    __builtin_amdgcn_raw_buffer_store_b32(0u, mk_rsrc(out, S_ * D_ * 2), lane == 0 ? (row * D_ + col0) * 2 : 0x7f000000u, 0, 0);
    const unsigned a0 = OFF_X1 + ((col0 / 8) * S_ + row) * 16, a1 = OFF_H2 + ((c0 / 8) * S_ + m0) * 16, a2 = OFF_H2 + ((c0 / 8 + ntile * 4 - 1) * S_ + m0) * 16;
    __builtin_amdgcn_raw_buffer_store_b32(0u, mk_rsrc(ws, 20 * MB_), lane == 0 ? a0 : lane == 1 ? a1 : lane == 2 ? a2 : 0x7f000000u, 0, 16);
  }
  unsigned cpf = 0;
  float* p3 = (float*)(lds + 65536);
  if (sums) {
    float cs[8];
#pragma unroll
    for (int j = 0; j < 8; ++j) cs[j] = 0.f;
#pragma unroll
    for (int i = 0; i < 4; ++i)
#pragma unroll
      for (int j = 0; j < 4; ++j) { cs[2 * j] += bf_lo(md[i][j]); cs[2 * j + 1] += bf_hi(md[i][j]); }
#pragma unroll
    for (int j = 0; j < 8; ++j) { cs[j] += __shfl_xor(cs[j], 16); cs[j] = xhalf_sum(cs[j]); }
    if (lane < 16) {
      *(f32x4*)(p3 + wid * 128 + lane * 8) = f32x4{cs[0], cs[1], cs[2], cs[3]};
      *(f32x4*)(p3 + wid * 128 + lane * 8 + 4) = f32x4{cs[4], cs[5], cs[6], cs[7]};
    }
  } else {
#pragma unroll
    for (int i = 0; i < 4; ++i) asm volatile("" ::"v"(md[i]));
  }
  asm volatile("" ::"v"(cpf));
  WAIT_V0();
  RAW_BARRIER();
  if (wid == 0) {
    if (sums) {
      f32x2 t = {0.f, 0.f};
#pragma unroll
      for (int w = 0; w < 4; ++w) t += *(const f32x2*)(p3 + w * 128 + lane * 2);
      __builtin_amdgcn_raw_buffer_store_b64(__builtin_bit_cast(u32x2, t), mk_rsrc(ws + OFF_CS3, CS3_P * CS3_LD * 4), ((u / 11) * CS3_LD + (u % 11) * 128 + lane * 2) * 4, 0, 16);
    }
    if (sums) {
      const unsigned cg = u % 11, rg = u / 11;
      const unsigned ja = cg < 2 ? 0 : cg < 3 ? 1 : cg < 5 ? 2 : cg < 6 ? 3 : cg < 7 ? 4 : cg < 9 ? 5 : cg < 10 ? 6 : 7;
      const unsigned jj = ja + (lane >> 5);
      const unsigned f = (0x98754310u >> (4 * (jj & 7))) & 15;
      WAIT_V0();
      flag_post(ws, mix_blk(lane & 31, jj & 7), 16 + rg * 2 + (cg - f), jj < 8 && cg - f < 2u);
    }
    sleep_n<B_PDELAY>();
    const unsigned w = lane & 31;
    flag_wait(ws, b, grp == 0 && w < 8 ? w + 8 : grp == 31 && w >= 8 && w < 16 ? w - 8 : w);
  }
  RAW_BARRIER();
#endif
}

#define K2_BDELAY 0
#define K2_PM1 0
#define K2_PM2 0
#define K2_PM3 0
#define K2_PE1 0
#define K2_PE2 2
#define K2_PE3 2
typedef __attribute__((ext_vector_type(16))) unsigned u32x16;
#define K2_WPRE (K2_WD - 1)
#define OLOAD_ASM \
  "s_mov_b32 %[st], 0\n\t" \
  "buffer_load_dwordx4 v[64:67], %[va], %[rs], %[st] offen sc1\n\t" \
  "s_mov_b32 %[st], 131072\n\t" \
  "buffer_load_dwordx4 v[68:71], %[va], %[rs], %[st] offen sc1\n\t" \
  "s_mov_b32 %[st], 262144\n\t" \
  "buffer_load_dwordx4 v[72:75], %[va], %[rs], %[st] offen sc1\n\t" \
  "s_mov_b32 %[st], 393216\n\t" \
  "buffer_load_dwordx4 v[76:79], %[va], %[rs], %[st] offen sc1\n\t" \
  "s_mov_b32 %[st], 524288\n\t" \
  "buffer_load_dwordx4 v[80:83], %[va], %[rs], %[st] offen sc1\n\t" \
  "s_mov_b32 %[st], 655360\n\t" \
  "buffer_load_dwordx4 v[84:87], %[va], %[rs], %[st] offen sc1\n\t" \
  "s_mov_b32 %[st], 786432\n\t" \
  "buffer_load_dwordx4 v[88:91], %[va], %[rs], %[st] offen sc1\n\t" \
  "s_mov_b32 %[st], 917504\n\t" \
  "buffer_load_dwordx4 v[92:95], %[va], %[rs], %[st] offen sc1\n\t" \
  "s_mov_b32 %[st], 1048576\n\t" \
  "buffer_load_dwordx4 v[96:99], %[va], %[rs], %[st] offen sc1\n\t" \
  "s_mov_b32 %[st], 1179648\n\t" \
  "buffer_load_dwordx4 v[100:103], %[va], %[rs], %[st] offen sc1\n\t" \
  "s_mov_b32 %[st], 1310720\n\t" \
  "buffer_load_dwordx4 v[104:107], %[va], %[rs], %[st] offen sc1\n\t" \
  "s_mov_b32 %[st], 1441792\n\t" \
  "buffer_load_dwordx4 v[108:111], %[va], %[rs], %[st] offen sc1\n\t" \
  "s_mov_b32 %[st], 1572864\n\t" \
  "buffer_load_dwordx4 v[112:115], %[va], %[rs], %[st] offen sc1\n\t" \
  "s_mov_b32 %[st], 1703936\n\t" \
  "buffer_load_dwordx4 v[116:119], %[va], %[rs], %[st] offen sc1\n\t" \
  "s_mov_b32 %[st], 1835008\n\t" \
  "buffer_load_dwordx4 v[120:123], %[va], %[rs], %[st] offen sc1\n\t" \
  "s_mov_b32 %[st], 1966080\n\t" \
  "buffer_load_dwordx4 v[124:127], %[va], %[rs], %[st] offen sc1\n\t"
#define OROWS_ASM0 \
  "s_waitcnt vmcnt(12)\n\t" \
  "v_max3_u32 %[t], v64, v65, v66\n\t" \
  "v_max3_u32 %[u], v67, v68, v69\n\t" \
  "v_max3_u32 %[t], %[t], v70, v71\n\t" \
  "v_max3_u32 %[u], %[u], v72, v73\n\t" \
  "v_max3_u32 %[t], %[t], v74, v75\n\t" \
  "v_max3_u32 %[u], %[u], v76, v77\n\t" \
  "v_max3_u32 %[t], %[t], v78, v79\n\t" \
  "v_max_u32 %[t], %[t], %[u]\n\t" \
  "v_cmp_eq_u32 vcc, -1, %[t]\n\t" \
  "s_cbranch_vccz 9f\n\t" \
  "s_setprio %[plo]\n\t" \
  "s_mov_b32 %[ct], %[pmax]\n\t" \
  "1:\n\t" \
  "v_max3_u32 %[t], v64, v65, v66\n\t" \
  "v_max_u32 %[t], %[t], v67\n\t" \
  "v_cmp_eq_u32 vcc, -1, %[t]\n\t" \
  "s_mov_b32 %[st], 0\n\t" \
  "s_cbranch_vccz 10f\n\t" \
  "buffer_load_dwordx4 v[64:67], %[va], %[rs], %[st] offen sc1\n\t" \
  "10:\n\t" \
  "v_max3_u32 %[t], v68, v69, v70\n\t" \
  "v_max_u32 %[t], %[t], v71\n\t" \
  "v_cmp_eq_u32 vcc, -1, %[t]\n\t" \
  "s_mov_b32 %[st], 131072\n\t" \
  "s_cbranch_vccz 11f\n\t" \
  "buffer_load_dwordx4 v[68:71], %[va], %[rs], %[st] offen sc1\n\t" \
  "11:\n\t" \
  "v_max3_u32 %[t], v72, v73, v74\n\t" \
  "v_max_u32 %[t], %[t], v75\n\t" \
  "v_cmp_eq_u32 vcc, -1, %[t]\n\t" \
  "s_mov_b32 %[st], 262144\n\t" \
  "s_cbranch_vccz 12f\n\t" \
  "buffer_load_dwordx4 v[72:75], %[va], %[rs], %[st] offen sc1\n\t" \
  "12:\n\t" \
  "v_max3_u32 %[t], v76, v77, v78\n\t" \
  "v_max_u32 %[t], %[t], v79\n\t" \
  "v_cmp_eq_u32 vcc, -1, %[t]\n\t" \
  "s_mov_b32 %[st], 393216\n\t" \
  "s_cbranch_vccz 13f\n\t" \
  "buffer_load_dwordx4 v[76:79], %[va], %[rs], %[st] offen sc1\n\t" \
  "13:\n\t" \
  "v_max3_u32 %[t], v80, v81, v82\n\t" \
  "v_max_u32 %[t], %[t], v83\n\t" \
  "v_cmp_eq_u32 vcc, -1, %[t]\n\t" \
  "s_mov_b32 %[st], 524288\n\t" \
  "s_cbranch_vccz 14f\n\t" \
  "buffer_load_dwordx4 v[80:83], %[va], %[rs], %[st] offen sc1\n\t" \
  "14:\n\t" \
  "v_max3_u32 %[t], v84, v85, v86\n\t" \
  "v_max_u32 %[t], %[t], v87\n\t" \
  "v_cmp_eq_u32 vcc, -1, %[t]\n\t" \
  "s_mov_b32 %[st], 655360\n\t" \
  "s_cbranch_vccz 15f\n\t" \
  "buffer_load_dwordx4 v[84:87], %[va], %[rs], %[st] offen sc1\n\t" \
  "15:\n\t" \
  "v_max3_u32 %[t], v88, v89, v90\n\t" \
  "v_max_u32 %[t], %[t], v91\n\t" \
  "v_cmp_eq_u32 vcc, -1, %[t]\n\t" \
  "s_mov_b32 %[st], 786432\n\t" \
  "s_cbranch_vccz 16f\n\t" \
  "buffer_load_dwordx4 v[88:91], %[va], %[rs], %[st] offen sc1\n\t" \
  "16:\n\t" \
  "v_max3_u32 %[t], v92, v93, v94\n\t" \
  "v_max_u32 %[t], %[t], v95\n\t" \
  "v_cmp_eq_u32 vcc, -1, %[t]\n\t" \
  "s_mov_b32 %[st], 917504\n\t" \
  "s_cbranch_vccz 17f\n\t" \
  "buffer_load_dwordx4 v[92:95], %[va], %[rs], %[st] offen sc1\n\t" \
  "17:\n\t" \
  "v_max3_u32 %[t], v96, v97, v98\n\t" \
  "v_max_u32 %[t], %[t], v99\n\t" \
  "v_cmp_eq_u32 vcc, -1, %[t]\n\t" \
  "s_mov_b32 %[st], 1048576\n\t" \
  "s_cbranch_vccz 18f\n\t" \
  "buffer_load_dwordx4 v[96:99], %[va], %[rs], %[st] offen sc1\n\t" \
  "18:\n\t" \
  "v_max3_u32 %[t], v100, v101, v102\n\t" \
  "v_max_u32 %[t], %[t], v103\n\t" \
  "v_cmp_eq_u32 vcc, -1, %[t]\n\t" \
  "s_mov_b32 %[st], 1179648\n\t" \
  "s_cbranch_vccz 19f\n\t" \
  "buffer_load_dwordx4 v[100:103], %[va], %[rs], %[st] offen sc1\n\t" \
  "19:\n\t" \
  "v_max3_u32 %[t], v104, v105, v106\n\t" \
  "v_max_u32 %[t], %[t], v107\n\t" \
  "v_cmp_eq_u32 vcc, -1, %[t]\n\t" \
  "s_mov_b32 %[st], 1310720\n\t" \
  "s_cbranch_vccz 20f\n\t" \
  "buffer_load_dwordx4 v[104:107], %[va], %[rs], %[st] offen sc1\n\t" \
  "20:\n\t" \
  "v_max3_u32 %[t], v108, v109, v110\n\t" \
  "v_max_u32 %[t], %[t], v111\n\t" \
  "v_cmp_eq_u32 vcc, -1, %[t]\n\t" \
  "s_mov_b32 %[st], 1441792\n\t" \
  "s_cbranch_vccz 21f\n\t" \
  "buffer_load_dwordx4 v[108:111], %[va], %[rs], %[st] offen sc1\n\t" \
  "21:\n\t" \
  "v_max3_u32 %[t], v112, v113, v114\n\t" \
  "v_max_u32 %[t], %[t], v115\n\t" \
  "v_cmp_eq_u32 vcc, -1, %[t]\n\t" \
  "s_mov_b32 %[st], 1572864\n\t" \
  "s_cbranch_vccz 22f\n\t" \
  "buffer_load_dwordx4 v[112:115], %[va], %[rs], %[st] offen sc1\n\t" \
  "22:\n\t" \
  "v_max3_u32 %[t], v116, v117, v118\n\t" \
  "v_max_u32 %[t], %[t], v119\n\t" \
  "v_cmp_eq_u32 vcc, -1, %[t]\n\t" \
  "s_mov_b32 %[st], 1703936\n\t" \
  "s_cbranch_vccz 23f\n\t" \
  "buffer_load_dwordx4 v[116:119], %[va], %[rs], %[st] offen sc1\n\t" \
  "23:\n\t" \
  "v_max3_u32 %[t], v120, v121, v122\n\t" \
  "v_max_u32 %[t], %[t], v123\n\t" \
  "v_cmp_eq_u32 vcc, -1, %[t]\n\t" \
  "s_mov_b32 %[st], 1835008\n\t" \
  "s_cbranch_vccz 24f\n\t" \
  "buffer_load_dwordx4 v[120:123], %[va], %[rs], %[st] offen sc1\n\t" \
  "24:\n\t" \
  "v_max3_u32 %[t], v124, v125, v126\n\t" \
  "v_max_u32 %[t], %[t], v127\n\t" \
  "v_cmp_eq_u32 vcc, -1, %[t]\n\t" \
  "s_mov_b32 %[st], 1966080\n\t" \
  "s_cbranch_vccz 25f\n\t" \
  "buffer_load_dwordx4 v[124:127], %[va], %[rs], %[st] offen sc1\n\t" \
  "25:\n\t" \
  "s_waitcnt vmcnt(0)\n\t" \
  "v_max3_u32 %[t], v64, v65, v66\n\t" \
  "v_max3_u32 %[u], v67, v68, v69\n\t" \
  "v_max3_u32 %[t], %[t], v70, v71\n\t" \
  "v_max3_u32 %[u], %[u], v72, v73\n\t" \
  "v_max3_u32 %[t], %[t], v74, v75\n\t" \
  "v_max3_u32 %[u], %[u], v76, v77\n\t" \
  "v_max3_u32 %[t], %[t], v78, v79\n\t" \
  "v_max_u32 %[t], %[t], %[u]\n\t" \
  "v_cmp_eq_u32 vcc, -1, %[t]\n\t" \
  "s_cbranch_vccz 8f\n\t" \
  "s_sleep %[osl]\n\t" \
  "s_sub_u32 %[ct], %[ct], 1\n\t" \
  "s_cmp_lg_u32 %[ct], 0\n\t" \
  "s_cbranch_scc1 1b\n\t" \
  "8:\n\t" \
  "s_setprio %[phi]\n\t" \
  "9:\n\t"
#define OROWS_ASM1 \
  "s_waitcnt vmcnt(8)\n\t" \
  "v_max3_u32 %[t], v80, v81, v82\n\t" \
  "v_max3_u32 %[u], v83, v84, v85\n\t" \
  "v_max3_u32 %[t], %[t], v86, v87\n\t" \
  "v_max3_u32 %[u], %[u], v88, v89\n\t" \
  "v_max3_u32 %[t], %[t], v90, v91\n\t" \
  "v_max3_u32 %[u], %[u], v92, v93\n\t" \
  "v_max3_u32 %[t], %[t], v94, v95\n\t" \
  "v_max_u32 %[t], %[t], %[u]\n\t" \
  "v_cmp_eq_u32 vcc, -1, %[t]\n\t" \
  "s_cbranch_vccz 9f\n\t" \
  "s_setprio %[plo]\n\t" \
  "s_mov_b32 %[ct], %[pmax]\n\t" \
  "1:\n\t" \
  "v_max3_u32 %[t], v80, v81, v82\n\t" \
  "v_max_u32 %[t], %[t], v83\n\t" \
  "v_cmp_eq_u32 vcc, -1, %[t]\n\t" \
  "s_mov_b32 %[st], 524288\n\t" \
  "s_cbranch_vccz 14f\n\t" \
  "buffer_load_dwordx4 v[80:83], %[va], %[rs], %[st] offen sc1\n\t" \
  "14:\n\t" \
  "v_max3_u32 %[t], v84, v85, v86\n\t" \
  "v_max_u32 %[t], %[t], v87\n\t" \
  "v_cmp_eq_u32 vcc, -1, %[t]\n\t" \
  "s_mov_b32 %[st], 655360\n\t" \
  "s_cbranch_vccz 15f\n\t" \
  "buffer_load_dwordx4 v[84:87], %[va], %[rs], %[st] offen sc1\n\t" \
  "15:\n\t" \
  "v_max3_u32 %[t], v88, v89, v90\n\t" \
  "v_max_u32 %[t], %[t], v91\n\t" \
  "v_cmp_eq_u32 vcc, -1, %[t]\n\t" \
  "s_mov_b32 %[st], 786432\n\t" \
  "s_cbranch_vccz 16f\n\t" \
  "buffer_load_dwordx4 v[88:91], %[va], %[rs], %[st] offen sc1\n\t" \
  "16:\n\t" \
  "v_max3_u32 %[t], v92, v93, v94\n\t" \
  "v_max_u32 %[t], %[t], v95\n\t" \
  "v_cmp_eq_u32 vcc, -1, %[t]\n\t" \
  "s_mov_b32 %[st], 917504\n\t" \
  "s_cbranch_vccz 17f\n\t" \
  "buffer_load_dwordx4 v[92:95], %[va], %[rs], %[st] offen sc1\n\t" \
  "17:\n\t" \
  "v_max3_u32 %[t], v96, v97, v98\n\t" \
  "v_max_u32 %[t], %[t], v99\n\t" \
  "v_cmp_eq_u32 vcc, -1, %[t]\n\t" \
  "s_mov_b32 %[st], 1048576\n\t" \
  "s_cbranch_vccz 18f\n\t" \
  "buffer_load_dwordx4 v[96:99], %[va], %[rs], %[st] offen sc1\n\t" \
  "18:\n\t" \
  "v_max3_u32 %[t], v100, v101, v102\n\t" \
  "v_max_u32 %[t], %[t], v103\n\t" \
  "v_cmp_eq_u32 vcc, -1, %[t]\n\t" \
  "s_mov_b32 %[st], 1179648\n\t" \
  "s_cbranch_vccz 19f\n\t" \
  "buffer_load_dwordx4 v[100:103], %[va], %[rs], %[st] offen sc1\n\t" \
  "19:\n\t" \
  "v_max3_u32 %[t], v104, v105, v106\n\t" \
  "v_max_u32 %[t], %[t], v107\n\t" \
  "v_cmp_eq_u32 vcc, -1, %[t]\n\t" \
  "s_mov_b32 %[st], 1310720\n\t" \
  "s_cbranch_vccz 20f\n\t" \
  "buffer_load_dwordx4 v[104:107], %[va], %[rs], %[st] offen sc1\n\t" \
  "20:\n\t" \
  "v_max3_u32 %[t], v108, v109, v110\n\t" \
  "v_max_u32 %[t], %[t], v111\n\t" \
  "v_cmp_eq_u32 vcc, -1, %[t]\n\t" \
  "s_mov_b32 %[st], 1441792\n\t" \
  "s_cbranch_vccz 21f\n\t" \
  "buffer_load_dwordx4 v[108:111], %[va], %[rs], %[st] offen sc1\n\t" \
  "21:\n\t" \
  "v_max3_u32 %[t], v112, v113, v114\n\t" \
  "v_max_u32 %[t], %[t], v115\n\t" \
  "v_cmp_eq_u32 vcc, -1, %[t]\n\t" \
  "s_mov_b32 %[st], 1572864\n\t" \
  "s_cbranch_vccz 22f\n\t" \
  "buffer_load_dwordx4 v[112:115], %[va], %[rs], %[st] offen sc1\n\t" \
  "22:\n\t" \
  "v_max3_u32 %[t], v116, v117, v118\n\t" \
  "v_max_u32 %[t], %[t], v119\n\t" \
  "v_cmp_eq_u32 vcc, -1, %[t]\n\t" \
  "s_mov_b32 %[st], 1703936\n\t" \
  "s_cbranch_vccz 23f\n\t" \
  "buffer_load_dwordx4 v[116:119], %[va], %[rs], %[st] offen sc1\n\t" \
  "23:\n\t" \
  "v_max3_u32 %[t], v120, v121, v122\n\t" \
  "v_max_u32 %[t], %[t], v123\n\t" \
  "v_cmp_eq_u32 vcc, -1, %[t]\n\t" \
  "s_mov_b32 %[st], 1835008\n\t" \
  "s_cbranch_vccz 24f\n\t" \
  "buffer_load_dwordx4 v[120:123], %[va], %[rs], %[st] offen sc1\n\t" \
  "24:\n\t" \
  "v_max3_u32 %[t], v124, v125, v126\n\t" \
  "v_max_u32 %[t], %[t], v127\n\t" \
  "v_cmp_eq_u32 vcc, -1, %[t]\n\t" \
  "s_mov_b32 %[st], 1966080\n\t" \
  "s_cbranch_vccz 25f\n\t" \
  "buffer_load_dwordx4 v[124:127], %[va], %[rs], %[st] offen sc1\n\t" \
  "25:\n\t" \
  "s_waitcnt vmcnt(0)\n\t" \
  "v_max3_u32 %[t], v80, v81, v82\n\t" \
  "v_max3_u32 %[u], v83, v84, v85\n\t" \
  "v_max3_u32 %[t], %[t], v86, v87\n\t" \
  "v_max3_u32 %[u], %[u], v88, v89\n\t" \
  "v_max3_u32 %[t], %[t], v90, v91\n\t" \
  "v_max3_u32 %[u], %[u], v92, v93\n\t" \
  "v_max3_u32 %[t], %[t], v94, v95\n\t" \
  "v_max_u32 %[t], %[t], %[u]\n\t" \
  "v_cmp_eq_u32 vcc, -1, %[t]\n\t" \
  "s_cbranch_vccz 8f\n\t" \
  "s_sleep %[osl]\n\t" \
  "s_sub_u32 %[ct], %[ct], 1\n\t" \
  "s_cmp_lg_u32 %[ct], 0\n\t" \
  "s_cbranch_scc1 1b\n\t" \
  "8:\n\t" \
  "s_setprio %[phi]\n\t" \
  "9:\n\t"
#define OROWS_ASM2 \
  "s_waitcnt vmcnt(4)\n\t" \
  "v_max3_u32 %[t], v96, v97, v98\n\t" \
  "v_max3_u32 %[u], v99, v100, v101\n\t" \
  "v_max3_u32 %[t], %[t], v102, v103\n\t" \
  "v_max3_u32 %[u], %[u], v104, v105\n\t" \
  "v_max3_u32 %[t], %[t], v106, v107\n\t" \
  "v_max3_u32 %[u], %[u], v108, v109\n\t" \
  "v_max3_u32 %[t], %[t], v110, v111\n\t" \
  "v_max_u32 %[t], %[t], %[u]\n\t" \
  "v_cmp_eq_u32 vcc, -1, %[t]\n\t" \
  "s_cbranch_vccz 9f\n\t" \
  "s_setprio %[plo]\n\t" \
  "s_mov_b32 %[ct], %[pmax]\n\t" \
  "1:\n\t" \
  "v_max3_u32 %[t], v96, v97, v98\n\t" \
  "v_max_u32 %[t], %[t], v99\n\t" \
  "v_cmp_eq_u32 vcc, -1, %[t]\n\t" \
  "s_mov_b32 %[st], 1048576\n\t" \
  "s_cbranch_vccz 18f\n\t" \
  "buffer_load_dwordx4 v[96:99], %[va], %[rs], %[st] offen sc1\n\t" \
  "18:\n\t" \
  "v_max3_u32 %[t], v100, v101, v102\n\t" \
  "v_max_u32 %[t], %[t], v103\n\t" \
  "v_cmp_eq_u32 vcc, -1, %[t]\n\t" \
  "s_mov_b32 %[st], 1179648\n\t" \
  "s_cbranch_vccz 19f\n\t" \
  "buffer_load_dwordx4 v[100:103], %[va], %[rs], %[st] offen sc1\n\t" \
  "19:\n\t" \
  "v_max3_u32 %[t], v104, v105, v106\n\t" \
  "v_max_u32 %[t], %[t], v107\n\t" \
  "v_cmp_eq_u32 vcc, -1, %[t]\n\t" \
  "s_mov_b32 %[st], 1310720\n\t" \
  "s_cbranch_vccz 20f\n\t" \
  "buffer_load_dwordx4 v[104:107], %[va], %[rs], %[st] offen sc1\n\t" \
  "20:\n\t" \
  "v_max3_u32 %[t], v108, v109, v110\n\t" \
  "v_max_u32 %[t], %[t], v111\n\t" \
  "v_cmp_eq_u32 vcc, -1, %[t]\n\t" \
  "s_mov_b32 %[st], 1441792\n\t" \
  "s_cbranch_vccz 21f\n\t" \
  "buffer_load_dwordx4 v[108:111], %[va], %[rs], %[st] offen sc1\n\t" \
  "21:\n\t" \
  "v_max3_u32 %[t], v112, v113, v114\n\t" \
  "v_max_u32 %[t], %[t], v115\n\t" \
  "v_cmp_eq_u32 vcc, -1, %[t]\n\t" \
  "s_mov_b32 %[st], 1572864\n\t" \
  "s_cbranch_vccz 22f\n\t" \
  "buffer_load_dwordx4 v[112:115], %[va], %[rs], %[st] offen sc1\n\t" \
  "22:\n\t" \
  "v_max3_u32 %[t], v116, v117, v118\n\t" \
  "v_max_u32 %[t], %[t], v119\n\t" \
  "v_cmp_eq_u32 vcc, -1, %[t]\n\t" \
  "s_mov_b32 %[st], 1703936\n\t" \
  "s_cbranch_vccz 23f\n\t" \
  "buffer_load_dwordx4 v[116:119], %[va], %[rs], %[st] offen sc1\n\t" \
  "23:\n\t" \
  "v_max3_u32 %[t], v120, v121, v122\n\t" \
  "v_max_u32 %[t], %[t], v123\n\t" \
  "v_cmp_eq_u32 vcc, -1, %[t]\n\t" \
  "s_mov_b32 %[st], 1835008\n\t" \
  "s_cbranch_vccz 24f\n\t" \
  "buffer_load_dwordx4 v[120:123], %[va], %[rs], %[st] offen sc1\n\t" \
  "24:\n\t" \
  "v_max3_u32 %[t], v124, v125, v126\n\t" \
  "v_max_u32 %[t], %[t], v127\n\t" \
  "v_cmp_eq_u32 vcc, -1, %[t]\n\t" \
  "s_mov_b32 %[st], 1966080\n\t" \
  "s_cbranch_vccz 25f\n\t" \
  "buffer_load_dwordx4 v[124:127], %[va], %[rs], %[st] offen sc1\n\t" \
  "25:\n\t" \
  "s_waitcnt vmcnt(0)\n\t" \
  "v_max3_u32 %[t], v96, v97, v98\n\t" \
  "v_max3_u32 %[u], v99, v100, v101\n\t" \
  "v_max3_u32 %[t], %[t], v102, v103\n\t" \
  "v_max3_u32 %[u], %[u], v104, v105\n\t" \
  "v_max3_u32 %[t], %[t], v106, v107\n\t" \
  "v_max3_u32 %[u], %[u], v108, v109\n\t" \
  "v_max3_u32 %[t], %[t], v110, v111\n\t" \
  "v_max_u32 %[t], %[t], %[u]\n\t" \
  "v_cmp_eq_u32 vcc, -1, %[t]\n\t" \
  "s_cbranch_vccz 8f\n\t" \
  "s_sleep %[osl]\n\t" \
  "s_sub_u32 %[ct], %[ct], 1\n\t" \
  "s_cmp_lg_u32 %[ct], 0\n\t" \
  "s_cbranch_scc1 1b\n\t" \
  "8:\n\t" \
  "s_setprio %[phi]\n\t" \
  "9:\n\t"
#define OROWS_ASM3 \
  "s_waitcnt vmcnt(0)\n\t" \
  "v_max3_u32 %[t], v112, v113, v114\n\t" \
  "v_max3_u32 %[u], v115, v116, v117\n\t" \
  "v_max3_u32 %[t], %[t], v118, v119\n\t" \
  "v_max3_u32 %[u], %[u], v120, v121\n\t" \
  "v_max3_u32 %[t], %[t], v122, v123\n\t" \
  "v_max3_u32 %[u], %[u], v124, v125\n\t" \
  "v_max3_u32 %[t], %[t], v126, v127\n\t" \
  "v_max_u32 %[t], %[t], %[u]\n\t" \
  "v_cmp_eq_u32 vcc, -1, %[t]\n\t" \
  "s_cbranch_vccz 9f\n\t" \
  "s_setprio %[plo]\n\t" \
  "s_mov_b32 %[ct], %[pmax]\n\t" \
  "1:\n\t" \
  "v_max3_u32 %[t], v112, v113, v114\n\t" \
  "v_max_u32 %[t], %[t], v115\n\t" \
  "v_cmp_eq_u32 vcc, -1, %[t]\n\t" \
  "s_mov_b32 %[st], 1572864\n\t" \
  "s_cbranch_vccz 22f\n\t" \
  "buffer_load_dwordx4 v[112:115], %[va], %[rs], %[st] offen sc1\n\t" \
  "22:\n\t" \
  "v_max3_u32 %[t], v116, v117, v118\n\t" \
  "v_max_u32 %[t], %[t], v119\n\t" \
  "v_cmp_eq_u32 vcc, -1, %[t]\n\t" \
  "s_mov_b32 %[st], 1703936\n\t" \
  "s_cbranch_vccz 23f\n\t" \
  "buffer_load_dwordx4 v[116:119], %[va], %[rs], %[st] offen sc1\n\t" \
  "23:\n\t" \
  "v_max3_u32 %[t], v120, v121, v122\n\t" \
  "v_max_u32 %[t], %[t], v123\n\t" \
  "v_cmp_eq_u32 vcc, -1, %[t]\n\t" \
  "s_mov_b32 %[st], 1835008\n\t" \
  "s_cbranch_vccz 24f\n\t" \
  "buffer_load_dwordx4 v[120:123], %[va], %[rs], %[st] offen sc1\n\t" \
  "24:\n\t" \
  "v_max3_u32 %[t], v124, v125, v126\n\t" \
  "v_max_u32 %[t], %[t], v127\n\t" \
  "v_cmp_eq_u32 vcc, -1, %[t]\n\t" \
  "s_mov_b32 %[st], 1966080\n\t" \
  "s_cbranch_vccz 25f\n\t" \
  "buffer_load_dwordx4 v[124:127], %[va], %[rs], %[st] offen sc1\n\t" \
  "25:\n\t" \
  "s_waitcnt vmcnt(0)\n\t" \
  "v_max3_u32 %[t], v112, v113, v114\n\t" \
  "v_max3_u32 %[u], v115, v116, v117\n\t" \
  "v_max3_u32 %[t], %[t], v118, v119\n\t" \
  "v_max3_u32 %[u], %[u], v120, v121\n\t" \
  "v_max3_u32 %[t], %[t], v122, v123\n\t" \
  "v_max3_u32 %[u], %[u], v124, v125\n\t" \
  "v_max3_u32 %[t], %[t], v126, v127\n\t" \
  "v_max_u32 %[t], %[t], %[u]\n\t" \
  "v_cmp_eq_u32 vcc, -1, %[t]\n\t" \
  "s_cbranch_vccz 8f\n\t" \
  "s_sleep %[osl]\n\t" \
  "s_sub_u32 %[ct], %[ct], 1\n\t" \
  "s_cmp_lg_u32 %[ct], 0\n\t" \
  "s_cbranch_scc1 1b\n\t" \
  "8:\n\t" \
  "s_setprio %[phi]\n\t" \
  "9:\n\t"
DEV void pin4(u32x4& v) { asm volatile("" : "+v"(v)); }
#define OUT_AUX 0
#define K2_WD 3
#define O_PRIO 0
#define O_SLEEP 2
#define BPRIO(n) do { SCHED_FENCE(); __builtin_amdgcn_s_setprio(n); SCHED_FENCE(); } while (0)
DEV void b_compute(char* lds, int wid, int lane, int b, int grp, int j, int c0, int ntile, const u16* x, const u16* w2, const u16* w3,
                   const u16* w4, const u16* wo, char* ws, u16* out) {
#if defined(__HIP_DEVICE_COMPILE__)
  using namespace bsched;
  STAMP_DECL
  RSTAMP(0); STAMP(1);
  const int wm = wid & 1, kp = wid >> 1;
  const int m0 = grp * 64;
  auto hrs = mk_rsrc(ws + OFF_HB, 64 * HB_ROWS * 16);
  auto x1rs = mk_rsrc(ws + OFF_X1, 64 * S_ * 16);
  auto h2rs = mk_rsrc(ws + OFF_H2, 172 * S_ * 16);
  auto xrs = mk_rsrc(x, S_ * D_ * 2);
  const unsigned col0 = j * 64 + kp * 32;
  unsigned l31, h, blk, q, p, row, sw, cpart, wlo[2], whi[2];
  auto lane_init = [&]() {
    lane = fresh_lane();
    l31 = lane & 31; h = lane >> 5; blk = (lane >> 4) & 1; q = (lane & 15) >> 2; p = lane & 3;
    row = m0 + wm * 32 + l31; sw = (q >> 1) & 1; cpart = 8 * (p & 1);
#pragma unroll
    for (int a = 0; a < 2; ++a) {
      wlo[a] = (unsigned)(size_t)lds + (16 * kp + 8 * h + q) * 128 + (((4 * (a ^ kp) + 2 * blk + (p >> 1)) ^ (sw << 2)) << 4) + cpart;
      whi[a] = wlo[a] + 65536;
      asm volatile("" : "+v"(wlo[a]), "+v"(whi[a]));
    }
  };
  auto wfrag = [&](int a, int off) {
    typedef __attribute__((address_space(3))) char* lp_t;
    typedef __attribute__((address_space(3))) s16x4* lv_t;
    lp_t base = off < 65536 - 1024 ? (lp_t)(size_t)wlo[a] + off : (lp_t)(size_t)whi[a] + (off - 65536);
    s16x4 u = __builtin_amdgcn_ds_read_tr16_b64_v4i16((lv_t)base), v = __builtin_amdgcn_ds_read_tr16_b64_v4i16((lv_t)(base + 512));
    typedef __attribute__((ext_vector_type(8))) short s16x8;
    return __builtin_bit_cast(bf16x8, __builtin_shufflevector(u, v, 0, 1, 2, 3, 4, 5, 6, 7));
  };
  b_prologue(lds, wid, b, grp, w2, w3, w4, wo, ws, out, j, c0, ntile);
  PROBE_AT(5);
  STAMP(58); RSTAMP(59);
  lane_init();
  u32x2 xv[4];
#pragma unroll
  for (int i = 0; i < 4; ++i) xv[i] = __builtin_amdgcn_raw_buffer_load_b64(xrs, (row * D_ + col0 + 4 * h) * 2, i * 16, 0);
  const f32x2 st0 = __builtin_bit_cast(f32x2, __builtin_amdgcn_raw_buffer_load_b64(mk_rsrc(ws + OFF_ST0, S_ * 8), row * 8, 0, 16));

  auto phase13 = [&](auto G0_, auto NI_, auto QD_, auto loadA, f32x16 (&acc)[2], auto&& first_issue_done) {
    constexpr int G0 = G0_, NI = NI_, QD = QD_;
    u32x4 aq[QD];
    static_for<QD>([&](auto I) { if constexpr (I < NI) aq[I] = loadA(I); });
    first_issue_done();
    bf16x8 wf[2][2];
    auto rd = [&](auto I) {
      constexpr int i = I;
      constexpr int off = pos(G0 + (i >> 1)) * 1024 + (i & 1) * 4096;
      wf[i & 1][0] = wfrag(0, off);
      wf[i & 1][1] = wfrag(1, off);
    };
    auto mm = [&](auto I) {
      constexpr int i = I;
      bf16x8 a = __builtin_bit_cast(bf16x8, aq[i % QD]);
      acc[0] = mfma32(wf[i & 1][0], a, acc[0]);
      acc[1] = mfma32(wf[i & 1][1], a, acc[1]);
      if constexpr (i + QD < NI) aq[i % QD] = loadA(IC<i + QD>{});
    };
    static_for<NI>([&](auto I) {
      constexpr int i = I;
      if constexpr ((i & 1) == 0) { BARRIER_NOWAIT(); STAMP(2 + (G0 + (i >> 1))); }
      rd(I);
      if constexpr (i > 0) mm(IC<i - 1>{});
      SCHED_FENCE();
    });
    mm(IC<NI - 1>{});
  };
  auto exchange = [&](f32x16 (&acc)[2], int xpos) {
    char* xb = lds + xpos * 1024 + fresh_lane() * 16;
    char* mine = xb + (wm * 2 + (kp ^ 1)) * 4096;
    const char* theirs = xb + (wm * 2 + kp) * 4096;
#pragma unroll
    for (int i = 0; i < 4; ++i) *(f32x4*)(mine + i * 1024) = f32x4{acc[1][4 * i], acc[1][4 * i + 1], acc[1][4 * i + 2], acc[1][4 * i + 3]};
    RAW_BARRIER();
#pragma unroll
    for (int i = 0; i < 4; ++i) {
      f32x4 t = *(const f32x4*)(theirs + i * 1024);
#pragma unroll
      for (int c = 0; c < 4; ++c) acc[0][4 * i + c] += t[c];
    }
  };

  {
    f32x16 acc[2] = {};
    const unsigned va = (h * HB_ROWS + row) * 16 + kp * (2 * HB_ROWS * 16);
    auto loadA = [&](auto I) {
      constexpr int i = I;
      constexpr int kk0 = 4 * (i >> 1) + 2 * (i & 1), g = i >> 2;
      constexpr int d = g == 0 ? -1 : g == 1 ? 1 : g == 2 ? -2 : 2;
      return ld_sc1(hrs, va, kk0 * (2 * HB_ROWS * 16) + (HB_OFF + d) * 16);
    };
    BPRIO(K2_PM1);
    phase13(IC<0>{}, IC<16>{}, IC<10>{}, loadA, acc, [] {});
    BPRIO(K2_PE1);
    exchange(acc, 64);
    STAMP(50);
    float v[16], a1 = 0.f, a2 = 0.f;
#pragma unroll
    for (int i = 0; i < 4; ++i) {
      v[4 * i + 0] = (bf_lo(xv[i][0]) - st0[0]) * st0[1] + acc[0][4 * i + 0];
      v[4 * i + 1] = (bf_hi(xv[i][0]) - st0[0]) * st0[1] + acc[0][4 * i + 1];
      v[4 * i + 2] = (bf_lo(xv[i][1]) - st0[0]) * st0[1] + acc[0][4 * i + 2];
      v[4 * i + 3] = (bf_hi(xv[i][1]) - st0[0]) * st0[1] + acc[0][4 * i + 3];
    }
#pragma unroll
    for (int i = 0; i < 16; ++i) { a1 += v[i]; a2 += v[i] * v[i]; }
    a1 = xhalf_sum(a1); a2 = xhalf_sum(a2);
#pragma unroll
    for (int g = 0; g < 2; ++g) st_sc1(x1rs, pack8(v + 8 * g), ((col0 / 8 + 2 * g + h) * S_ + row) * 16, 0);
    if (lane < 32) {
      auto srs = mk_rsrc(ws + OFF_ST1, 16 * S_ * 8);
      u32x2 sv = {__float_as_uint(a1), __float_as_uint(a2)};
      __builtin_amdgcn_raw_buffer_store_b64(sv, srs, ((col0 >> 5) * S_ + row) * 8, 0, 16);
    }
    WAIT_V(0);
    BARRIER_NOWAIT();
    STAMP(51);
  }
  {
    const int wn = kp;
    lane_init();
    float cs[3];
    {
      auto crs = mk_rsrc(ws + OFF_CS3, CS3_P * CS3_LD * 4);
      const unsigned cp = (h * 4 * CS3_LD + c0 + wn * 96 + l31) * 4;
#pragma unroll
      for (int a = 0; a < 3; ++a) {
        float t = 0.f;
#pragma unroll
        for (int i = 0; i < 4; ++i) t += __uint_as_float(__builtin_amdgcn_raw_buffer_load_b32(crs, cp, (i * CS3_LD + a * 32) * 4, 16));
        cs[a] = xhalf_sum(t);
      }
    }
    unsigned wo2[3];
#pragma unroll
    for (int a = 0; a < 3; ++a) wo2[a] = (8 * h + q) * 384 + (((4 * (3 * wn + a) + 2 * blk + (p >> 1)) ^ (sw << 2)) << 4) + cpart;
    f32x16 acc[3] = {};
    constexpr int QD = 8;
    u32x4 aq[QD];
    const unsigned va = (h * S_ + row) * 16;
    BARRIER_NOWAIT();
    STAMP(52);
    u32x2 sp[8];
    {
      auto srs = mk_rsrc(ws + OFF_ST1, 16 * S_ * 8);
#pragma unroll
      for (int i = 0; i < 8; ++i) sp[i] = __builtin_amdgcn_raw_buffer_load_b64(srs, ((h * 8 + i) * S_ + row) * 8, 0, 16);
    }
    static_for<QD>([&](auto I) { aq[I] = ld_sc1(x1rs, va, I * 65536); });
    bf16x8 wf[2][3];
    auto rd = [&](auto I) {
      constexpr int i = I;
      const char* sb = lds + pos(8 + (i >> 1)) * 1024 + (i & 1) * (16 * 384);
#pragma unroll
      for (int a = 0; a < 3; ++a) wf[i & 1][a] = tr_frag(sb + wo2[a], sb + wo2[a] + 4 * 384);
    };
    auto mm = [&](auto I) {
      constexpr int i = I;
      bf16x8 a = __builtin_bit_cast(bf16x8, aq[i % QD]);
#pragma unroll
      for (int t = 0; t < 3; ++t) acc[t] = mfma32(wf[i & 1][t], a, acc[t]);
      if constexpr (i + QD < 32) aq[i % QD] = ld_sc1(x1rs, va, (i + QD) * 65536);
    };
    float mu, rs;
    {
      float a1 = 0.f, a2 = 0.f;
#pragma unroll
      for (int t = 0; t < 8; ++t) { a1 += __uint_as_float(sp[t][0]); a2 += __uint_as_float(sp[t][1]); }
      a1 = xhalf_sum(a1); a2 = xhalf_sum(a2);
      mu = a1 * (1.0f / D_);
      rs = __builtin_amdgcn_rsqf(fmaxf(a2 * (1.0f / D_) - mu * mu, 0.f) + LN_EPS);
      SCHED_FENCE();
    }
    BPRIO(K2_PM2);
    static_for<32>([&](auto I) {
      constexpr int i = I;
      if constexpr ((i & 1) == 0 && i > 0) { BARRIER_NOWAIT(); STAMP(2 + 8 + (i >> 1)); }
      rd(I);
      if constexpr (i > 0) mm(IC<i - 1>{});
      SCHED_FENCE();
    });
    mm(IC<31>{});
    BPRIO(K2_PE2);
    {
      unsigned mh = pk_bf16(-mu, -mu);
      float mlo = -mu - bf_lo(mh);
      u32x4 xe = {h ? 0u : mh, h ? 0u : (pk_bf16(mlo, 0.f)), 0u, 0u};
#pragma unroll
      for (int t = 0; t < 3; ++t) {
        unsigned ch = pk_bf16(cs[t], cs[t]);
        float clo = cs[t] - bf_lo(ch);
        u32x4 we = {h ? 0u : ((ch & 0xffffu) | (pk_bf16(clo, clo) << 16)), h ? 0u : (ch & 0xffffu), 0u, 0u};
        acc[t] = mfma32(__builtin_bit_cast(bf16x8, we), __builtin_bit_cast(bf16x8, xe), acc[t]);
      }
    }
    STAMP(53);
    const f32x2 gk1 = splat2(GELU_C1 * rs), gk2 = splat2(GELU_C2 * rs * rs * rs), gir = splat2(__builtin_amdgcn_rcpf(rs));
#pragma unroll
    for (int t = 0; t < 3; ++t) {
      if (3 * wn + t < ntile) {
        float v[16];
#pragma unroll
        for (int i = 0; i < 8; ++i) {
          const f32x2 a = {acc[t][2 * i], acc[t][2 * i + 1]};
          const f32x2 tt = a * fma2(a * a, gk2, gk1);
          const f32x2 e = {__builtin_amdgcn_exp2f(tt[0]), __builtin_amdgcn_exp2f(tt[1])};
          const f32x2 d = fma2(e, gir, gir);
          const f32x2 o = a * f32x2{__builtin_amdgcn_rcpf(d[0]), __builtin_amdgcn_rcpf(d[1])};
          v[2 * i] = o[0]; v[2 * i + 1] = o[1];
        }
        const unsigned kt8 = (c0 + (3 * wn + t) * 32) >> 3;
#pragma unroll
        for (int g = 0; g < 2; ++g) st_sc1(h2rs, pack8(v + 8 * g), ((kt8 + 2 * g + h) * S_ + row) * 16, 0);
      }
    }
    WAIT_V(0);
    BARRIER_NOWAIT();
    STAMP(54);
  }
  {
    lane_init();
    auto ors = mk_rsrc_u(ws + OFF_O, 64 * S_ * 16);
    const unsigned va = (h * S_ + row) * 16 + kp * 65536;
    u32x2 xr[4], x1r[4];
#pragma unroll
    for (int t = 0; t < 4; ++t) {
      xr[t] = __builtin_amdgcn_raw_buffer_load_b64(xrs, (row * D_ + col0 + 4 * h) * 2, t * 16, 0);
      x1r[t] = __builtin_amdgcn_raw_buffer_load_b64(x1rs, ((col0 / 8 + t) * S_ + row) * 16 + 8 * h, 0, 16);
    }
    BARRIER_NOWAIT();
    STAMP(55);
    constexpr int NW = 43, QD = 10, NS = 16;
    u32x4 aq[NS];
    auto loadH = [&](auto I) { constexpr int i = I; return ld_sc1(h2rs, va, (4 * (i >> 1) + 2 * (i & 1)) * 65536); };
    static_for<QD>([&](auto I) { aq[I] = loadH(I); });
    f32x16 acc[2] = {};
#pragma unroll
    for (int i = 0; i < 4; ++i) {
      acc[0][4 * i + 0] = bf_lo(xr[i][0]) + bf_lo(x1r[i][0]);
      acc[0][4 * i + 1] = bf_hi(xr[i][0]) + bf_hi(x1r[i][0]);
      acc[0][4 * i + 2] = bf_lo(xr[i][1]) + bf_lo(x1r[i][1]);
      acc[0][4 * i + 3] = bf_hi(xr[i][1]) + bf_hi(x1r[i][1]);
    }
    BPRIO(K2_PM3);
    u32x16 og[4];
    {
      bf16x8 wf[2][2];
      auto rd = [&](auto I) {
        constexpr int i = I;
        constexpr int off = pos(24 + (i >> 1)) * 1024 + (i & 1) * 4096;
        wf[i & 1][0] = wfrag(0, off);
        wf[i & 1][1] = wfrag(1, off);
      };
      auto mm = [&](auto I) {
        constexpr int i = I;
        bf16x8 a = __builtin_bit_cast(bf16x8, aq[i % QD]);
        acc[0] = mfma32(wf[i & 1][0], a, acc[0]);
        acc[1] = mfma32(wf[i & 1][1], a, acc[1]);
        asm volatile("" : "+v"(acc[0]), "+v"(acc[1]));
        if constexpr (i + QD < NW) aq[i % QD] = loadH(IC<i + QD>{});
      };
      static_for<NW>([&](auto I) {
        constexpr int i = I;
        if constexpr ((i & 1) == 0 && i > 0) { BARRIER_NOWAIT(); STAMP(2 + 24 + (i >> 1)); }
        if constexpr (i == 2 * (K2_XBAR - 27)) {
          unsigned st;
          static_for<NW - i + 1>([&](auto K) { pin4(aq[(i - 1 + K) % QD]); });
          asm volatile(OLOAD_ASM
                       : "=&{v[64:79]}"(og[0]), "=&{v[80:95]}"(og[1]), "=&{v[96:111]}"(og[2]), "=&{v[112:127]}"(og[3]), [st] "=&s"(st)
                       : [va] "v"(va), [rs] "s"(ors) : "memory");
          SCHED_FENCE();
        }
        rd(I);
        if constexpr (i > 0) mm(IC<i - 1>{});
        SCHED_FENCE();
      });
      mm(IC<NW - 1>{});
    }
    STAMP(48);
    BPRIO(K2_PE3);
    BARRIER_NOWAIT();
    STAMP(49);
    {
      constexpr int WD = K2_WD;
      bf16x8 wf[WD][2];
      auto rd = [&](auto N) {
        constexpr int n = N;
        constexpr int off = pos(46 + (n >> 1)) * 1024 + (n & 1) * 4096;
        wf[n % WD][0] = wfrag(0, off);
        wf[n % WD][1] = wfrag(1, off);
      };
      static_for<K2_WPRE>([&](auto N) { rd(N); });
      SCHED_FENCE();
      static_for<WD - 1>([&](auto N) { if constexpr (N >= K2_WPRE) rd(N); });
      auto ocheck = [&](auto G) {
        constexpr int g = G;
        unsigned t, u, st, ct;
        u32x16 o0 = og[0], o1 = og[1], o2 = og[2], o3 = og[3];
#define OCK_TAIL [t] "=&v"(t), [u] "=&v"(u), [st] "=&s"(st), [ct] "=&s"(ct) : [va] "v"(va), [rs] "s"(ors), [pmax] "n"(POLL_MAX), [osl] "n"(O_SLEEP), [plo] "n"(O_PRIO), [phi] "n"(K2_PE3) : "vcc", "scc", "memory"
        if constexpr (g == 0) asm volatile(OROWS_ASM0 : "+{v[64:79]}"(o0), "+{v[80:95]}"(o1), "+{v[96:111]}"(o2), "+{v[112:127]}"(o3), OCK_TAIL);
        if constexpr (g == 1) asm volatile(OROWS_ASM1 : "+{v[80:95]}"(o1), "+{v[96:111]}"(o2), "+{v[112:127]}"(o3), OCK_TAIL);
        if constexpr (g == 2) asm volatile(OROWS_ASM2 : "+{v[96:111]}"(o2), "+{v[112:127]}"(o3), OCK_TAIL);
        if constexpr (g == 3) asm volatile(OROWS_ASM3 : "+{v[112:127]}"(o3), OCK_TAIL);
        og[0] = o0; og[1] = o1; og[2] = o2; og[3] = o3;
        SCHED_FENCE();
      };
      static_for<16>([&](auto N) {
        constexpr int n = N, k = 4 * (n & 3);
        if constexpr ((n & 3) == 0) { ocheck(IC<(n >> 2)>{}); if constexpr (n == 0) STAMP(57); }
        if constexpr (n + WD - 1 == 12) BARRIER_NOWAIT();
        if constexpr (n + WD - 1 < 16) rd(IC<n + WD - 1>{});
        const u32x16 ot = og[n >> 2];
        bf16x8 a = __builtin_bit_cast(bf16x8, __builtin_shufflevector(ot, ot, k, k + 1, k + 2, k + 3));
        acc[0] = mfma32(wf[n % WD][0], a, acc[0]);
        acc[1] = mfma32(wf[n % WD][1], a, acc[1]);
        asm volatile("" : "+v"(acc[0]), "+v"(acc[1]));
        SCHED_FENCE();
      });
    }
    {
      char* xb = lds + (wm ? 72 : 48) * 1024 + fresh_lane() * 16;
      char* mine = xb + (kp ^ 1) * 4096;
      const char* theirs = xb + kp * 4096;
#pragma unroll
      for (int i = 0; i < 4; ++i) *(f32x4*)(mine + i * 1024) = f32x4{acc[1][4 * i], acc[1][4 * i + 1], acc[1][4 * i + 2], acc[1][4 * i + 3]};
      RAW_BARRIER();
#pragma unroll
      for (int i = 0; i < 4; ++i) {
        f32x4 t = *(const f32x4*)(theirs + i * 1024);
#pragma unroll
        for (int c = 0; c < 4; ++c) acc[0][4 * i + c] += t[c];
      }
    }
    STAMP(56);
    float v[16];
#pragma unroll
    for (int i = 0; i < 16; ++i) v[i] = acc[0][i];
    auto outrs = mk_rsrc(out, S_ * D_ * 2);
    const unsigned l2 = fresh_lane();
    const unsigned vo = ((m0 + wm * 32 + (l2 & 31)) * D_ + col0 + (l2 >> 5) * 8) * 2;
#pragma unroll
    for (int g = 0; g < 2; ++g) __builtin_amdgcn_raw_buffer_store_b128(pack8(v + 8 * g), outrs, vo, g * 32, OUT_AUX);
  }
  PROBE_AT(4);
  STAMP(60); RSTAMP(61); HWSTAMP(); STAMP_FLUSH(0);
#endif
}

constexpr int K2_LDS = 81920;
#define KA_MANUAL 1
extern "C" __global__ __launch_bounds__(512, 4) void km(const u16* __restrict__ x_, const u16* __restrict__ wqkv_, const u16* __restrict__ w1_,
                                                        const u16* __restrict__ w2_, const u16* __restrict__ w3_, const u16* __restrict__ w4_,
                                                        const u16* __restrict__ wo_, char* __restrict__ ws_, u16* __restrict__ out_) {
#if defined(__HIP_DEVICE_COMPILE__)
  extern __shared__ __attribute__((aligned(16))) char lds[];
#define K2_BPRIO 2
  const int b = blockIdx.x;
  const int wid = __builtin_amdgcn_readfirstlane(threadIdx.x >> 6);
  PROBE_T0();
  u32x16 ka; u32x2 kb;
  asm volatile("s_load_dwordx16 %0, %2, 0x0\n\ts_load_dwordx2 %1, %2, 0x40" : "=&s"(ka), "=&s"(kb) : "s"(__builtin_amdgcn_kernarg_segment_ptr()));
  unsigned cpf = 0;
  if (b < CODEPF0) {
    unsigned lo, hi;
    asm volatile("s_getpc_b64 s[20:21]\n\ts_add_u32 s20, s20, km@rel32@lo+4\n\ts_addc_u32 s21, s21, km@rel32@hi+12\n\ts_mov_b32 %0, s20\n\ts_mov_b32 %1, s21" : "=s"(lo), "=s"(hi) :: "s20", "s21", "scc");
    cpf = *(const __attribute__((address_space(1))) unsigned*)((((unsigned long long)hi << 32) | lo) + min(threadIdx.x * 128u, (unsigned)(CODEPF_BYTES - 4)));
  }
  asm volatile("s_waitcnt lgkmcnt(0)" : "+s"(ka), "+s"(kb));
  typedef __attribute__((address_space(1))) char* gp_t;
  auto arg = [&](int i) { return (char*)(gp_t)(((unsigned long long)ka[2 * i + 1] << 32) | ka[2 * i]); };
  const u16 *x = (const u16*)arg(0), *wqkv = (const u16*)arg(1), *w1 = (const u16*)arg(2), *w2 = (const u16*)arg(3), *w3 = (const u16*)arg(4),
            *w4 = (const u16*)arg(5), *wo = (const u16*)arg(6);
  char* ws = arg(7);
  u16* out = (u16*)(char*)(gp_t)(((unsigned long long)kb[1] << 32) | kb[0]);
  PROBE_AT(9);
  if (__builtin_expect(b < 256, 1)) {
    g_phase(lds, b, wid, x, wqkv, w1, ws, cpf);
    RAW_BARRIER();
    __builtin_amdgcn_s_setprio(K2_APRIO0);
    attn_role(lds, b, ws);
    PROBE_END();
    return;
  }
  const int lane = threadIdx.x & 63;
  const int b2 = b - 256;
  const int grp = (b2 & 7) * 4 + (b2 >> 6), j = (b2 >> 3) & 7;
  const int c0 = j < 3 ? j * 192 : 576 + (j - 3) * 160;
  const int ntile = j < 3 ? 6 : 5;
  __builtin_amdgcn_s_setprio(K2_BPRIO);
  if (wid >= 4) b_loader(lds, wid - 4, lane, grp, j, c0, w2, w3, w4, wo, ws);
  else b_compute(lds, wid, lane, b, grp, j, c0, ntile, x, w2, w3, w4, wo, ws, out);
  PROBE_END();
#endif
}

#define SEL(i) true
extern "C" void kernel_launch(void* const* d_in, const int* in_sizes, int n_in, void* d_out, int out_size,
                              void* d_ws, size_t ws_size, hipStream_t stream) {
  const u16* x = (const u16*)d_in[0];
  const u16* wqkv = (const u16*)d_in[1];
  const u16* wo = (const u16*)d_in[2];
  const u16* w1 = (const u16*)d_in[4];
  const u16* w2 = (const u16*)d_in[5];
  const u16* w3 = (const u16*)d_in[6];
  const u16* w4 = (const u16*)d_in[7];
  char* ws = (char*)d_ws;
  (void)hipFuncSetAttribute((const void*)km, hipFuncAttributeMaxDynamicSharedMemorySize, K2_LDS);
  km<<<512, 512, K2_LDS, stream>>>(x, wqkv, w1, w2, w3, w4, wo, ws, (u16*)d_out);
}
```
